```python
import torch

def _softmax(x, axis=-1):
    return torch.softmax(x, dim=axis)

def _gelu(x):
    return torch.nn.functional.gelu(x, approximate='tanh')

def _silu(x):
    return torch.nn.functional.silu(x)

def _sigmoid(x):
    return torch.sigmoid(x)

def _layernorm(x, axis=-1, eps=1e-05):
    m = x.mean(dim=axis, keepdim=True)
    v = x.var(dim=axis, keepdim=True, unbiased=False)
    return (x - m) / torch.sqrt(v + eps)

def _scatter_add(tgt, idx, src, axis=0):
    return tgt.clone().index_add_(axis, idx, src)

def _segment_sum(src, seg, n):
    out = torch.zeros((n,) + src.shape[1:], dtype=src.dtype)
    return out.index_add_(0, seg, src)

def _pad(x, pw):
    flat = tuple((v for pair in reversed(pw) for v in pair))
    return torch.nn.functional.pad(x, flat)
kernel_name = 'interleave_layers__memory_aug_recurrent_mem_tr__swiglu_ffn__prompt'

def setup_inputs():

    _w = lambda a: (lambda t: t.to(torch.bfloat16) if t.is_floating_point() else t)(torch.as_tensor(a))

    def _A_setup():
        B, S, Dm = (1, 2048, 448)
        H, Dh = (16, 256)
        M, seg = (8, 32)
        seg = min(seg, S)
        S = S // seg * seg
        return {'x': _w((lambda *s: torch.randn(*s, dtype=torch.bfloat16) * 0.05)(B, S, Dm)), 'mem0': _w((lambda *s: torch.randn(*s, dtype=torch.bfloat16) * 0.8)(B, M, Dm)), 'Wq': _w((lambda *s: torch.randn(*s, dtype=torch.bfloat16) * 1.6)(Dm, H, Dh) * Dm ** (-0.5)), 'Wk': _w((lambda *s: torch.randn(*s, dtype=torch.bfloat16) * 1.6)(Dm, H, Dh) * Dm ** (-0.5)), 'Wv': _w((lambda *s: torch.randn(*s, dtype=torch.bfloat16) * 0.8)(Dm, H, Dh) * Dm ** (-0.5)), 'Wo': _w((lambda *s: torch.randn(*s, dtype=torch.bfloat16) * 0.8)(H, Dh, Dm) * (H * Dh) ** (-0.5)), 'seg_len': seg}
    _A_in = _A_setup()

    def _B_setup():
        B, S, D, F = (1, 2048, 448, 1792)
        return {'x': _w((lambda *s: torch.randn(*s, dtype=torch.bfloat16) * 0.05)(B, S, D)), 'w_gate': _w((lambda *s: torch.randn(*s, dtype=torch.bfloat16) * 0.05)(D, F)), 'w_up': _w((lambda *s: torch.randn(*s, dtype=torch.bfloat16) * 0.05)(D, F)), 'w_down': _w((lambda *s: torch.randn(*s, dtype=torch.bfloat16) * 0.05)(F, D))}
    _B_in = _B_setup()
    return {'A_x': _A_in['x'], 'A_mem0': _A_in['mem0'], 'A_Wq': _A_in['Wq'], 'A_Wk': _A_in['Wk'], 'A_Wv': _A_in['Wv'], 'A_Wo': _A_in['Wo'], 'A_seg_len': _A_in['seg_len'], 'B_x': _B_in['x'], 'B_w_gate': _B_in['w_gate'], 'B_w_up': _B_in['w_up'], 'B_w_down': _B_in['w_down']}

def reference(A_x, A_mem0, A_Wq, A_Wk, A_Wv, A_Wo, A_seg_len, B_x, B_w_gate, B_w_up, B_w_down):

    def _A_ref(x, mem0, Wq, Wk, Wv, Wo, seg_len):
        B, S, Dm = x.shape
        H, Dh = (Wq.shape[1], Wq.shape[2])
        M = mem0.shape[1]
        scale = Dh ** (-0.5)
        n_seg = S // seg_len
        L = M + seg_len
        idx = torch.arange(L)
        mask = (idx[:, None] < M) | (idx[:, None] >= idx[None, :])
        mem = mem0
        outs = []
        for s in range(n_seg):
            xs = x[:, s * seg_len:(s + 1) * seg_len, :].clone()
            h = torch.concatenate([mem, xs], axis=1)
            q = torch.einsum('btd,dhe->bhte', h, Wq)
            k = torch.einsum('btd,dhe->bhte', h, Wk)
            v = torch.einsum('btd,dhe->bhte', h, Wv)
            scores = torch.einsum('bhqe,bhke->bhqk', q, k) * scale
            scores = torch.where(mask[None, None], scores, -1000000000.0)
            p = _softmax(scores, axis=-1)
            o = torch.einsum('bhqk,bhke->bhqe', p, v)
            o = torch.einsum('bhte,hed->btd', o, Wo)
            mem = o[:, :M, :].clone()
            outs.append(o[:, M:, :])
        return torch.concatenate(outs, axis=1)

    def _B_ref(x, w_gate, w_up, w_down):
        g = _silu(x @ w_gate)
        u = x @ w_up
        return g * u @ w_down
    x = A_x
    x = x + _A_ref(_layernorm(x, axis=-1), A_mem0, A_Wq, A_Wk, A_Wv, A_Wo, A_seg_len)
    x = x + _B_ref(_layernorm(x, axis=-1), B_w_gate, B_w_up, B_w_down)
    x = x + _A_ref(_layernorm(x, axis=-1), A_mem0, A_Wq, A_Wk, A_Wv, A_Wo, A_seg_len)
    x = x + _B_ref(_layernorm(x, axis=-1), B_w_gate, B_w_up, B_w_down)
    return x
```

```cpp
#include <hip/hip_runtime.h>
#include <hip/hip_bf16.h>

typedef __bf16 bf16x8 __attribute__((ext_vector_type(8)));
typedef __bf16 bf16x16 __attribute__((ext_vector_type(16)));
typedef __bf16 bf16x2 __attribute__((ext_vector_type(2)));
typedef float f32x2 __attribute__((ext_vector_type(2)));
typedef float f32x4 __attribute__((ext_vector_type(4)));
typedef unsigned u32x2 __attribute__((ext_vector_type(2)));
typedef unsigned u32x4 __attribute__((ext_vector_type(4)));
typedef unsigned u32x8 __attribute__((ext_vector_type(8)));
typedef short s16x4 __attribute__((ext_vector_type(4)));
#define LAS __attribute__((address_space(3)))
#define DI __device__ __forceinline__
typedef __amdgpu_buffer_rsrc_t rsrc_t;

constexpr int D = 448, NH = 16, FF = 1792, NSEG = 64;
constexpr unsigned MB = 1u << 20;
constexpr unsigned O_HOP0 = 0;
constexpr unsigned O_HOP1 = 2 * MB;
constexpr unsigned O_FLAG = 4 * MB;
constexpr unsigned O_CTL = O_FLAG + 32768;
constexpr unsigned O_STAT = O_FLAG + 65536;
constexpr unsigned O_WS = 5 * MB;
constexpr unsigned O_WV = 12 * MB;
constexpr unsigned XSZ = 2 * MB;
constexpr unsigned O_XN = 19 * MB;
constexpr unsigned O_XR = 23 * MB;
constexpr unsigned O_X1 = 27 * MB;
constexpr unsigned O_PT = 31 * MB;
constexpr unsigned PTSZ = 3 * MB;
constexpr unsigned O_HID = 37 * MB;
constexpr unsigned HIDSZ = 8 * MB;
constexpr unsigned O_DBG = 53 * MB;
constexpr unsigned WS_USED = 60 * MB;
constexpr unsigned SEGB = 28672;
constexpr unsigned POISON = 0xFFFFFFFFu;
constexpr unsigned SPIN_CAP = 1u << 19;

struct Params {
    const unsigned short *x, *mem0, *wq, *wk, *wv, *wo, *wg, *wu, *wd;
    unsigned short* out;
    char* ws;
};

DI f32x4 mfma(u32x4 a, u32x4 b, f32x4 c) {
    return __builtin_amdgcn_mfma_f32_16x16x32_bf16(__builtin_bit_cast(bf16x8, a), __builtin_bit_cast(bf16x8, b), c, 0, 0, 0);
}
DI f32x4 smfma(u32x4 a, u32x8 b, f32x4 c, int idx) {
    return __builtin_amdgcn_smfmac_f32_16x16x64_bf16(__builtin_bit_cast(bf16x8, a), __builtin_bit_cast(bf16x16, b), c, idx, 0, 0);
}
DI unsigned pk(float a, float b) { f32x2 v = {a, b}; return __builtin_bit_cast(unsigned, __builtin_convertvector(v, bf16x2)); }
DI float asf(unsigned u) { return __builtin_bit_cast(float, u); }
DI unsigned asu(float f) { return __builtin_bit_cast(unsigned, f); }
DI float blo(unsigned u) { return __builtin_bit_cast(float, u << 16); }
DI float bhi(unsigned u) { return __builtin_bit_cast(float, u & 0xFFFF0000u); }
DI u32x4 lo4(u32x8 v) { return __builtin_shufflevector(v, v, 0, 1, 2, 3); }
DI u32x4 hi4(u32x8 v) { return __builtin_shufflevector(v, v, 4, 5, 6, 7); }
DI u32x8 cat8(u32x4 a, u32x4 b) { return __builtin_shufflevector(a, b, 0, 1, 2, 3, 4, 5, 6, 7); }
DI void bar() { asm volatile("s_waitcnt lgkmcnt(0)\n\ts_barrier" ::: "memory"); }
DI void drain() { asm volatile("s_waitcnt vmcnt(0)" ::: "memory"); }
template <int C> DI float dpp(float v) { return __builtin_bit_cast(float, __builtin_amdgcn_update_dpp(0, __builtin_bit_cast(int, v), C, 0xF, 0xF, true)); }
DI float ex2(float v) { return __builtin_amdgcn_exp2f(v); }
DI float rcp(float v) { return __builtin_amdgcn_rcpf(v); }
DI float x16add(float v) { auto r = __builtin_amdgcn_permlane16_swap(asu(v), asu(v), false, false); return asf(r[0]) + asf(r[1]); }
DI float x16max(float v) { auto r = __builtin_amdgcn_permlane16_swap(asu(v), asu(v), false, false); return fmaxf(asf(r[0]), asf(r[1])); }
DI float x32add(float v) { auto r = __builtin_amdgcn_permlane32_swap(asu(v), asu(v), false, false); return asf(r[0]) + asf(r[1]); }
DI float x32max(float v) { auto r = __builtin_amdgcn_permlane32_swap(asu(v), asu(v), false, false); return fmaxf(asf(r[0]), asf(r[1])); }
DI float halfsum(float a, float b) { auto r = __builtin_amdgcn_permlane32_swap(asu(a), asu(b), false, false); return asf(r[0]) + asf(r[1]); }
DI float row16add(float v) { v += dpp<0x128>(v); v += dpp<0x124>(v); v += dpp<0x122>(v); v += dpp<0x121>(v); return v; }
DI float row16max(float v) { v = fmaxf(v, dpp<0x128>(v)); v = fmaxf(v, dpp<0x124>(v)); v = fmaxf(v, dpp<0x122>(v)); v = fmaxf(v, dpp<0x121>(v)); return v; }
DI float oct8add(float v) { v += dpp<0xB1>(v); v += dpp<0x4E>(v); v += dpp<0x141>(v); return v; }
DI float oct8max(float v) { v = fmaxf(v, dpp<0xB1>(v)); v = fmaxf(v, dpp<0x4E>(v)); v = fmaxf(v, dpp<0x141>(v)); return v; }
DI unsigned nopoison(unsigned v) { return v < 0xFFFFFFFEu ? v : 0xFFFFFFFEu; }
DI unsigned umax4(u32x4 v) { unsigned a = v[0] > v[1] ? v[0] : v[1], b = v[2] > v[3] ? v[2] : v[3]; return a > b ? a : b; }

DI u32x4 ld16(rsrc_t r, unsigned v, unsigned s) { return __builtin_amdgcn_raw_buffer_load_b128(r, v, s, 16); }
DI u32x2 ld8(rsrc_t r, unsigned v, unsigned s) { return __builtin_amdgcn_raw_buffer_load_b64(r, v, s, 16); }
DI unsigned ld4(rsrc_t r, unsigned v, unsigned s) { return __builtin_amdgcn_raw_buffer_load_b32(r, v, s, 16); }
DI u32x4 ld16p(rsrc_t r, unsigned v, unsigned s) { return __builtin_amdgcn_raw_buffer_load_b128(r, v, s, 0); }
DI void st16(rsrc_t r, unsigned v, unsigned s, u32x4 d) { __builtin_amdgcn_raw_buffer_store_b128(d, r, v, s, 16); }
DI void st8(rsrc_t r, unsigned v, unsigned s, u32x2 d) { __builtin_amdgcn_raw_buffer_store_b64(d, r, v, s, 16); }
DI void st4(rsrc_t r, unsigned v, unsigned s, unsigned d) { __builtin_amdgcn_raw_buffer_store_b32(d, r, v, s, 16); }

DI u32x4 lds16(const char* sm, unsigned o) { return *(const LAS u32x4*)(LAS const char*)(sm + o); }
DI f32x4 lds16f(const char* sm, unsigned o) { return *(const LAS f32x4*)(LAS const char*)(sm + o); }
DI float lds4f(const char* sm, unsigned o) { return *(const LAS float*)(LAS const char*)(sm + o); }
DI void sts16(char* sm, unsigned o, u32x4 v) { *(LAS u32x4*)(LAS char*)(sm + o) = v; }
DI void sts16f(char* sm, unsigned o, f32x4 v) { *(LAS f32x4*)(LAS char*)(sm + o) = v; }
DI void sts8(char* sm, unsigned o, u32x2 v) { *(LAS u32x2*)(LAS char*)(sm + o) = v; }
DI void sts4f(char* sm, unsigned o, float v) { *(LAS float*)(LAS char*)(sm + o) = v; }
DI u32x2 ldtr(const char* sm, unsigned o) { return __builtin_bit_cast(u32x2, __builtin_amdgcn_ds_read_tr16_b64_v4i16((LAS s16x4*)(LAS char*)(const_cast<char*>(sm) + o))); }

#define STAMP(slot) do {} while (0)

DI int s_nmap(int w, int t, int rho) {
    const int g = rho >> 2, r = rho & 3;
    return t < 6 ? 112 * w + 32 * (t >> 1) + 8 * g + 4 * (t & 1) + r : 112 * w + 96 + 8 * (g & 1) + 4 * (g >> 1) + r;
}

template <bool isS> DI void fold(const Params& p, char* sm, int b) {
    const int t = threadIdx.x, lane = t & 63, wv = t >> 6, l15 = lane & 15, g = lane >> 4;
    {
        const int h = (b & 127) >> 3, q4 = (b & 7) >> 1, kh = b & 1;
        const int ks = 7 * kh + wv;
        const unsigned short* am = isS ? p.wk : p.wv;
        u32x4 af[2][8];
#pragma unroll
        for (int u = 0; u < 2; ++u) {
            const int krow = 32 * ks + 8 * (l15 >> 2) + 4 * u + (l15 & 3);
            const unsigned short* src = am + (size_t)krow * 4096 + h * 256 + g * 8;
#pragma unroll
            for (int es = 0; es < 8; ++es) af[u][es] = *(const u32x4*)(src + es * 32);
        }
        if (isS) {
#pragma unroll
            for (int it = 0; it < 8; ++it) {
                const int unit = it * 7 + wv;
                const int row = (unit >> 2) * 8 + (lane & 7), ch = (unit & 3) * 8 + (lane >> 3);
                const int n = s_nmap(q4, row >> 4, row & 15);
                const u32x4 v = *(const u32x4*)(p.wq + (size_t)n * 4096 + h * 256 + ch * 8);
                sts16(sm, ch * 1792 + row * 16, v);
            }
        } else {
#pragma unroll
            for (int it = 0; it < 8; ++it) {
                const int i = it * 448 + t, e = i / 14, ch = i % 14;
                const u32x4 v = *(const u32x4*)(p.wo + ((size_t)h * 256 + e) * D + q4 * 112 + ch * 8);
                sts16(sm, e * 224 + ch * 16, v);
            }
        }
        __syncthreads();
        f32x4 acc[7][2];
#pragma unroll
        for (int tt = 0; tt < 7; ++tt) { acc[tt][0] = (f32x4){0, 0, 0, 0}; acc[tt][1] = (f32x4){0, 0, 0, 0}; }
#pragma unroll
        for (int es = 0; es < 8; ++es) {
#pragma unroll
            for (int tt = 0; tt < 7; ++tt) {
                u32x4 bf;
                if (isS) bf = lds16(sm, (4 * es + g) * 1792 + (tt * 16 + l15) * 16);
                else {
                    const unsigned o = (32 * es + 8 * g + (l15 >> 2)) * 224 + tt * 32 + (l15 & 3) * 8;
                    const u32x2 a = ldtr(sm, o), c = ldtr(sm, o + 4 * 224);
                    bf = (u32x4){a[0], a[1], c[0], c[1]};
                }
                acc[tt][0] = mfma(af[0][es], bf, acc[tt][0]);
                acc[tt][1] = mfma(af[1][es], bf, acc[tt][1]);
            }
        }
        const float sc = isS ? 0.0625f * 1.4426950408889634f : 1.0f;
#pragma unroll
        for (int tt = 0; tt < 7; ++tt) {
            const f32x4 a = acc[tt][0] * sc, c = acc[tt][1] * sc;
            const u32x4 v = {pk(a[0], a[1]), pk(a[2], a[3]), pk(c[0], c[1]), pk(c[2], c[3])};
            const unsigned o = isS ? O_WS + (((h * 4 + q4) * 7 + tt) * 14 + ks) * 1024 : O_WV + (((q4 * 7 + tt) * 16 + h) * 14 + ks) * 1024;
            *(u32x4*)(p.ws + o + lane * 16) = v;
        }
    }
}

__global__ void __launch_bounds__(448) prologue_kernel(Params p) {
    extern __shared__ __attribute__((aligned(16))) char sm[];
    const int b = blockIdx.x, t = threadIdx.x;
    if (b < 128) fold<true>(p, sm, b);
    else if (b < 256) {
        fold<false>(p, sm, b);
    } else if (b < 320) {
        if (t >= 256) return;
        const int s = b - 256, row = t >> 3, q = t & 7;
        u32x4 v[7]; float f[56]; float sum = 0.f;
#pragma unroll
        for (int i = 0; i < 7; ++i) v[i] = *(const u32x4*)(p.x + (size_t)(s * 32 + row) * D + (q + 8 * i) * 8);
#pragma unroll
        for (int i = 0; i < 7; ++i)
#pragma unroll
            for (int j = 0; j < 4; ++j) { f[i * 8 + 2 * j] = blo(v[i][j]); f[i * 8 + 2 * j + 1] = bhi(v[i][j]); sum += f[i * 8 + 2 * j] + f[i * 8 + 2 * j + 1]; }
        const float mean = oct8add(sum) * (1.0f / D);
        float sq = 0.f;
#pragma unroll
        for (int i = 0; i < 56; ++i) { f[i] -= mean; sq += f[i] * f[i]; }
        const float rs = __builtin_amdgcn_rsqf(oct8add(sq) * (1.0f / D) + 1e-5f);
#pragma unroll
        for (int i = 0; i < 7; ++i) {
            const unsigned o = s * SEGB + (q + 8 * i) * 512 + row * 16;
            const u32x4 n = {pk(f[i * 8] * rs, f[i * 8 + 1] * rs), pk(f[i * 8 + 2] * rs, f[i * 8 + 3] * rs), pk(f[i * 8 + 4] * rs, f[i * 8 + 5] * rs), pk(f[i * 8 + 6] * rs, f[i * 8 + 7] * rs)};
            *(u32x4*)(p.ws + O_XN + o) = n;
            *(u32x4*)(p.ws + O_XR + o) = v[i];
        }
    } else {
        const rsrc_t rs = __builtin_amdgcn_make_buffer_rsrc(p.ws, 0, WS_USED, 0x00020000);
        const int j = b - 320;
        const u32x4 P = {POISON, POISON, POISON, POISON}, Z = {0, 0, 0, 0};
        if (j == 0) {
            const int c = t >> 4, r = t & 15, i = r & 7, par = r >> 3;
            const unsigned* src = (const unsigned*)(p.mem0 + i * D + 16 * c) + par;
            const u32x4 v = {nopoison(src[0]), nopoison(src[2]), nopoison(src[4]), nopoison(src[6])};
            st16(rs, t * 16, O_HOP0, v); st16(rs, t * 16, O_HOP0 + 8192, v);
        } else if (j < 160) {
            const unsigned base = j < 96 ? O_HOP0 + j * 16384 : O_HOP1 + (j - 96) * 16384;
            for (int i = t; i < 1024; i += 448) st16(rs, i * 16, base, P);
        } else {
            const unsigned base = O_FLAG + (j - 160) * 16384;
            for (int i = t; i < 1024; i += 448) st16(rs, i * 16, base, j < 164 ? Z : P);
        }
    }
}

struct Ctx {
    rsrc_t rs;
    char* sm;
    char* ws;
    int lane, w, layer, id;
    unsigned hop0, hop1;
    bool dead;
};

DI bool give_up(Ctx& C, unsigned& spins) {
    ++spins;
    if (spins > SPIN_CAP) { st4(C.rs, 0, O_CTL + 256, 1u); C.dead = true; }
    else if ((spins & 1023u) == 0u && __builtin_amdgcn_readfirstlane(ld4(C.rs, 0, O_CTL + 256)) != 0u) C.dead = true;
    return C.dead;
}
DI void wait_flags(Ctx& C, unsigned line, unsigned want) {
    unsigned spins = 0;
    while (!C.dead) {
        asm volatile("" ::: "memory");
        const unsigned v = ld4(C.rs, line * 128u, O_FLAG);
        if (__all(v >= want)) break;
        if (give_up(C, spins)) break;
        __builtin_amdgcn_s_sleep(2);
    }
    asm volatile("" ::: "memory");
}
DI void publish_flag(Ctx& C, unsigned val) {
    drain(); bar();
    if (threadIdx.x == 0) st4(C.rs, 0, O_FLAG + C.id * 128u, val);
}
template <int N> DI void poll16(Ctx& C, u32x4 (&v)[N], const unsigned (&off)[N], unsigned base) {
    unsigned spins = 0;
    for (;;) {
        asm volatile("" ::: "memory");
        unsigned m = 0;
#pragma unroll
        for (int k = 0; k < N; ++k) v[k] = ld16(C.rs, off[k], base);
#pragma unroll
        for (int k = 0; k < N; ++k) { const unsigned x = umax4(v[k]); m = m > x ? m : x; }
        if (__all(m != POISON)) break;
        if (C.dead || give_up(C, spins)) break;
    }
    asm volatile("" ::: "memory");
}
DI unsigned cu_line(int layer, int sub, int idx) { return (unsigned)((layer * 4 + sub) * 32 + idx); }
DI unsigned g2_line(int layer, int rep, int cs) { return cu_line(layer, rep + 1, 28 + cs); }

DI void stage_xn(Ctx& C, int s, unsigned dst) {
    if (C.layer == 1) {
        const int u = 2 * s + ((C.lane >> 2) & 1);
        wait_flags(C, g2_line(0, u % 3, C.lane & 3), (unsigned)u + 1u);
    }
    const unsigned t16 = threadIdx.x * 16u, base = O_XN + C.layer * XSZ + s * SEGB;
    u32x4 v[7];
#pragma unroll
    for (int i = 0; i < 7; ++i) v[i] = ld16(C.rs, t16 + i * 4096u, base);
#pragma unroll
    for (int i = 0; i < 7; ++i) sts16(C.sm, dst + t16 + i * 4096u, v[i]);
}
DI void stage_mem(Ctx& C, int s, unsigned dst) {
    const unsigned t = threadIdx.x;
    const unsigned off[2] = {t * 16u, (t < 192u ? t + 256u : t) * 16u};
    u32x4 v[2];
    poll16<2>(C, v, off, C.hop0 + s * 16384u);
    sts16(C.sm, dst + off[0], v[0]);
    sts16(C.sm, dst + off[1], v[1]);
}

constexpr unsigned SL_MF = 0, SL_SP = 8192, SL_SMT = 28672, SL_SMM = 36864, SL_TR = 40960, SL_ML = 55296, SL_X = 57344;
DI void role_S(Ctx& C, int h, int rep) {
    const int lane = C.lane, w = C.w, l15 = lane & 15, g = lane >> 4;
    char* sm = C.sm;
    u32x8 W[7][7];
    {
        const unsigned base = O_WS + (h * 4 + w) * 100352u;
#pragma unroll
        for (int t = 0; t < 7; ++t)
#pragma unroll
            for (int k = 0; k < 7; ++k)
                W[t][k] = cat8(ld16p(C.rs, lane * 16u + (t * 14 + 2 * k) * 1024u, base), ld16p(C.rs, lane * 16u + (t * 14 + 2 * k + 1) * 1024u, base));
    }
    const int idx = (lane & 8) ? 0xEEEE : 0x4444;
    const f32x4 Z4 = {0, 0, 0, 0};
    for (int s = rep; s < NSEG; s += 2) {
        const unsigned xb = SL_X + ((s >> 1) & 1) * SEGB;
        STAMP(s * 8 + 0);
        stage_xn(C, s, xb);
        bar();
        STAMP(s * 8 + 1);
        u32x8 KA[2], KB[2];
#pragma unroll
        for (int kt = 0; kt < 2; ++kt) {
            f32x4 acc[7];
#pragma unroll
            for (int t = 0; t < 7; ++t) acc[t] = Z4;
#pragma unroll
            for (int ks = 0; ks < 14; ++ks) {
                const u32x4 x0 = lds16(sm, xb + (4 * ks + g) * 512 + kt * 256 + l15 * 16);
#pragma unroll
                for (int t = 0; t < 7; ++t) acc[t] = mfma((ks & 1) ? hi4(W[t][ks >> 1]) : lo4(W[t][ks >> 1]), x0, acc[t]);
            }
#pragma unroll
            for (int t = 0; t < 4; ++t) { KA[kt][2 * t] = pk(acc[t][0], acc[t][1]); KA[kt][2 * t + 1] = pk(acc[t][2], acc[t][3]); }
#pragma unroll
            for (int t = 0; t < 2; ++t) { KB[kt][2 * t] = pk(acc[4 + t][0], acc[4 + t][1]); KB[kt][2 * t + 1] = pk(acc[4 + t][2], acc[4 + t][3]); }
            const unsigned v0 = pk(acc[6][0], acc[6][1]), v1 = pk(acc[6][2], acc[6][3]);
            KB[kt][4] = g < 2 ? v0 : 0u; KB[kt][5] = g < 2 ? v1 : 0u; KB[kt][6] = g < 2 ? 0u : v0; KB[kt][7] = g < 2 ? 0u : v1;
            __builtin_amdgcn_sched_barrier(0);
        }
        const unsigned xq = xb + (14 * w + g) * 512 + l15 * 16, xq3 = xb + (14 * w + 12 + (g & 1)) * 512 + l15 * 16;
        {
            f32x4 s00 = Z4, s10 = Z4, s11 = Z4;
#pragma unroll
            for (int pp = 0; pp < 4; ++pp) {
                const unsigned o = pp < 3 ? xq + pp * 2048 : xq3;
                const u32x4 q0 = lds16(sm, o), q1 = lds16(sm, o + 256);
                const u32x4 k0 = pp == 0 ? lo4(KA[0]) : pp == 1 ? hi4(KA[0]) : pp == 2 ? lo4(KB[0]) : hi4(KB[0]);
                const u32x4 k1 = pp == 0 ? lo4(KA[1]) : pp == 1 ? hi4(KA[1]) : pp == 2 ? lo4(KB[1]) : hi4(KB[1]);
                s00 = mfma(k0, q0, s00); s10 = mfma(k0, q1, s10); s11 = mfma(k1, q1, s11);
            }
            sts16f(sm, SL_SP + (w * 5 + 0) * 1024 + lane * 16, s00);
            sts16f(sm, SL_SP + (w * 5 + 1) * 1024 + lane * 16, s10);
            sts16f(sm, SL_SP + (w * 5 + 2) * 1024 + lane * 16, s11);
        }
        STAMP(s * 8 + 2);
        stage_mem(C, s, SL_MF);
        bar();
        STAMP(s * 8 + 3);
        const bool more = s + 1 < NSEG;
        u32x4 mf[7];
        const u32x4 mA = lds16(sm, SL_MF + 7 * w * 256 + lane * 16), mB = lds16(sm, SL_MF + (7 * w + 4 + (g < 2 ? g : 2)) * 256 + l15 * 16);
#pragma unroll
        for (int k = 0; k < 7; ++k) mf[k] = lds16(sm, SL_MF + k * 1024 + lane * 16);
#pragma unroll
        for (int kt = 0; kt < 2; ++kt) {
            f32x4 d = smfma(mA, KA[kt], Z4, idx);
            d = smfma(mB, KB[kt], d, idx);
#pragma unroll
            for (int r = 0; r < 4; ++r) sts4f(sm, SL_SMT + (((2 * w + (g >> 1)) * 8 + 4 * (g & 1) + r) * 32 + kt * 16 + l15) * 4, d[r]);
        }
        u32x2 kt_[7];
#pragma unroll
        for (int t = 0; t < 7; ++t) {
            f32x4 d = Z4;
#pragma unroll
            for (int k = 0; k < 7; ++k) d = smfma(mf[k], W[t][k], d, idx);
            sts8(sm, SL_TR + (w * 7 + t) * 512 + l15 * 32 + g * 8, (u32x2){pk(d[0], d[1]), pk(d[2], d[3])});
            kt_[t] = ldtr(sm, SL_TR + (w * 7 + t) * 512 + lane * 8);
            if (t == 0) {
                bar();
                const int i = 2 * w + (lane >> 5);
                float v = 0.f;
#pragma unroll
                for (int pch = 0; pch < 8; ++pch) v += lds4f(sm, SL_SMT + ((pch * 8 + i) * 32 + (lane & 31)) * 4);
                const float m = x16max(row16max(v));
                const float e = ex2(v - m);
                const float l = x16add(row16add(e));
                const float pn = e * rcp(l);
                const unsigned d2 = nopoison(pk(pn, dpp<0xB1>(pn)));
                if (more && !(lane & 1)) st4(C.rs, h * 512 + w * 128 + lane * 2, C.hop1 + s * 16384u, d2);
                if ((lane & 31) == 0) { sts4f(sm, SL_ML + i * 8, m); sts4f(sm, SL_ML + i * 8 + 4, l); }
            }
        }
        const u32x8 MA = {kt_[0][0], kt_[0][1], kt_[1][0], kt_[1][1], kt_[2][0], kt_[2][1], kt_[3][0], kt_[3][1]};
        const u32x8 MBq = {kt_[4][0], kt_[4][1], kt_[5][0], kt_[5][1], g < 2 ? kt_[6][0] : 0u, g < 2 ? kt_[6][1] : 0u, g < 2 ? 0u : kt_[6][0], g < 2 ? 0u : kt_[6][1]};
        {
            f32x4 d = smfma(mA, MA, Z4, idx);
            d = smfma(mB, MBq, d, idx);
            sts16f(sm, SL_SMM + (w * 256 + l15 * 16 + 4 * g) * 4, d);
        }
        bar();
        if (w == 0) {
            const int i = lane >> 3, j = lane & 7;
            float v = 0.f;
#pragma unroll
            for (int q = 0; q < 8; ++q) {
                const unsigned o = SL_SMM + ((q >> 1) * 256 + (j + 8 * (q & 1)) * 16 + i) * 4;
                v += lds4f(sm, o) + lds4f(sm, o + 32);
            }
            const float mt = lds4f(sm, SL_ML + i * 8), lt = lds4f(sm, SL_ML + i * 8 + 4);
            const float m = fmaxf(oct8max(v), mt);
            const float e = ex2(v - m), a = lt * ex2(mt - m);
            const float r = rcp(oct8add(e) + a);
            const float pn = e * r, al = a * r;
            const float pnx = dpp<0xB1>(pn);
            const unsigned d2 = (j & 1) ? pk(i == j - 1 ? al : 0.f, i == j ? al : 0.f) : pk(pn, pnx);
            if (more) st4(C.rs, h * 256 + (j & 1) * 128 + i * 16 + (j >> 1) * 4, C.hop0 + (64 + (s >> 1)) * 16384u + (s & 1) * 4096u, nopoison(d2));
        }
        STAMP(s * 8 + 4);
        {
            f32x4 s0 = Z4, s1 = Z4;
#pragma unroll
            for (int pp = 0; pp < 4; ++pp) {
                const unsigned o = pp < 3 ? xq + pp * 2048 : xq3;
                const u32x4 q0 = lds16(sm, o), q1 = lds16(sm, o + 256);
                const u32x4 k0 = pp == 0 ? lo4(MA) : pp == 1 ? hi4(MA) : pp == 2 ? lo4(MBq) : hi4(MBq);
                s0 = mfma(k0, q0, s0); s1 = mfma(k0, q1, s1);
            }
            sts16f(sm, SL_SP + (w * 5 + 3) * 1024 + lane * 16, s0);
            sts16f(sm, SL_SP + (w * 5 + 4) * 1024 + lane * 16, s1);
        }
        bar();
        if (w < 2) {
            f32x4 a = Z4, bq = Z4, mm = Z4;
#pragma unroll
            for (int q = 0; q < 4; ++q) {
                a += lds16f(sm, SL_SP + (q * 5 + w) * 1024 + lane * 16);
                bq += lds16f(sm, SL_SP + (q * 5 + 2) * 1024 + lane * 16);
                mm += lds16f(sm, SL_SP + (q * 5 + 3 + w) * 1024 + lane * 16);
            }
            const float NEG = -1e30f;
            float mx = NEG;
#pragma unroll
            for (int r = 0; r < 4; ++r) {
                mm[r] = x32add(mm[r]);
                const bool vis = 4 * g + r <= l15;
                if (w == 0) { a[r] = vis ? a[r] : NEG; bq[r] = NEG; } else bq[r] = vis ? bq[r] : NEG;
                mx = fmaxf(mx, fmaxf(mm[r], fmaxf(a[r], bq[r])));
            }
            mx = x32max(x16max(mx));
            float l = 0.f;
#pragma unroll
            for (int r = 0; r < 4; ++r) {
                a[r] = ex2(a[r] - mx); bq[r] = ex2(bq[r] - mx); mm[r] = ex2(mm[r] - mx);
                l += a[r] + bq[r] + (g < 2 ? mm[r] : 0.f);
            }
            const float rl = rcp(x32add(x16add(l)));
            const unsigned base = O_PT + C.layer * PTSZ + (s * 16 + h) * 2560u;
            st16(C.rs, w * 1024 + lane * 16, base, (u32x4){pk(a[0] * rl, a[1] * rl), pk(a[2] * rl, a[3] * rl), pk(bq[0] * rl, bq[1] * rl), pk(bq[2] * rl, bq[3] * rl)});
            if (g < 2) st8(C.rs, 2048 + (w * 16 + l15) * 16 + g * 8, base, (u32x2){pk(mm[0] * rl, mm[1] * rl), pk(mm[2] * rl, mm[3] * rl)});
        }
        publish_flag(C, (unsigned)s + 1u);
        STAMP(s * 8 + 5);
    }
}

constexpr unsigned VL_X = 0, VL_MF = 28672, VL_RED = 36864, VL_TOK = 40960;
DI void v_gemm(Ctx& C, const u32x8 (&W)[4][7], u32x4 (&VB)[4]) {
    const int l15 = C.lane & 15, g = C.lane >> 4;
    f32x4 acc[4][2];
#pragma unroll
    for (int a = 0; a < 4; ++a) { acc[a][0] = (f32x4){0, 0, 0, 0}; acc[a][1] = (f32x4){0, 0, 0, 0}; }
#pragma unroll
    for (int ks = 0; ks < 14; ++ks) {
        const u32x4 x0 = lds16(C.sm, VL_X + (4 * ks + g) * 512 + l15 * 16), x1 = lds16(C.sm, VL_X + (4 * ks + g) * 512 + 256 + l15 * 16);
#pragma unroll
        for (int a = 0; a < 4; ++a) {
            const u32x4 b = (ks & 1) ? hi4(W[a][ks >> 1]) : lo4(W[a][ks >> 1]);
            acc[a][0] = mfma(x0, b, acc[a][0]);
            acc[a][1] = mfma(x1, b, acc[a][1]);
        }
    }
#pragma unroll
    for (int a = 0; a < 4; ++a) VB[a] = (u32x4){pk(acc[a][0][0], acc[a][0][1]), pk(acc[a][0][2], acc[a][0][3]), pk(acc[a][1][0], acc[a][1][1]), pk(acc[a][1][2], acc[a][1][3])};
}
DI void role_V(Ctx& C, int c, int rep) {
    const int lane = C.lane, w = C.w, l15 = lane & 15, g = lane >> 4;
    char* sm = C.sm;
    u32x8 W[4][7];
    {
        const unsigned base = O_WV + (c * 16 + 4 * w) * 14336u;
#pragma unroll
        for (int a = 0; a < 4; ++a)
#pragma unroll
            for (int k = 0; k < 7; ++k)
                W[a][k] = cat8(ld16p(C.rs, lane * 16u + (a * 14 + 2 * k) * 1024u, base), ld16p(C.rs, lane * 16u + (a * 14 + 2 * k + 1) * 1024u, base));
    }
    const int idx = (lane & 8) ? 0xEEEE : 0x4444;
    const f32x4 Z4 = {0, 0, 0, 0};
    u32x4 VB[4];
    stage_xn(C, rep, VL_X);
    bar();
    v_gemm(C, W, VB);
    for (int s = rep; s < NSEG; s += 2) {
        STAMP(s * 8 + 0);
        stage_mem(C, s, VL_MF);
        bar();
        STAMP(s * 8 + 1);
        u32x4 UF;
        {
            u32x4 mf[7];
#pragma unroll
            for (int k = 0; k < 7; ++k) mf[k] = lds16(sm, VL_MF + k * 1024 + lane * 16);
            f32x4 u[4];
#pragma unroll
            for (int a = 0; a < 4; ++a) u[a] = Z4;
#pragma unroll
            for (int k = 0; k < 7; ++k)
#pragma unroll
                for (int a = 0; a < 4; ++a) u[a] = smfma(mf[k], W[a][k], u[a], idx);
#pragma unroll
            for (int a = 0; a < 2; ++a) {
                UF[2 * a] = pk(halfsum(u[2 * a][0], u[2 * a + 1][0]), halfsum(u[2 * a][1], u[2 * a + 1][1]));
                UF[2 * a + 1] = pk(halfsum(u[2 * a][2], u[2 * a + 1][2]), halfsum(u[2 * a][3], u[2 * a + 1][3]));
            }
        }
        if (s + 1 < NSEG) {
            u32x4 pt[2];
            const unsigned o0 = (4 * w + ((lane >> 3) & 1)) * 512 + (lane & 7) * 64 + g * 16;
            const unsigned off[2] = {o0, o0 + 1024};
            poll16<2>(C, pt, off, C.hop1 + s * 16384u);
            STAMP(s * 8 + 2);
            u32x8 BF;
#pragma unroll
            for (int a = 0; a < 2; ++a) {
                const u32x4 v0 = VB[2 * a], v1 = VB[2 * a + 1];
                const f32x4 o = smfma(pt[a], (u32x8){v0[0], v1[0], v0[1], v1[1], v0[2], v1[2], v0[3], v1[3]}, Z4, idx);
                BF[4 * a] = UF[2 * a]; BF[4 * a + 1] = pk(o[0], o[1]); BF[4 * a + 2] = UF[2 * a + 1]; BF[4 * a + 3] = pk(o[2], o[3]);
            }
            u32x4 pm[1];
            const unsigned off1[1] = {(unsigned)(w * 1024 + lane * 16)};
            poll16<1>(C, pm, off1, C.hop0 + (64 + (s >> 1)) * 16384u + (s & 1) * 4096u);
            STAMP(s * 8 + 3);
            const f32x4 d = smfma(pm[0], BF, Z4, idx);
            sts16f(sm, VL_RED + ((2 * w + (g >> 1)) * 128 + l15 * 8 + 4 * (g & 1)) * 4, d);
            bar();
            if (w == 0) {
                const int r16 = lane >> 2, c0 = 4 * (lane & 3) + 2 * (r16 >> 3);
                float a0 = 0.f, a1 = 0.f;
#pragma unroll
                for (int q = 0; q < 8; ++q) {
                    const unsigned o = VL_RED + (q * 128 + c0 * 8 + (r16 & 7)) * 4;
                    a0 += lds4f(sm, o); a1 += lds4f(sm, o + 32);
                }
                st4(C.rs, c * 256 + lane * 4, C.hop0 + (s + 1) * 16384u, nopoison(pk(a0, a1)));
            }
        }
        STAMP(s * 8 + 4);
        const bool nxt = s + 2 < NSEG;
        if (nxt) stage_xn(C, s + 2, VL_X);
        wait_flags(C, cu_line(C.layer, 0, rep * 16 + 4 * w + (lane & 3)), (unsigned)s + 1u);
        STAMP(s * 8 + 5);
        u32x4 P[4][2]; u32x2 PM[2][2]; u32x2 res;
        {
            const unsigned base = O_PT + C.layer * PTSZ + (s * 16 + 4 * w) * 2560u;
#pragma unroll
            for (int a = 0; a < 4; ++a) { P[a][0] = ld16(C.rs, a * 2560 + lane * 16, base); P[a][1] = ld16(C.rs, a * 2560 + 1024 + lane * 16, base); }
#pragma unroll
            for (int tt = 0; tt < 2; ++tt)
#pragma unroll
                for (int a = 0; a < 2; ++a) PM[tt][a] = ld8(C.rs, (2 * a + (g >> 1)) * 2560 + 2048 + (tt * 16 + l15) * 16 + (g & 1) * 8, base);
        }
        const unsigned xo = (2 * c + (g >> 1)) * 512 + ((w & 1) * 16 + l15) * 16 + (g & 1) * 8;
        res = ld8(C.rs, xo, O_XR + C.layer * XSZ + s * SEGB);
        u32x4 VN[4];
        bar();
        if (nxt) v_gemm(C, W, VN);
        STAMP(s * 8 + 6);
#pragma unroll
        for (int tt = 0; tt < 2; ++tt) {
            f32x4 d = mfma(UF, (u32x4){PM[tt][0][0], PM[tt][0][1], PM[tt][1][0], PM[tt][1][1]}, Z4);
#pragma unroll
            for (int a = 0; a < 4; ++a) d = mfma(VB[a], P[a][tt], d);
            sts16f(sm, VL_TOK + (w * 2 + tt) * 1024 + lane * 16, d);
        }
        bar();
        if (w < 2) {
            f32x4 d = Z4;
#pragma unroll
            for (int q = 0; q < 4; ++q) d += lds16f(sm, VL_TOK + (q * 2 + w) * 1024 + lane * 16);
            st8(C.rs, xo, O_X1 + C.layer * XSZ + s * SEGB, (u32x2){pk(d[0] + blo(res[0]), d[1] + bhi(res[0])), pk(d[2] + blo(res[1]), d[3] + bhi(res[1]))});
        }
        publish_flag(C, (unsigned)s + 1u);
        STAMP(s * 8 + 7);
        if (nxt) {
#pragma unroll
            for (int a = 0; a < 4; ++a) VB[a] = VN[a];
        }
    }
}

DI void role_G1(Ctx& C, const Params& p, int th, int fs) {
    const int lane = C.lane, w = C.w, l15 = lane & 15, g = lane >> 4;
    char* sm = C.sm;
    u32x4 W[4][14];
    {
        const unsigned sb = w * 28672u;
        const int f0 = fs * 128 + w * 32;
#pragma unroll
        for (int m = 0; m < 2; ++m) {
            const unsigned short* src = (m ? p.wu : p.wg) + f0 + (lane & 3) * 8;
#pragma unroll 7
            for (int it = 0; it < 28; ++it) {
                const int k = it * 16 + (lane >> 2);
                sts16(sm, sb + k * 64 + (lane & 3) * 16, *(const u32x4*)(src + (size_t)k * FF));
            }
#pragma unroll
            for (int ks = 0; ks < 14; ++ks)
#pragma unroll
                for (int u = 0; u < 2; ++u) {
                    const unsigned o = sb + (32 * ks + 8 * g + (l15 >> 2)) * 64 + (l15 & 3) * 16 + u * 8;
                    const u32x2 a = ldtr(sm, o), b = ldtr(sm, o + 256);
                    W[2 * m + u][ks] = (u32x4){a[0], a[1], b[0], b[1]};
                }
        }
    }
    bar();
    const int row = threadIdx.x >> 4;
    for (int s = 0; s < NSEG; ++s) {
        STAMP(s * 8 + 0);
        wait_flags(C, cu_line(C.layer, 1 + (s & 1), lane < 28 ? lane : 0), (unsigned)s + 1u);
        STAMP(s * 8 + 1);
        const unsigned base = O_X1 + C.layer * XSZ + s * SEGB;
        const unsigned xb = (s & 1) * 16384u;
        const bool tail = l15 < 8;
        u32x4 v[4]; float f[32]; float sum = 0.f;
#pragma unroll
        for (int i = 0; i < 4; ++i) v[i] = ld16(C.rs, ((i < 3 || tail) ? l15 + 16 * i : l15) * 512 + (th * 16 + row) * 16, base);
        if (!tail) v[3] = (u32x4){0, 0, 0, 0};
#pragma unroll
        for (int i = 0; i < 4; ++i)
#pragma unroll
            for (int j = 0; j < 4; ++j) { f[i * 8 + 2 * j] = blo(v[i][j]); f[i * 8 + 2 * j + 1] = bhi(v[i][j]); sum += f[i * 8 + 2 * j] + f[i * 8 + 2 * j + 1]; }
        const float mean = row16add(sum) * (1.0f / D);
        float sq = 0.f, sq3 = 0.f;
#pragma unroll
        for (int i = 0; i < 24; ++i) { f[i] -= mean; sq += f[i] * f[i]; }
#pragma unroll
        for (int i = 24; i < 32; ++i) { f[i] -= mean; sq3 += f[i] * f[i]; }
        const float rs = __builtin_amdgcn_rsqf(row16add(sq + (tail ? sq3 : 0.f)) * (1.0f / D) + 1e-5f);
#pragma unroll
        for (int i = 0; i < 4; ++i) {
            const u32x4 n = {pk(f[i * 8] * rs, f[i * 8 + 1] * rs), pk(f[i * 8 + 2] * rs, f[i * 8 + 3] * rs), pk(f[i * 8 + 4] * rs, f[i * 8 + 5] * rs), pk(f[i * 8 + 6] * rs, f[i * 8 + 7] * rs)};
            sts16(sm, xb + (l15 + 16 * i) * 256 + row * 16, n);
        }
        bar();
        STAMP(s * 8 + 2);
        f32x4 acc[4];
#pragma unroll
        for (int a = 0; a < 4; ++a) acc[a] = (f32x4){0, 0, 0, 0};
#pragma unroll
        for (int ks = 0; ks < 14; ++ks) {
            const u32x4 x0 = lds16(sm, xb + ks * 1024 + lane * 16);
#pragma unroll
            for (int a = 0; a < 4; ++a) acc[a] = mfma(W[a][ks], x0, acc[a]);
        }
        float hv[8];
#pragma unroll
        for (int e = 0; e < 8; ++e) {
            const float gt = acc[e >> 2][e & 3], up = acc[2 + (e >> 2)][e & 3];
            hv[e] = gt * rcp(1.0f + ex2(gt * -1.4426950408889634f)) * up;
        }
        st16(C.rs, lane * 16, O_HID + C.layer * HIDSZ + ((2 * s + th) * 56 + 4 * fs + w) * 1024u, (u32x4){pk(hv[0], hv[1]), pk(hv[2], hv[3]), pk(hv[4], hv[5]), pk(hv[6], hv[7])});
        publish_flag(C, (unsigned)s + 1u);
        STAMP(s * 8 + 3);
    }
}

DI void role_G2(Ctx& C, const Params& p, int rep, int cs) {
    const int lane = C.lane, w = C.w, l15 = lane & 15, g = lane >> 4;
    char* sm = C.sm;
    u32x4 W[7][14];
    {
        const unsigned sb = w * 14336u;
#pragma unroll
        for (int rd = 0; rd < 7; ++rd) {
#pragma unroll
            for (int it = 0; it < 14; ++it) {
                const int i = it * 64 + lane, k = i / 14, ch = i % 14;
                sts16(sm, sb + i * 16, *(const u32x4*)(p.wd + (size_t)(448 * w + 64 * rd + k) * D + cs * 112 + ch * 8));
            }
#pragma unroll
            for (int u = 0; u < 2; ++u)
#pragma unroll
                for (int t = 0; t < 7; ++t) {
                    const unsigned o = sb + (32 * u + 8 * g + (l15 >> 2)) * 224 + t * 32 + (l15 & 3) * 8;
                    const u32x2 a = ldtr(sm, o), b = ldtr(sm, o + 4 * 224);
                    W[t][2 * rd + u] = (u32x4){a[0], a[1], b[0], b[1]};
                }
        }
    }
    bar();
    const int tok = threadIdx.x >> 4;
    const bool act = l15 < 14;
    const int ch = act ? l15 : 0;
    for (int u = rep; u < 2 * NSEG; u += 3) {
        const int s = u >> 1, th = u & 1;
        STAMP(u * 8 + 0);
        wait_flags(C, lane < 14 ? cu_line(C.layer, 3, th * 14 + lane) : cu_line(C.layer, 1 + (s & 1), cs * 7 + (lane % 7)), (unsigned)s + 1u);
        STAMP(u * 8 + 1);
        u32x4 hf[14];
#pragma unroll
        for (int ks = 0; ks < 14; ++ks) hf[ks] = ld16(C.rs, lane * 16 + ks * 1024, O_HID + C.layer * HIDSZ + (u * 56 + 14 * w) * 1024u);
        const unsigned xo = s * SEGB + (cs * 14 + ch) * 512 + (th * 16 + tok) * 16;
        const u32x4 res = ld16(C.rs, xo, O_X1 + C.layer * XSZ);
        f32x4 acc[7];
#pragma unroll
        for (int t = 0; t < 7; ++t) acc[t] = (f32x4){0, 0, 0, 0};
#pragma unroll
        for (int ks = 0; ks < 14; ++ks)
#pragma unroll
            for (int t = 0; t < 7; ++t) acc[t] = mfma(W[t][ks], hf[ks], acc[t]);
#pragma unroll
        for (int t = 0; t < 7; ++t) sts16f(sm, (w * 7 + t) * 1024 + lane * 16, acc[t]);
        bar();
        STAMP(u * 8 + 2);
        f32x4 a = {0, 0, 0, 0}, b = {0, 0, 0, 0};
#pragma unroll
        for (int q = 0; q < 4; ++q) {
            const unsigned o = (q * 7 + (ch >> 1)) * 1024 + ((ch & 1) * 32 + tok) * 16;
            a += lds16f(sm, o); b += lds16f(sm, o + 256);
        }
        const u32x4 y = {pk(a[0] + blo(res[0]), a[1] + bhi(res[0])), pk(a[2] + blo(res[1]), a[3] + bhi(res[1])),
                         pk(b[0] + blo(res[2]), b[1] + bhi(res[2])), pk(b[2] + blo(res[3]), b[3] + bhi(res[3]))};
        if (C.layer == 1) {
            if (act) *(u32x4*)(p.out + (size_t)(s * 32 + th * 16 + tok) * D + cs * 112 + ch * 8) = y;
            bar();
        } else {
            float f[8]; float sum = 0.f;
#pragma unroll
            for (int j = 0; j < 4; ++j) { f[2 * j] = blo(y[j]); f[2 * j + 1] = bhi(y[j]); sum += f[2 * j] + f[2 * j + 1]; }
            const float mean = row16add(act ? sum : 0.f) * (1.0f / 112);
            float sq = 0.f;
#pragma unroll
            for (int j = 0; j < 8; ++j) { const float dlt = f[j] - mean; sq += dlt * dlt; }
            const float m2 = row16add(act ? sq : 0.f);
            if (l15 == 0) st8(C.rs, (u * 4 + cs) * 128 + tok * 8, O_STAT, (u32x2){nopoison(asu(mean)), nopoison(asu(m2))});
            if (act) st16(C.rs, xo, O_XR + XSZ, y);
            u32x2 o4;
            {
                unsigned spins = 0;
                for (;;) {
                    asm volatile("" ::: "memory");
                    o4 = ld8(C.rs, (u * 4 + (l15 & 3)) * 128 + tok * 8, O_STAT);
                    if (__all(o4[0] != POISON && o4[1] != POISON)) break;
                    if (C.dead || give_up(C, spins)) break;
                }
            }
            const float mq = asf(o4[0]);
            float gm = mq + dpp<0xB1>(mq); gm = (gm + dpp<0x4E>(gm)) * 0.25f;
            float vq = asf(o4[1]) + 112.0f * (mq - gm) * (mq - gm);
            vq += dpp<0xB1>(vq); vq += dpp<0x4E>(vq);
            const float rs = __builtin_amdgcn_rsqf(vq * (1.0f / D) + 1e-5f);
            const u32x4 n = {pk((f[0] - gm) * rs, (f[1] - gm) * rs), pk((f[2] - gm) * rs, (f[3] - gm) * rs), pk((f[4] - gm) * rs, (f[5] - gm) * rs), pk((f[6] - gm) * rs, (f[7] - gm) * rs)};
            if (act) st16(C.rs, xo, O_XN + XSZ, n);
            publish_flag(C, (unsigned)u + 1u);
        }
        STAMP(u * 8 + 3);
    }
}

DI unsigned probe(rsrc_t rs, unsigned off) {
    unsigned best = ~0u;
    for (int r = 0; r < 3; ++r) {
        unsigned long long t0, t1;
        asm volatile("s_memtime %0\n\ts_waitcnt lgkmcnt(0)" : "=s"(t0) :: "memory");
        st4(rs, (threadIdx.x & 63) * 128, off, 0u);
        drain();
        asm volatile("s_memtime %0\n\ts_waitcnt lgkmcnt(0)" : "=s"(t1) :: "memory");
        const unsigned d = (unsigned)(t1 - t0);
        best = d < best ? d : best;
    }
    return best;
}
DI unsigned wait_word(Ctx& C, unsigned off) {
    unsigned spins = 0, v = 0;
    while (!C.dead) {
        asm volatile("" ::: "memory");
        v = __builtin_amdgcn_readfirstlane(ld4(C.rs, 0, off));
        if (v != 0u) break;
        if (give_up(C, spins)) break;
    }
    return v;
}

__global__ void __launch_bounds__(256, 1) main_kernel(Params p) {
    extern __shared__ __attribute__((aligned(16))) char sm[];
    Ctx C;
    C.rs = __builtin_amdgcn_make_buffer_rsrc(p.ws, 0, WS_USED, 0x00020000);
    C.sm = sm; C.ws = p.ws; C.lane = threadIdx.x & 63; C.w = __builtin_amdgcn_readfirstlane(threadIdx.x >> 6); C.dead = false;
    const int b = blockIdx.x;
    if (b == 0 && threadIdx.x == 0) st4(C.rs, 0, O_CTL, 1u + ((unsigned)__builtin_amdgcn_s_getreg(20 | (3 << 11)) & 7u));
    const int rot = (int)(wait_word(C, O_CTL) - 1u) & 7;
    const int grp = (b + rot) & 7, idx = b >> 3, sub = grp & 3;
    C.layer = grp >> 2; C.id = grp * 32 + idx;
    if (grp == 1 && idx == 28 && C.w == 0) {
        const unsigned a0 = probe(C.rs, O_HOP0 + 100 * 16384), a1 = probe(C.rs, O_HOP0 + 100 * 16384 + 8192);
        const unsigned b0 = probe(C.rs, O_HOP1 + 100 * 16384), b1 = probe(C.rs, O_HOP1 + 100 * 16384 + 8192);
        if (C.lane == 0) st4(C.rs, 0, O_CTL + 128, 4u | (a1 < a0 ? 1u : 0u) | (b1 < b0 ? 2u : 0u));
    }
    if (sub < 3 && (sub == 0 || idx < 28)) {
        const unsigned pw = wait_word(C, O_CTL + 128);
        C.hop0 = O_HOP0 + (((pw & 1u) ^ (unsigned)C.layer) << 13);
        C.hop1 = O_HOP1 + ((((pw >> 1) & 1u) ^ (unsigned)C.layer) << 13);
        if (sub == 0) role_S(C, idx & 15, idx >> 4);
        else role_V(C, idx, sub - 1);
    } else if (idx < 28) role_G1(C, p, idx / 14, idx % 14);
    else role_G2(C, p, sub - 1, idx - 28);
}

extern "C" void kernel_launch(void* const* d_in, const int* in_sizes, int n_in, void* d_out, int out_size, void* d_ws, size_t ws_size,
                              hipStream_t stream) {
    Params p;
    p.x = (const unsigned short*)d_in[0]; p.mem0 = (const unsigned short*)d_in[1];
    p.wq = (const unsigned short*)d_in[2]; p.wk = (const unsigned short*)d_in[3]; p.wv = (const unsigned short*)d_in[4]; p.wo = (const unsigned short*)d_in[5];
    p.wg = (const unsigned short*)d_in[8]; p.wu = (const unsigned short*)d_in[9]; p.wd = (const unsigned short*)d_in[10];
    p.out = (unsigned short*)d_out;
    p.ws = (char*)(((size_t)d_ws + (2u << 20) - 1) & ~(size_t)((2u << 20) - 1));
    constexpr int kLds = 116 * 1024;
    (void)hipFuncSetAttribute((const void*)main_kernel, hipFuncAttributeMaxDynamicSharedMemorySize, kLds);
    hipLaunchKernelGGL(prologue_kernel, dim3(320 + 168), dim3(448), 57344, stream, p);
    hipLaunchKernelGGL(main_kernel, dim3(256), dim3(256), kLds, stream, p);
}
```

```cpp
#include <hip/hip_runtime.h>
#include <hip/hip_bf16.h>
#include <type_traits>

typedef __bf16 bf16x8 __attribute__((ext_vector_type(8)));
typedef __bf16 bf16x16 __attribute__((ext_vector_type(16)));
typedef __bf16 bf16x2 __attribute__((ext_vector_type(2)));
typedef float f32x2 __attribute__((ext_vector_type(2)));
typedef float f32x4 __attribute__((ext_vector_type(4)));
typedef unsigned u32x2 __attribute__((ext_vector_type(2)));
typedef unsigned u32x4 __attribute__((ext_vector_type(4)));
typedef unsigned u32x8 __attribute__((ext_vector_type(8)));
typedef unsigned u32x16 __attribute__((ext_vector_type(16)));
typedef unsigned u32x32 __attribute__((ext_vector_type(32)));
typedef int i32x4 __attribute__((ext_vector_type(4)));
typedef short s16x4 __attribute__((ext_vector_type(4)));
#define LAS __attribute__((address_space(3)))
#define DI __device__ __forceinline__
typedef __amdgpu_buffer_rsrc_t rsrc_t;

constexpr int D = 448, NH = 16, FF = 1792, NSEG = 64;
constexpr unsigned MB = 1u << 20;
constexpr unsigned O_HOP0 = 0;
constexpr unsigned O_HOP1 = 2 * MB;
constexpr unsigned J_PM = 76, J_STAT = 80, J_PROBE = 120;
constexpr unsigned O_CTL = 4 * MB + 32768;
constexpr unsigned O_WS = 5 * MB;
constexpr unsigned O_WV = 12 * MB;
constexpr unsigned XSZ = 3 * MB;
constexpr unsigned O_XN = 19 * MB;
constexpr unsigned O_XR = 25 * MB;
constexpr unsigned O_X1 = 31 * MB;
constexpr unsigned O_PT = 37 * MB;
constexpr unsigned PTSZ = 3 * MB;
constexpr unsigned O_HID = 43 * MB;
constexpr unsigned HIDSZ = 9 * MB;
constexpr unsigned O_DBG = 61 * MB;
constexpr unsigned O_HOP2 = 64 * MB;
constexpr unsigned WS_USED = 66 * MB;
constexpr unsigned SEGB = 28672;
constexpr unsigned POISON = 0xFFFFFFFFu;
constexpr unsigned SPIN_CAP = 1u << 19;
constexpr unsigned OOB = 0x7FFFFFF0u;
#define MIX 1
#define STAG0 0
#define CODEPF 1
constexpr unsigned PRO_CODE = 16384, MAIN_CODE = 32768;
#define NPROBE 2
#define STAG1 0
#define STAG2 6
#define NTW 1
#define WAUX 16
#define NOST 0
#define STAG3 4
#define FFPF 1
#define STAGGER 0
constexpr int AHEAD = 8;

struct Params {
    const unsigned short *x, *mem0, *wq, *wk, *wv, *wo, *wg, *wu, *wd;
    unsigned short* out;
    char* ws;
};

DI f32x4 mfma(u32x4 a, u32x4 b, f32x4 c) {
    return __builtin_amdgcn_mfma_f32_16x16x32_bf16(__builtin_bit_cast(bf16x8, a), __builtin_bit_cast(bf16x8, b), c, 0, 0, 0);
}
DI f32x4 smfma(u32x4 a, u32x8 b, f32x4 c, int idx) {
    return __builtin_amdgcn_smfmac_f32_16x16x64_bf16(__builtin_bit_cast(bf16x8, a), __builtin_bit_cast(bf16x16, b), c, idx, 0, 0);
}
DI unsigned pk(float a, float b) { f32x2 v = {a, b}; return __builtin_bit_cast(unsigned, __builtin_convertvector(v, bf16x2)); }
DI float asf(unsigned u) { return __builtin_bit_cast(float, u); }
DI unsigned asu(float f) { return __builtin_bit_cast(unsigned, f); }
DI float blo(unsigned u) { return __builtin_bit_cast(float, u << 16); }
DI float bhi(unsigned u) { return __builtin_bit_cast(float, u & 0xFFFF0000u); }
DI u32x4 lo4(u32x8 v) { return __builtin_shufflevector(v, v, 0, 1, 2, 3); }
DI u32x4 hi4(u32x8 v) { return __builtin_shufflevector(v, v, 4, 5, 6, 7); }
DI u32x8 cat8(u32x4 a, u32x4 b) { return __builtin_shufflevector(a, b, 0, 1, 2, 3, 4, 5, 6, 7); }
DI void bar() { asm volatile("s_waitcnt lgkmcnt(0)\n\ts_barrier" ::: "memory"); }
DI void drain() { asm volatile("s_waitcnt vmcnt(0)" ::: "memory"); }
template <int C> DI float dpp(float v) { return __builtin_bit_cast(float, __builtin_amdgcn_update_dpp(0, __builtin_bit_cast(int, v), C, 0xF, 0xF, true)); }
DI float ex2(float v) { return __builtin_amdgcn_exp2f(v); }
DI float rcp(float v) { return __builtin_amdgcn_rcpf(v); }
DI float x16add(float v) { auto r = __builtin_amdgcn_permlane16_swap(asu(v), asu(v), false, false); return asf(r[0]) + asf(r[1]); }
DI float x16max(float v) { auto r = __builtin_amdgcn_permlane16_swap(asu(v), asu(v), false, false); return __builtin_amdgcn_fmed3f(asf(r[0]), asf(r[1]), __builtin_inff()); }
DI float x32add(float v) { auto r = __builtin_amdgcn_permlane32_swap(asu(v), asu(v), false, false); return asf(r[0]) + asf(r[1]); }
DI float x32max(float v) { auto r = __builtin_amdgcn_permlane32_swap(asu(v), asu(v), false, false); return __builtin_amdgcn_fmed3f(asf(r[0]), asf(r[1]), __builtin_inff()); }
DI float halfsum(float a, float b) { auto r = __builtin_amdgcn_permlane32_swap(asu(a), asu(b), false, false); return asf(r[0]) + asf(r[1]); }
DI float row16add(float v) { v += dpp<0x128>(v); v += dpp<0x124>(v); v += dpp<0x122>(v); v += dpp<0x121>(v); return v; }
DI float row16max(float v) { v = fmaxf(v, dpp<0x128>(v)); v = fmaxf(v, dpp<0x124>(v)); v = fmaxf(v, dpp<0x122>(v)); v = fmaxf(v, dpp<0x121>(v)); return v; }
DI float oct8add(float v) { v += dpp<0xB1>(v); v += dpp<0x4E>(v); v += dpp<0x141>(v); return v; }
DI float oct8max(float v) { v = fmaxf(v, dpp<0xB1>(v)); v = fmaxf(v, dpp<0x4E>(v)); v = fmaxf(v, dpp<0x141>(v)); return v; }
#define SB __builtin_amdgcn_sched_barrier(0)
#define PINV(x) asm volatile("" : "+v"(x))
#define PINA(x) asm volatile("" : "+a"(x))
#define DPPMAX(name, ctl) DI float name(float v) { float r; asm volatile("s_nop 1\n\tv_max_f32_dpp %0, %1, %1 " ctl " row_mask:0xf bank_mask:0xf" : "=v"(r) : "v"(v)); return r; }
DPPMAX(mx_ror8, "row_ror:8") DPPMAX(mx_ror4, "row_ror:4") DPPMAX(mx_ror2, "row_ror:2") DPPMAX(mx_ror1, "row_ror:1")
DPPMAX(mx_x1, "quad_perm:[1,0,3,2]") DPPMAX(mx_x2, "quad_perm:[2,3,0,1]") DPPMAX(mx_hm, "row_half_mirror")
DI float max2(float a, float b) { return __builtin_amdgcn_fmed3f(a, b, __builtin_inff()); }
DI float max3(float a, float b, float c) { float r; asm("v_max3_f32 %0, %1, %2, %3" : "=v"(r) : "v"(a), "v"(b), "v"(c)); return r; }
DI unsigned nopoison(unsigned v) { return v < 0xFFFFFFFEu ? v : 0xFFFFFFFEu; }
DI unsigned pkn(float a, float b) { return nopoison(pk(a, b)); }
DI unsigned umax2(unsigned a, unsigned b) { return a > b ? a : b; }
DI unsigned umax4(u32x4 v) { unsigned a = v[0] > v[1] ? v[0] : v[1], b = v[2] > v[3] ? v[2] : v[3]; return a > b ? a : b; }

DI u32x4 ld16(rsrc_t r, unsigned v, unsigned s) { return __builtin_amdgcn_raw_buffer_load_b128(r, v, s, 16); }
DI u32x2 ld8(rsrc_t r, unsigned v, unsigned s) { return __builtin_amdgcn_raw_buffer_load_b64(r, v, s, 16); }
DI unsigned ld4(rsrc_t r, unsigned v, unsigned s) { return __builtin_amdgcn_raw_buffer_load_b32(r, v, s, 16); }
DI u32x4 ld16p(rsrc_t r, unsigned v, unsigned s) { return __builtin_amdgcn_raw_buffer_load_b128(r, v, s, 0); }
DI void st16(rsrc_t r, unsigned v, unsigned s, u32x4 d) { __builtin_amdgcn_raw_buffer_store_b128(d, r, v, s, 16); asm volatile("s_nop 1" :: "v"(d) : "memory"); }
DI void st8(rsrc_t r, unsigned v, unsigned s, u32x2 d) { __builtin_amdgcn_raw_buffer_store_b64(d, r, v, s, 16); }
DI void st4(rsrc_t r, unsigned v, unsigned s, unsigned d) { __builtin_amdgcn_raw_buffer_store_b32(d, r, v, s, 16); }

DI u32x4 lds16(const char* sm, unsigned o) { return *(const LAS u32x4*)(LAS const char*)(sm + o); }
DI f32x4 lds16f(const char* sm, unsigned o) { return *(const LAS f32x4*)(LAS const char*)(sm + o); }
DI float lds4f(const char* sm, unsigned o) { return *(const LAS float*)(LAS const char*)(sm + o); }
DI void sts16(char* sm, unsigned o, u32x4 v) { *(LAS u32x4*)(LAS char*)(sm + o) = v; }
DI void sts16f(char* sm, unsigned o, f32x4 v) { *(LAS f32x4*)(LAS char*)(sm + o) = v; }
DI void sts8(char* sm, unsigned o, u32x2 v) { *(LAS u32x2*)(LAS char*)(sm + o) = v; }
DI void sts4(char* sm, unsigned o, unsigned v) { *(LAS unsigned*)(LAS char*)(sm + o) = v; }
DI f32x2 lds8f(const char* sm, unsigned o) { return *(const LAS f32x2*)(LAS const char*)(sm + o); }
DI void sts4f(char* sm, unsigned o, float v) { *(LAS float*)(LAS char*)(sm + o) = v; }
DI u32x2 ldtr(const char* sm, unsigned o) { return __builtin_bit_cast(u32x2, __builtin_amdgcn_ds_read_tr16_b64_v4i16((LAS s16x4*)(LAS char*)(const_cast<char*>(sm) + o))); }

template <int N> struct WR;
#define DEFW(N, FULL, LO, HI) template <> struct WR<N> { \
    static DI void pin(u32x8& w) { asm volatile("" : "+{" FULL "}"(w)); } \
    template <int H, int Z> static DI void ma(f32x4& c, const u32x8& w, const u32x4& x) {   \
        if (Z) { if (H) asm volatile("v_mfma_f32_16x16x32_bf16 %0, " HI ", %1, 0" : "=&v"(c) : "v"(x), "{" FULL "}"(w)); \
                 else asm volatile("v_mfma_f32_16x16x32_bf16 %0, " LO ", %1, 0" : "=&v"(c) : "v"(x), "{" FULL "}"(w)); } \
        else { if (H) asm volatile("v_mfma_f32_16x16x32_bf16 %0, " HI ", %1, %0" : "+v"(c) : "v"(x), "{" FULL "}"(w)); \
               else asm volatile("v_mfma_f32_16x16x32_bf16 %0, " LO ", %1, %0" : "+v"(c) : "v"(x), "{" FULL "}"(w)); } } \
    template <int H, int Z> static DI void mb(f32x4& c, const u32x8& w, const u32x4& x) {   \
        if (Z) { if (H) asm volatile("v_mfma_f32_16x16x32_bf16 %0, %1, " HI ", 0" : "=&v"(c) : "v"(x), "{" FULL "}"(w)); \
                 else asm volatile("v_mfma_f32_16x16x32_bf16 %0, %1, " LO ", 0" : "=&v"(c) : "v"(x), "{" FULL "}"(w)); } \
        else { if (H) asm volatile("v_mfma_f32_16x16x32_bf16 %0, %1, " HI ", %0" : "+v"(c) : "v"(x), "{" FULL "}"(w)); \
               else asm volatile("v_mfma_f32_16x16x32_bf16 %0, %1, " LO ", %0" : "+v"(c) : "v"(x), "{" FULL "}"(w)); } } \
    static DI void sp(f32x4& c, const u32x8& w, const u32x4& x, int idx) {   \
        asm volatile("v_smfmac_f32_16x16x64_bf16 %0, %1, " FULL ", %2" : "+v"(c) : "v"(x), "v"(idx), "{" FULL "}"(w)); } \
};
DEFW(0, "a[0:7]", "a[0:3]", "a[4:7]")
DEFW(1, "a[8:15]", "a[8:11]", "a[12:15]")
DEFW(2, "a[16:23]", "a[16:19]", "a[20:23]")
DEFW(3, "a[24:31]", "a[24:27]", "a[28:31]")
DEFW(4, "a[32:39]", "a[32:35]", "a[36:39]")
DEFW(5, "a[40:47]", "a[40:43]", "a[44:47]")
DEFW(6, "a[48:55]", "a[48:51]", "a[52:55]")
DEFW(7, "a[56:63]", "a[56:59]", "a[60:63]")
DEFW(8, "a[64:71]", "a[64:67]", "a[68:71]")
DEFW(9, "a[72:79]", "a[72:75]", "a[76:79]")
DEFW(10, "a[80:87]", "a[80:83]", "a[84:87]")
DEFW(11, "a[88:95]", "a[88:91]", "a[92:95]")
DEFW(12, "a[96:103]", "a[96:99]", "a[100:103]")
DEFW(13, "a[104:111]", "a[104:107]", "a[108:111]")
DEFW(14, "a[112:119]", "a[112:115]", "a[116:119]")
DEFW(15, "a[120:127]", "a[120:123]", "a[124:127]")
DEFW(16, "a[128:135]", "a[128:131]", "a[132:135]")
DEFW(17, "a[136:143]", "a[136:139]", "a[140:143]")
DEFW(18, "a[144:151]", "a[144:147]", "a[148:151]")
DEFW(19, "a[152:159]", "a[152:155]", "a[156:159]")
DEFW(20, "a[160:167]", "a[160:163]", "a[164:167]")
DEFW(21, "a[168:175]", "a[168:171]", "a[172:175]")
DEFW(22, "a[176:183]", "a[176:179]", "a[180:183]")
DEFW(23, "a[184:191]", "a[184:187]", "a[188:191]")
DEFW(24, "a[192:199]", "a[192:195]", "a[196:199]")
DEFW(25, "a[200:207]", "a[200:203]", "a[204:207]")
DEFW(26, "a[208:215]", "a[208:211]", "a[212:215]")
DEFW(27, "a[216:223]", "a[216:219]", "a[220:223]")
DEFW(28, "a[224:231]", "a[224:227]", "a[228:231]")
DEFW(29, "a[232:239]", "a[232:235]", "a[236:239]")
DEFW(30, "a[240:247]", "a[240:243]", "a[244:247]")
DEFW(31, "a[248:255]", "a[248:251]", "a[252:255]")
DEFW(32, "v[120:127]", "v[120:123]", "v[124:127]")
DEFW(33, "v[128:135]", "v[128:131]", "v[132:135]")
DEFW(34, "v[136:143]", "v[136:139]", "v[140:143]")
DEFW(35, "v[144:151]", "v[144:147]", "v[148:151]")
DEFW(36, "v[152:159]", "v[152:155]", "v[156:159]")
DEFW(37, "v[160:167]", "v[160:163]", "v[164:167]")
DEFW(38, "v[168:175]", "v[168:171]", "v[172:175]")
DEFW(39, "v[176:183]", "v[176:179]", "v[180:183]")
DEFW(40, "v[184:191]", "v[184:187]", "v[188:191]")
DEFW(41, "v[192:199]", "v[192:195]", "v[196:199]")
DEFW(42, "v[200:207]", "v[200:203]", "v[204:207]")
DEFW(43, "v[208:215]", "v[208:211]", "v[212:215]")
DEFW(44, "v[216:223]", "v[216:219]", "v[220:223]")
DEFW(45, "v[224:231]", "v[224:227]", "v[228:231]")
DEFW(46, "v[232:239]", "v[232:235]", "v[236:239]")
DEFW(47, "v[240:247]", "v[240:243]", "v[244:247]")
DEFW(48, "v[248:255]", "v[248:251]", "v[252:255]")
template <int I, int E, class F> DI void sfor(F&& f) { if constexpr (I < E) { f(std::integral_constant<int, I>{}); sfor<I + 1, E>(f); } }
#define MFENCE(x) asm volatile("s_nop 7" : "+v"(x))
template <int Z> DI void mfv(f32x4& c, const u32x4& a, const u32x4& b) {
    if (Z) asm volatile("v_mfma_f32_16x16x32_bf16 %0, %1, %2, 0" : "=&v"(c) : "v"(a), "v"(b));
    else asm volatile("v_mfma_f32_16x16x32_bf16 %0, %1, %2, %0" : "+v"(c) : "v"(a), "v"(b));
}
DI void spv(f32x4& c, const u32x4& a, const u32x8& b, int idx) { asm volatile("v_smfmac_f32_16x16x64_bf16 %0, %1, %2, %3" : "+v"(c) : "v"(a), "v"(b), "v"(idx)); }
DI f32x4 zero4() { f32x2 a, b; asm volatile("v_mov_b64 %0, 0\n\tv_mov_b64 %1, 0" : "=v"(a), "=v"(b)); return __builtin_shufflevector(a, b, 0, 1, 2, 3); }
DI int fresh(int v) { asm volatile("" : "+v"(v)); return v; }
#define NOP2 asm volatile("s_nop 1")

#define STAMP(slot) do {} while (0)
#define DBG_ARGS
#define DBG_TXT
#define DBG_OUT
#define DBG_DECL do {} while (0)
#define DBG_PASS
#define DBG_PUT(slot) do {} while (0)

DI int s_nmap(int w, int t, int rho) {
    const int g = rho >> 2, r = rho & 3;
    return t < 6 ? 112 * w + 32 * (t >> 1) + 8 * g + 4 * (t & 1) + r : 112 * w + 96 + 8 * (g & 1) + 4 * (g >> 1) + r;
}

#define PST(k) do {} while (0)
#define LDW(q) __builtin_nontemporal_load((const u32x4*)(q))
template <bool isS> DI void fold(const Params& p, char* sm, rsrc_t rs, int h, int q4, int kh) {
    const int t = threadIdx.x, lane = t & 63, wv = t >> 6, l15 = lane & 15, g = lane >> 4;
    const int ks = 7 * kh + (isS ? wv : (wv + 2 * q4) % 7);
    const int tb = (wv + 4 * kh) % 7;
    const int c32 = lane & 31, r2 = lane >> 5;
    u32x4 bv[8], av[16];
#pragma unroll
    for (int it = 0; it < 8; ++it) {
        if (isS) bv[it] = LDW(p.wq + (size_t)s_nmap(q4, tb, 2 * it + r2) * 4096 + h * 256 + c32 * 8);
        else { const int i = tb * 512 + it * 64 + lane; bv[it] = LDW(p.wo + ((size_t)h * 256 + i / 14) * D + q4 * 112 + (i % 14) * 8); }
    }
    const unsigned short* am = isS ? p.wk : p.wv;
#pragma unroll
    for (int i = 0; i < 16; ++i) {
        const int rho = 2 * (i & 7) + r2;
        const int krow = ((isS ? 112 * q4 : 0) + 32 * ks + 8 * (rho >> 2) + 4 * (i >> 3) + (rho & 3)) % D;
        av[i] = LDW(am + (size_t)krow * 4096 + h * 256 + c32 * 8);
    }
    SB;
#pragma unroll
    for (int it = 0; it < 8; ++it) {
        if (isS) sts16(sm, (tb * 16 + 2 * it + r2) * 512 + ((c32 ^ (2 * it + r2)) * 16), bv[it]);
        else sts16(sm, (tb * 512 + it * 64 + lane) * 16, bv[it]);
    }
    u32x4 af[2][8];
    const unsigned ab = 57344 + wv * 8192;
#pragma unroll
    for (int u = 0; u < 2; ++u) {
#pragma unroll
        for (int i = 0; i < 8; ++i) sts16(sm, ab + (2 * i + r2) * 512 + ((c32 ^ (2 * i + r2)) * 16), av[8 * u + i]);
#pragma unroll
        for (int es = 0; es < 8; ++es) af[u][es] = lds16(sm, ab + l15 * 512 + (((4 * es + g) ^ l15) * 16));
    }
    PST(0);
    bar();
    PST(1);
    auto rdb = [&](int tt, u32x4 (&bf)[8]) {
#pragma unroll
        for (int es = 0; es < 8; ++es) {
            if (isS) bf[es] = lds16(sm, (tt * 16 + l15) * 512 + (((4 * es + g) ^ l15) * 16));
            else {
                const unsigned o = (32 * es + 8 * g + (l15 >> 2)) * 224 + tt * 32 + (l15 & 3) * 8;
                const u32x2 a = ldtr(sm, o), c = ldtr(sm, o + 4 * 224);
                bf[es] = (u32x4){a[0], a[1], c[0], c[1]};
            }
        }
    };
    const float sc = isS ? 0.0625f * 1.4426950408889634f : 1.0f;
    const unsigned ob = (isS ? O_WS + ((h * 4 + q4) * 49 + (ks >> 1) * 7) * 2048 : O_WV + ((q4 * 28 + (h >> 2)) * 28 + (ks >> 1) * 4 + (h & 3)) * 2048) + (ks & 1) * 1024 + lane * 16;
    u32x4 bf[2][8];
    f32x4 acc[2][2];
    rdb(0, bf[0]);
#pragma unroll
    for (int tt = 0; tt < 8; ++tt) {
        if (tt < 6) rdb(tt + 1, bf[(tt + 1) & 1]);
        SB;
        if (tt < 7) {
            acc[tt & 1][0] = (f32x4){0, 0, 0, 0}; acc[tt & 1][1] = (f32x4){0, 0, 0, 0};
#pragma unroll
            for (int es = 0; es < 8; ++es) {
                acc[tt & 1][0] = mfma(af[0][es], bf[tt & 1][es], acc[tt & 1][0]);
                acc[tt & 1][1] = mfma(af[1][es], bf[tt & 1][es], acc[tt & 1][1]);
            }
        }
        SB;
        if (tt) {
            const f32x4 a = acc[(tt - 1) & 1][0] * sc, c = acc[(tt - 1) & 1][1] * sc;
            const u32x4 ov = {pk(a[0], a[1]), pk(a[2], a[3]), pk(c[0], c[1]), pk(c[2], c[3])};
            __builtin_amdgcn_raw_buffer_store_b128(ov, rs, NOST ? OOB : ob, (tt - 1) * (isS ? 2048 : 4 * 28 * 2048), WAUX);
            asm volatile("s_nop 1" :: "v"(ov) : "memory");
        }
    }
    PST(2);
}
DI unsigned probe(rsrc_t rs, unsigned off, int n) {
    unsigned best = ~0u;
    for (int r = 0; r < n; ++r) {
        unsigned long long t0, t1;
        asm volatile("s_memtime %0\n\ts_waitcnt lgkmcnt(0)" : "=s"(t0) :: "memory");
        st4(rs, (threadIdx.x & 63) ? OOB : (r & 1) * 4096u, off, 0u);
        drain();
        asm volatile("s_memtime %0\n\ts_waitcnt lgkmcnt(0)" : "=s"(t1) :: "memory");
        const unsigned d = (unsigned)(t1 - t0);
        best = d < best ? d : best;
    }
    return best;
}
DI unsigned xcc_id() { return (unsigned)__builtin_amdgcn_s_getreg(20 | (3 << 11)) & 7u; }

__global__ void __launch_bounds__(512) prologue_kernel(Params p) {
    extern __shared__ __attribute__((aligned(16))) char sm[];
    const int b = blockIdx.x;
    struct Keep { unsigned x0, x1; __device__ ~Keep() { asm volatile("" :: "v"(x0), "v"(x1)); } } keep{0, 0};
    {
        unsigned long long pc;
        asm volatile("s_getpc_b64 %0" : "=s"(pc));
        const __attribute__((address_space(1))) char* code = (const __attribute__((address_space(1))) char*)(pc & ~63ull);
        keep.x0 = *(const __attribute__((address_space(1))) unsigned*)(code + threadIdx.x * 32u);
        keep.x1 = *(const __attribute__((address_space(1))) unsigned*)(code + PRO_CODE + (unsigned)(b >> 3) * (MAIN_CODE / 32) + (threadIdx.x & 63) * (MAIN_CODE / 2048));
        asm volatile("" ::: "memory");
        SB;
    }
    const rsrc_t rs = __builtin_amdgcn_make_buffer_rsrc(p.ws, 0, WS_USED, 0x00020000);
    if (__builtin_amdgcn_readfirstlane(threadIdx.x) < 448) {
        const int m = (b & 7) * 4 + (b >> 6), part = (b >> 3) & 7;
        if (m & 1) fold<false>(p, sm, rs, m >> 1, part >> 1, part & 1);
        else fold<true>(p, sm, rs, m >> 1, part >> 1, part & 1);
        return;
    }
    const int t = threadIdx.x & 63;
    const unsigned x = xcc_id(), po = O_HOP0 + J_PROBE * 16384 + (((b >> 5) ^ (x >> 2)) & 1) * 8192 + (b & 31) * 128;
    if (t == 0) {
        if (b == 0) { st4(rs, 0, O_CTL, x); st4(rs, 0, O_CTL + 256, 0u); }
        if ((b >> 6) == 1) st4(rs, 0, po, 0u);
    }
    {
        const int s = b >> 2, row = (b & 3) * 8 + (t >> 3), q = t & 7;
        u32x4 v[7]; float f[56]; float sum = 0.f;
#pragma unroll
        for (int i = 0; i < 7; ++i) v[i] = *(const u32x4*)(p.x + (size_t)(s * 32 + row) * D + (q + 8 * i) * 8);
        unsigned mv[2][4];
        const int j0 = (b & 3) * 128 + t;
#pragma unroll
        for (int i = 0; i < 2; ++i) {
            const int j = (j0 + 64 * i) % 448, c = j >> 4, r = j & 15;
            const unsigned* src = (const unsigned*)(p.mem0 + (r & 7) * D + 16 * c) + (r >> 3);
#pragma unroll
            for (int q2 = 0; q2 < 4; ++q2) mv[i][q2] = src[2 * q2];
        }
        SB;
        bar();
        if ((b >> 6) == 1) {
            drain();
            const unsigned d = probe(rs, po, NPROBE);
            if (t == 0) st4(rs, (b & 63) * 4, O_CTL + 512, d);
        }
        const u32x4 P = {POISON, POISON, POISON, POISON};
        for (int k0 = b; k0 < 276; k0 += 252) {
            if (b >= 252) break;
            int k = k0; unsigned base;
            if (k < 8) base = O_HOP0 + (1 + k) * 16384;
            else if ((k -= 8) < 4) base = O_HOP0 + (J_PM + k) * 16384;
            else if ((k -= 4) < 8) base = O_HOP1 + k * 16384;
            else if ((k -= 8) < 32) base = O_HOP1 + (J_STAT + k) * 16384;
            else if ((k -= 32) < 40) base = O_PT + (k / 20) * PTSZ + (k % 20) * 16384;
            else if ((k -= 40) < 28) base = O_X1 + (k / 14) * XSZ + (k % 14) * 16384;
            else if ((k -= 28) < 32) base = (k < 16 ? O_XN : O_XR) + XSZ + (k & 15) * 16384;
            else if ((k -= 32) < 112) base = O_HID + (k / 56) * HIDSZ + (k % 56) * 16384;
            else { k -= 112; base = O_HOP2 + (k < 8 ? 1 + k : J_PM + k - 8) * 16384; }
#pragma unroll
            for (int i = 0; i < 16; ++i) st16(rs, t * 16 + i * 1024, base, P);
        }
        if (b >= 252) {
#pragma unroll
            for (int i = 0; i < 2; ++i) {
                const int j = j0 + 64 * i;
                const u32x4 m4 = j < 448 ? (u32x4){nopoison(mv[i][0]), nopoison(mv[i][1]), nopoison(mv[i][2]), nopoison(mv[i][3])} : P;
                st16(rs, j * 16, O_HOP0, m4); st16(rs, j * 16, O_HOP0 + 8192, m4);
            }
        }
#pragma unroll
        for (int i = 0; i < 7; ++i)
#pragma unroll
            for (int j = 0; j < 4; ++j) { f[i * 8 + 2 * j] = blo(v[i][j]); f[i * 8 + 2 * j + 1] = bhi(v[i][j]); sum += f[i * 8 + 2 * j] + f[i * 8 + 2 * j + 1]; }
        const float mean = oct8add(sum) * (1.0f / D);
        float sq = 0.f;
#pragma unroll
        for (int i = 0; i < 56; ++i) { f[i] -= mean; sq += f[i] * f[i]; }
        const float r2 = __builtin_amdgcn_rsqf(oct8add(sq) * (1.0f / D) + 1e-5f);
#pragma unroll
        for (int i = 0; i < 7; ++i) {
            const unsigned o = s * SEGB + (q + 8 * i) * 512 + row * 16;
            const u32x4 n = {pkn(f[i * 8] * r2, f[i * 8 + 1] * r2), pkn(f[i * 8 + 2] * r2, f[i * 8 + 3] * r2), pkn(f[i * 8 + 4] * r2, f[i * 8 + 5] * r2), pkn(f[i * 8 + 6] * r2, f[i * 8 + 7] * r2)};
            *(u32x4*)(p.ws + O_XN + o) = n;
            *(u32x4*)(p.ws + O_XR + o) = (u32x4){nopoison(v[i][0]), nopoison(v[i][1]), nopoison(v[i][2]), nopoison(v[i][3])};
        }
    }
}

struct Ctx {
    rsrc_t rs;
    char* sm;
    char* ws;
    int lane, w, layer, id;
    unsigned hop0, hop1;
    i32x4 rw;
};

DI void s_asm(u32x16 A1, u32x4 A2, u32x16 M0, u32x16 M1, u32x16 M2, i32x4 rs, unsigned s, unsigned bm, unsigned bt, unsigned bp, unsigned bx, unsigned lx,
              unsigned bxc, unsigned lxc, unsigned pt, unsigned wb, unsigned wv, unsigned wq, unsigned w5, unsigned d2, unsigned dbg) {
    unsigned capv = SPIN_CAP, slowc = 1u << 16, da = 832; unsigned long long live;
    asm volatile(
        "s_mov_b64 %[live], -1\n"
        "s_mov_b32 m0, %[lx]\n"
        "s_nop 0\n"
        "buffer_load_dwordx4 v100, %[rs], %[bx] offen offset:0 sc1 lds\n"
        "buffer_load_dwordx4 v100, %[rs], %[bx] offen offset:1024 sc1 lds\n"
        "buffer_load_dwordx4 v100, %[rs], %[bx] offen offset:2048 sc1 lds\n"
        "buffer_load_dwordx4 v100, %[rs], %[bx] offen offset:3072 sc1 lds\n"
        "s_add_u32 s95, %[lx], 4096\n"
        "s_mov_b32 m0, s95\n"
        "s_add_u32 s95, %[bx], 4096\n"
        "buffer_load_dwordx4 v100, %[rs], s95 offen offset:0 sc1 lds\n"
        "buffer_load_dwordx4 v100, %[rs], s95 offen offset:1024 sc1 lds\n"
        "buffer_load_dwordx4 v100, %[rs], s95 offen offset:2048 sc1 lds\n"
        "s_mov_b32 s60, 1260\n"
        "s_mov_b32 s65, 1\n"
        "s_mov_b32 s94, %[wb]\n"
        "buffer_load_dwordx4 a[0:3], v100, %[rs], s94 offen offset:0\n"
        "buffer_load_dwordx4 a[4:7], v100, %[rs], s94 offen offset:1024\n"
        "buffer_load_dwordx4 a[8:11], v100, %[rs], s94 offen offset:2048\n"
        "buffer_load_dwordx4 a[12:15], v100, %[rs], s94 offen offset:3072\n"
        "s_add_u32 s94, s94, 4096\n"
        "buffer_load_dwordx4 a[16:19], v100, %[rs], s94 offen offset:0\n"
        "buffer_load_dwordx4 a[20:23], v100, %[rs], s94 offen offset:1024\n"
        "buffer_load_dwordx4 a[24:27], v100, %[rs], s94 offen offset:2048\n"
        "buffer_load_dwordx4 a[28:31], v100, %[rs], s94 offen offset:3072\n"
        "s_add_u32 s94, s94, 4096\n"
        "buffer_load_dwordx4 a[32:35], v100, %[rs], s94 offen offset:0\n"
        "buffer_load_dwordx4 a[36:39], v100, %[rs], s94 offen offset:1024\n"
        "buffer_load_dwordx4 a[40:43], v100, %[rs], s94 offen offset:2048\n"
        "buffer_load_dwordx4 a[44:47], v100, %[rs], s94 offen offset:3072\n"
        "s_add_u32 s94, s94, 4096\n"
        "buffer_load_dwordx4 a[48:51], v100, %[rs], s94 offen offset:0\n"
        "buffer_load_dwordx4 a[52:55], v100, %[rs], s94 offen offset:1024\n"
        "v_mov_b64 v[58:59], -1\n"
        "s_add_u32 s95, %[bt], 131072\n"
        "buffer_store_dword v59, v109, %[rs], s95 offen sc1\n"
        "s_add_u32 s95, %[bp], 65536\n"
        "buffer_store_dword v59, v110, %[rs], s95 offen sc1\n"
        "s_add_u32 s95, s95, %[d2]\n"
        "buffer_store_dword v59, v110, %[rs], s95 offen sc1\n"
        "s_add_u32 s95, %[bm], 138240\n"
        "s_mov_b64 exec, s[62:63]\n"
        "buffer_store_dwordx2 v[58:59], v100, %[rs], s95 offen sc1\n"
        "s_mov_b64 exec, -1\n"
        "s_add_u32 s92, %[bxc], 57344\n"
        "s_cmp_lt_u32 %[s], 62\n"
        "s_cselect_b32 s90, s92, %[bxc]\n"
        "s_xor_b32 s92, %[lxc], 32768\n"
        "s_mul_i32 s95, %[wq], 7168\n"
        "s_add_u32 %[lx], s95, s92\n"
        "s_add_u32 %[bx], s95, s90\n"
        "s_mov_b32 m0, %[lx]\n"
        "s_nop 0\n"
        "buffer_load_dwordx4 v100, %[rs], %[bx] offen offset:0 sc1 lds\n"
        "buffer_load_dwordx4 v100, %[rs], %[bx] offen offset:1024 sc1 lds\n"
        "buffer_load_dwordx4 v100, %[rs], %[bx] offen offset:2048 sc1 lds\n"
        "buffer_load_dwordx4 v100, %[rs], %[bx] offen offset:3072 sc1 lds\n"
        "s_add_u32 s95, %[lx], 4096\n"
        "s_mov_b32 m0, s95\n"
        "s_add_u32 s95, %[bx], 4096\n"
        "buffer_load_dwordx4 v100, %[rs], s95 offen offset:0 sc1 lds\n"
        "buffer_load_dwordx4 v100, %[rs], s95 offen offset:1024 sc1 lds\n"
        "buffer_load_dwordx4 v100, %[rs], s95 offen offset:2048 sc1 lds\n"
        "buffer_load_dwordx4 a[56:59], v100, %[rs], s94 offen offset:2048\n"
        "buffer_load_dwordx4 a[60:63], v100, %[rs], s94 offen offset:3072\n"
        "s_add_u32 s94, s94, 4096\n"
        "buffer_load_dwordx4 a[64:67], v100, %[rs], s94 offen offset:0\n"
        "buffer_load_dwordx4 a[68:71], v100, %[rs], s94 offen offset:1024\n"
        "buffer_load_dwordx4 a[72:75], v100, %[rs], s94 offen offset:2048\n"
        "buffer_load_dwordx4 a[76:79], v100, %[rs], s94 offen offset:3072\n"
        "s_add_u32 s94, s94, 4096\n"
        "buffer_load_dwordx4 a[80:83], v100, %[rs], s94 offen offset:0\n"
        "buffer_load_dwordx4 a[84:87], v100, %[rs], s94 offen offset:1024\n"
        "buffer_load_dwordx4 a[88:91], v100, %[rs], s94 offen offset:2048\n"
        "buffer_load_dwordx4 a[92:95], v100, %[rs], s94 offen offset:3072\n"
        "s_add_u32 s94, s94, 4096\n"
        "buffer_load_dwordx4 a[96:99], v100, %[rs], s94 offen offset:0\n"
        "buffer_load_dwordx4 a[100:103], v100, %[rs], s94 offen offset:1024\n"
        "buffer_load_dwordx4 a[104:107], v100, %[rs], s94 offen offset:2048\n"
        "buffer_load_dwordx4 a[108:111], v100, %[rs], s94 offen offset:3072\n"
        "s_add_u32 s94, s94, 4096\n"
        "buffer_load_dwordx4 a[112:115], v100, %[rs], s94 offen offset:0\n"
        "buffer_load_dwordx4 a[116:119], v100, %[rs], s94 offen offset:1024\n"
        "buffer_load_dwordx4 a[120:123], v100, %[rs], s94 offen offset:2048\n"
        "buffer_load_dwordx4 a[124:127], v100, %[rs], s94 offen offset:3072\n"
        "s_add_u32 s94, s94, 4096\n"
        "buffer_load_dwordx4 a[128:131], v100, %[rs], s94 offen offset:0\n"
        "buffer_load_dwordx4 a[132:135], v100, %[rs], s94 offen offset:1024\n"
        "buffer_load_dwordx4 a[136:139], v100, %[rs], s94 offen offset:2048\n"
        "buffer_load_dwordx4 a[140:143], v100, %[rs], s94 offen offset:3072\n"
        "s_add_u32 s94, s94, 4096\n"
        "buffer_load_dwordx4 a[144:147], v100, %[rs], s94 offen offset:0\n"
        "buffer_load_dwordx4 a[148:151], v100, %[rs], s94 offen offset:1024\n"
        "buffer_load_dwordx4 a[152:155], v100, %[rs], s94 offen offset:2048\n"
        "buffer_load_dwordx4 a[156:159], v100, %[rs], s94 offen offset:3072\n"
        "s_add_u32 s94, s94, 4096\n"
        "buffer_load_dwordx4 a[160:163], v100, %[rs], s94 offen offset:0\n"
        "buffer_load_dwordx4 a[164:167], v100, %[rs], s94 offen offset:1024\n"
        "s_waitcnt vmcnt(53)\n"
        "s_barrier\n"
        "v_add_u32 v60, -28672, v99\n"
        "v_mov_b32 v62, 0\n"
        "ds_read_b128 v[28:31], v99 offset:0\n"
        "ds_read_b128 v[116:119], v99 offset:256\n"
        "s_waitcnt vmcnt(39)\n"
        "s_waitcnt lgkmcnt(0)\n"
        "v_mfma_f32_16x16x32_bf16 v[64:67], a[0:3], v[28:31], 0\n"
        "ds_read_b128 v[92:95], v99 offset:2048\n"
        "v_mfma_f32_16x16x32_bf16 v[0:3], a[0:3], v[116:119], 0\n"
        "ds_read_b128 v[56:59], v99 offset:2304\n"
        "v_max3_u32 v62, v28, v29, v62\n"
        "v_mfma_f32_16x16x32_bf16 v[68:71], a[8:11], v[28:31], 0\n"
        "buffer_load_dwordx4 a[168:171], v100, %[rs], s94 offen offset:2048\n"
        "v_max3_u32 v62, v30, v31, v62\n"
        "v_mfma_f32_16x16x32_bf16 v[4:7], a[8:11], v[116:119], 0\n"
        "buffer_load_dwordx4 a[172:175], v100, %[rs], s94 offen offset:3072\n"
        "s_add_u32 s94, s94, 4096\n"
        "v_max3_u32 v62, v116, v117, v62\n"
        "v_mfma_f32_16x16x32_bf16 v[72:75], a[16:19], v[28:31], 0\n"
        "buffer_load_dwordx4 a[176:179], v100, %[rs], s94 offen offset:0\n"
        "v_max3_u32 v62, v118, v119, v62\n"
        "v_mfma_f32_16x16x32_bf16 v[8:11], a[16:19], v[116:119], 0\n"
        "buffer_load_dwordx4 a[180:183], v100, %[rs], s94 offen offset:1024\n"
        "v_mfma_f32_16x16x32_bf16 v[76:79], a[24:27], v[28:31], 0\n"
        "buffer_load_dwordx4 a[184:187], v100, %[rs], s94 offen offset:2048\n"
        "v_mfma_f32_16x16x32_bf16 v[12:15], a[24:27], v[116:119], 0\n"
        "buffer_load_dwordx4 a[188:191], v100, %[rs], s94 offen offset:3072\n"
        "s_add_u32 s94, s94, 4096\n"
        "v_mfma_f32_16x16x32_bf16 v[80:83], a[32:35], v[28:31], 0\n"
        "buffer_load_dwordx4 a[192:195], v100, %[rs], s94 offen offset:0\n"
        "v_mfma_f32_16x16x32_bf16 v[16:19], a[32:35], v[116:119], 0\n"
        "buffer_load_dwordx4 a[196:199], v100, %[rs], s94 offen offset:1024\n"
        "v_mfma_f32_16x16x32_bf16 v[84:87], a[40:43], v[28:31], 0\n"
        "buffer_load_dwordx4 a[200:203], v100, %[rs], s94 offen offset:2048\n"
        "v_mfma_f32_16x16x32_bf16 v[20:23], a[40:43], v[116:119], 0\n"
        "buffer_load_dwordx4 a[204:207], v100, %[rs], s94 offen offset:3072\n"
        "s_add_u32 s94, s94, 4096\n"
        "v_mfma_f32_16x16x32_bf16 v[88:91], a[48:51], v[28:31], 0\n"
        "buffer_load_dwordx4 a[208:211], v100, %[rs], s94 offen offset:0\n"
        "v_mfma_f32_16x16x32_bf16 v[24:27], a[48:51], v[116:119], 0\n"
        "buffer_load_dwordx4 a[212:215], v100, %[rs], s94 offen offset:1024\n"
        "s_waitcnt lgkmcnt(0)\n"
        "v_mfma_f32_16x16x32_bf16 v[64:67], a[4:7], v[92:95], v[64:67]\n"
        "ds_read_b128 v[28:31], v99 offset:4096\n"
        "v_mfma_f32_16x16x32_bf16 v[0:3], a[4:7], v[56:59], v[0:3]\n"
        "ds_read_b128 v[116:119], v99 offset:4352\n"
        "v_max3_u32 v62, v92, v93, v62\n"
        "v_mfma_f32_16x16x32_bf16 v[68:71], a[12:15], v[92:95], v[68:71]\n"
        "buffer_load_dwordx4 a[216:219], v100, %[rs], s94 offen offset:2048\n"
        "v_max3_u32 v62, v94, v95, v62\n"
        "v_mfma_f32_16x16x32_bf16 v[4:7], a[12:15], v[56:59], v[4:7]\n"
        "buffer_load_dwordx4 a[220:223], v100, %[rs], s94 offen offset:3072\n"
        "s_add_u32 s94, s94, 4096\n"
        "v_max3_u32 v62, v56, v57, v62\n"
        "v_mfma_f32_16x16x32_bf16 v[72:75], a[20:23], v[92:95], v[72:75]\n"
        "v_max3_u32 v62, v58, v59, v62\n"
        "v_mfma_f32_16x16x32_bf16 v[8:11], a[20:23], v[56:59], v[8:11]\n"
        "v_mfma_f32_16x16x32_bf16 v[76:79], a[28:31], v[92:95], v[76:79]\n"
        "v_mfma_f32_16x16x32_bf16 v[12:15], a[28:31], v[56:59], v[12:15]\n"
        "v_mfma_f32_16x16x32_bf16 v[80:83], a[36:39], v[92:95], v[80:83]\n"
        "v_mfma_f32_16x16x32_bf16 v[16:19], a[36:39], v[56:59], v[16:19]\n"
        "v_mfma_f32_16x16x32_bf16 v[84:87], a[44:47], v[92:95], v[84:87]\n"
        "v_mfma_f32_16x16x32_bf16 v[20:23], a[44:47], v[56:59], v[20:23]\n"
        "v_mfma_f32_16x16x32_bf16 v[88:91], a[52:55], v[92:95], v[88:91]\n"
        "v_mfma_f32_16x16x32_bf16 v[24:27], a[52:55], v[56:59], v[24:27]\n"
        "s_waitcnt vmcnt(28)\n"
        "s_waitcnt lgkmcnt(0)\n"
        "v_mfma_f32_16x16x32_bf16 v[64:67], a[56:59], v[28:31], v[64:67]\n"
        "buffer_load_dwordx4 a[224:227], v100, %[rs], s94 offen offset:0\n"
        "v_cndmask_b32 v61, v99, v60, s[66:67]\n"
        "v_mfma_f32_16x16x32_bf16 v[0:3], a[56:59], v[116:119], v[0:3]\n"
        "ds_read_b128 v[92:95], v61 offset:6144\n"
        "v_mfma_f32_16x16x32_bf16 v[68:71], a[64:67], v[28:31], v[68:71]\n"
        "ds_read_b128 v[56:59], v61 offset:6400\n"
        "v_max3_u32 v62, v28, v29, v62\n"
        "v_mfma_f32_16x16x32_bf16 v[4:7], a[64:67], v[116:119], v[4:7]\n"
        "buffer_load_dwordx4 a[228:231], v100, %[rs], s94 offen offset:1024\n"
        "v_max3_u32 v62, v30, v31, v62\n"
        "v_mfma_f32_16x16x32_bf16 v[72:75], a[72:75], v[28:31], v[72:75]\n"
        "buffer_load_dwordx4 a[232:235], v100, %[rs], s94 offen offset:2048\n"
        "v_max3_u32 v62, v116, v117, v62\n"
        "v_mfma_f32_16x16x32_bf16 v[8:11], a[72:75], v[116:119], v[8:11]\n"
        "buffer_load_dwordx4 a[236:239], v100, %[rs], s94 offen offset:3072\n"
        "s_add_u32 s94, s94, 4096\n"
        "v_max3_u32 v62, v118, v119, v62\n"
        "v_mfma_f32_16x16x32_bf16 v[76:79], a[80:83], v[28:31], v[76:79]\n"
        "buffer_load_dwordx4 a[240:243], v100, %[rs], s94 offen offset:0\n"
        "v_mfma_f32_16x16x32_bf16 v[12:15], a[80:83], v[116:119], v[12:15]\n"
        "buffer_load_dwordx4 a[244:247], v100, %[rs], s94 offen offset:1024\n"
        "v_mfma_f32_16x16x32_bf16 v[80:83], a[88:91], v[28:31], v[80:83]\n"
        "buffer_load_dwordx4 a[248:251], v100, %[rs], s94 offen offset:2048\n"
        "v_mfma_f32_16x16x32_bf16 v[16:19], a[88:91], v[116:119], v[16:19]\n"
        "buffer_load_dwordx4 a[252:255], v100, %[rs], s94 offen offset:3072\n"
        "s_add_u32 s94, s94, 4096\n"
        "v_mfma_f32_16x16x32_bf16 v[84:87], a[96:99], v[28:31], v[84:87]\n"
        "buffer_load_dwordx4 v[120:123], v100, %[rs], s94 offen offset:0\n"
        "v_mfma_f32_16x16x32_bf16 v[20:23], a[96:99], v[116:119], v[20:23]\n"
        "buffer_load_dwordx4 v[124:127], v100, %[rs], s94 offen offset:1024\n"
        "v_mfma_f32_16x16x32_bf16 v[88:91], a[104:107], v[28:31], v[88:91]\n"
        "buffer_load_dwordx4 v[128:131], v100, %[rs], s94 offen offset:2048\n"
        "v_mfma_f32_16x16x32_bf16 v[24:27], a[104:107], v[116:119], v[24:27]\n"
        "buffer_load_dwordx4 v[132:135], v100, %[rs], s94 offen offset:3072\n"
        "s_add_u32 s94, s94, 4096\n"
        "s_waitcnt lgkmcnt(0)\n"
        "v_mfma_f32_16x16x32_bf16 v[64:67], a[60:63], v[92:95], v[64:67]\n"
        "buffer_load_dwordx4 v[136:139], v100, %[rs], s94 offen offset:0\n"
        "v_cndmask_b32 v61, v99, v60, s[68:69]\n"
        "v_mfma_f32_16x16x32_bf16 v[0:3], a[60:63], v[56:59], v[0:3]\n"
        "ds_read_b128 v[28:31], v61 offset:8192\n"
        "v_mfma_f32_16x16x32_bf16 v[68:71], a[68:71], v[92:95], v[68:71]\n"
        "ds_read_b128 v[116:119], v61 offset:8448\n"
        "v_max3_u32 v62, v92, v93, v62\n"
        "v_mfma_f32_16x16x32_bf16 v[4:7], a[68:71], v[56:59], v[4:7]\n"
        "buffer_load_dwordx4 v[140:143], v100, %[rs], s94 offen offset:1024\n"
        "v_max3_u32 v62, v94, v95, v62\n"
        "v_mfma_f32_16x16x32_bf16 v[72:75], a[76:79], v[92:95], v[72:75]\n"
        "v_max3_u32 v62, v56, v57, v62\n"
        "v_max3_u32 v62, v58, v59, v62\n"
        "v_mfma_f32_16x16x32_bf16 v[8:11], a[76:79], v[56:59], v[8:11]\n"
        "v_mfma_f32_16x16x32_bf16 v[76:79], a[84:87], v[92:95], v[76:79]\n"
        "v_mfma_f32_16x16x32_bf16 v[12:15], a[84:87], v[56:59], v[12:15]\n"
        "v_mfma_f32_16x16x32_bf16 v[80:83], a[92:95], v[92:95], v[80:83]\n"
        "v_mfma_f32_16x16x32_bf16 v[16:19], a[92:95], v[56:59], v[16:19]\n"
        "v_mfma_f32_16x16x32_bf16 v[84:87], a[100:103], v[92:95], v[84:87]\n"
        "v_mfma_f32_16x16x32_bf16 v[20:23], a[100:103], v[56:59], v[20:23]\n"
        "v_mfma_f32_16x16x32_bf16 v[88:91], a[108:111], v[92:95], v[88:91]\n"
        "v_mfma_f32_16x16x32_bf16 v[24:27], a[108:111], v[56:59], v[24:27]\n"
        "s_waitcnt vmcnt(28)\n"
        "s_waitcnt lgkmcnt(0)\n"
        "v_mfma_f32_16x16x32_bf16 v[64:67], a[112:115], v[28:31], v[64:67]\n"
        "buffer_load_dwordx4 v[144:147], v100, %[rs], s94 offen offset:2048\n"
        "v_cndmask_b32 v61, v99, v60, s[70:71]\n"
        "v_mfma_f32_16x16x32_bf16 v[0:3], a[112:115], v[116:119], v[0:3]\n"
        "ds_read_b128 v[92:95], v61 offset:10240\n"
        "v_mfma_f32_16x16x32_bf16 v[68:71], a[120:123], v[28:31], v[68:71]\n"
        "ds_read_b128 v[56:59], v61 offset:10496\n"
        "v_max3_u32 v62, v28, v29, v62\n"
        "v_mfma_f32_16x16x32_bf16 v[4:7], a[120:123], v[116:119], v[4:7]\n"
        "buffer_load_dwordx4 v[148:151], v100, %[rs], s94 offen offset:3072\n"
        "s_add_u32 s94, s94, 4096\n"
        "v_max3_u32 v62, v30, v31, v62\n"
        "v_mfma_f32_16x16x32_bf16 v[72:75], a[128:131], v[28:31], v[72:75]\n"
        "buffer_load_dwordx4 v[152:155], v100, %[rs], s94 offen offset:0\n"
        "v_max3_u32 v62, v116, v117, v62\n"
        "v_mfma_f32_16x16x32_bf16 v[8:11], a[128:131], v[116:119], v[8:11]\n"
        "buffer_load_dwordx4 v[156:159], v100, %[rs], s94 offen offset:1024\n"
        "v_max3_u32 v62, v118, v119, v62\n"
        "v_mfma_f32_16x16x32_bf16 v[76:79], a[136:139], v[28:31], v[76:79]\n"
        "buffer_load_dwordx4 v[160:163], v100, %[rs], s94 offen offset:2048\n"
        "v_mfma_f32_16x16x32_bf16 v[12:15], a[136:139], v[116:119], v[12:15]\n"
        "buffer_load_dwordx4 v[164:167], v100, %[rs], s94 offen offset:3072\n"
        "s_add_u32 s94, s94, 4096\n"
        "v_mfma_f32_16x16x32_bf16 v[80:83], a[144:147], v[28:31], v[80:83]\n"
        "buffer_load_dwordx4 v[168:171], v100, %[rs], s94 offen offset:0\n"
        "v_mfma_f32_16x16x32_bf16 v[16:19], a[144:147], v[116:119], v[16:19]\n"
        "buffer_load_dwordx4 v[172:175], v100, %[rs], s94 offen offset:1024\n"
        "v_mfma_f32_16x16x32_bf16 v[84:87], a[152:155], v[28:31], v[84:87]\n"
        "buffer_load_dwordx4 v[176:179], v100, %[rs], s94 offen offset:2048\n"
        "v_mfma_f32_16x16x32_bf16 v[20:23], a[152:155], v[116:119], v[20:23]\n"
        "buffer_load_dwordx4 v[180:183], v100, %[rs], s94 offen offset:3072\n"
        "s_add_u32 s94, s94, 4096\n"
        "v_mfma_f32_16x16x32_bf16 v[88:91], a[160:163], v[28:31], v[88:91]\n"
        "buffer_load_dwordx4 v[184:187], v100, %[rs], s94 offen offset:0\n"
        "v_mfma_f32_16x16x32_bf16 v[24:27], a[160:163], v[116:119], v[24:27]\n"
        "buffer_load_dwordx4 v[188:191], v100, %[rs], s94 offen offset:1024\n"
        "s_waitcnt lgkmcnt(0)\n"
        "v_mfma_f32_16x16x32_bf16 v[64:67], a[116:119], v[92:95], v[64:67]\n"
        "buffer_load_dwordx4 v[192:195], v100, %[rs], s94 offen offset:2048\n"
        "v_cndmask_b32 v61, v99, v60, s[72:73]\n"
        "v_mfma_f32_16x16x32_bf16 v[0:3], a[116:119], v[56:59], v[0:3]\n"
        "ds_read_b128 v[28:31], v61 offset:12288\n"
        "v_mfma_f32_16x16x32_bf16 v[68:71], a[124:127], v[92:95], v[68:71]\n"
        "ds_read_b128 v[116:119], v61 offset:12544\n"
        "v_max3_u32 v62, v92, v93, v62\n"
        "v_mfma_f32_16x16x32_bf16 v[4:7], a[124:127], v[56:59], v[4:7]\n"
        "buffer_load_dwordx4 v[196:199], v100, %[rs], s94 offen offset:3072\n"
        "s_add_u32 s94, s94, 4096\n"
        "v_max3_u32 v62, v94, v95, v62\n"
        "v_mfma_f32_16x16x32_bf16 v[72:75], a[132:135], v[92:95], v[72:75]\n"
        "v_max3_u32 v62, v56, v57, v62\n"
        "v_max3_u32 v62, v58, v59, v62\n"
        "v_mfma_f32_16x16x32_bf16 v[8:11], a[132:135], v[56:59], v[8:11]\n"
        "v_mfma_f32_16x16x32_bf16 v[76:79], a[140:143], v[92:95], v[76:79]\n"
        "v_mfma_f32_16x16x32_bf16 v[12:15], a[140:143], v[56:59], v[12:15]\n"
        "v_mfma_f32_16x16x32_bf16 v[80:83], a[148:151], v[92:95], v[80:83]\n"
        "v_mfma_f32_16x16x32_bf16 v[16:19], a[148:151], v[56:59], v[16:19]\n"
        "v_mfma_f32_16x16x32_bf16 v[84:87], a[156:159], v[92:95], v[84:87]\n"
        "v_mfma_f32_16x16x32_bf16 v[20:23], a[156:159], v[56:59], v[20:23]\n"
        "v_mfma_f32_16x16x32_bf16 v[88:91], a[164:167], v[92:95], v[88:91]\n"
        "v_mfma_f32_16x16x32_bf16 v[24:27], a[164:167], v[56:59], v[24:27]\n"
        "s_waitcnt vmcnt(28)\n"
        "s_waitcnt lgkmcnt(0)\n"
        "v_mfma_f32_16x16x32_bf16 v[64:67], a[168:171], v[28:31], v[64:67]\n"
        "buffer_load_dwordx4 v[200:203], v100, %[rs], s94 offen offset:0\n"
        "v_cndmask_b32 v61, v99, v60, s[74:75]\n"
        "v_mfma_f32_16x16x32_bf16 v[0:3], a[168:171], v[116:119], v[0:3]\n"
        "ds_read_b128 v[92:95], v61 offset:14336\n"
        "v_mfma_f32_16x16x32_bf16 v[68:71], a[176:179], v[28:31], v[68:71]\n"
        "ds_read_b128 v[56:59], v61 offset:14592\n"
        "v_max3_u32 v62, v28, v29, v62\n"
        "v_mfma_f32_16x16x32_bf16 v[4:7], a[176:179], v[116:119], v[4:7]\n"
        "buffer_load_dwordx4 v[204:207], v100, %[rs], s94 offen offset:1024\n"
        "v_max3_u32 v62, v30, v31, v62\n"
        "v_mfma_f32_16x16x32_bf16 v[72:75], a[184:187], v[28:31], v[72:75]\n"
        "buffer_load_dwordx4 v[208:211], v100, %[rs], s94 offen offset:2048\n"
        "v_max3_u32 v62, v116, v117, v62\n"
        "v_mfma_f32_16x16x32_bf16 v[8:11], a[184:187], v[116:119], v[8:11]\n"
        "buffer_load_dwordx4 v[212:215], v100, %[rs], s94 offen offset:3072\n"
        "s_add_u32 s94, s94, 4096\n"
        "v_max3_u32 v62, v118, v119, v62\n"
        "v_mfma_f32_16x16x32_bf16 v[76:79], a[192:195], v[28:31], v[76:79]\n"
        "buffer_load_dwordx4 v[216:219], v100, %[rs], s94 offen offset:0\n"
        "v_mfma_f32_16x16x32_bf16 v[12:15], a[192:195], v[116:119], v[12:15]\n"
        "buffer_load_dwordx4 v[220:223], v100, %[rs], s94 offen offset:1024\n"
        "v_mfma_f32_16x16x32_bf16 v[80:83], a[200:203], v[28:31], v[80:83]\n"
        "buffer_load_dwordx4 v[224:227], v100, %[rs], s94 offen offset:2048\n"
        "v_mfma_f32_16x16x32_bf16 v[16:19], a[200:203], v[116:119], v[16:19]\n"
        "buffer_load_dwordx4 v[228:231], v100, %[rs], s94 offen offset:3072\n"
        "s_add_u32 s94, s94, 4096\n"
        "v_mfma_f32_16x16x32_bf16 v[84:87], a[208:211], v[28:31], v[84:87]\n"
        "buffer_load_dwordx4 v[232:235], v100, %[rs], s94 offen offset:0\n"
        "v_mfma_f32_16x16x32_bf16 v[20:23], a[208:211], v[116:119], v[20:23]\n"
        "buffer_load_dwordx4 v[236:239], v100, %[rs], s94 offen offset:1024\n"
        "v_mfma_f32_16x16x32_bf16 v[88:91], a[216:219], v[28:31], v[88:91]\n"
        "buffer_load_dwordx4 v[240:243], v100, %[rs], s94 offen offset:2048\n"
        "v_mfma_f32_16x16x32_bf16 v[24:27], a[216:219], v[116:119], v[24:27]\n"
        "buffer_load_dwordx4 v[244:247], v100, %[rs], s94 offen offset:3072\n"
        "s_add_u32 s94, s94, 4096\n"
        "s_waitcnt lgkmcnt(0)\n"
        "v_mfma_f32_16x16x32_bf16 v[64:67], a[172:175], v[92:95], v[64:67]\n"
        "buffer_load_dwordx4 v[248:251], v100, %[rs], s94 offen offset:0\n"
        "v_cndmask_b32 v61, v99, v60, s[76:77]\n"
        "v_mfma_f32_16x16x32_bf16 v[0:3], a[172:175], v[56:59], v[0:3]\n"
        "ds_read_b128 v[28:31], v61 offset:16384\n"
        "v_mfma_f32_16x16x32_bf16 v[68:71], a[180:183], v[92:95], v[68:71]\n"
        "ds_read_b128 v[116:119], v61 offset:16640\n"
        "v_max3_u32 v62, v92, v93, v62\n"
        "v_mfma_f32_16x16x32_bf16 v[4:7], a[180:183], v[56:59], v[4:7]\n"
        "buffer_load_dwordx4 v[252:255], v100, %[rs], s94 offen offset:1024\n"
        "v_max3_u32 v62, v94, v95, v62\n"
        "v_mfma_f32_16x16x32_bf16 v[72:75], a[188:191], v[92:95], v[72:75]\n"
        "v_max3_u32 v62, v56, v57, v62\n"
        "v_max3_u32 v62, v58, v59, v62\n"
        "v_mfma_f32_16x16x32_bf16 v[8:11], a[188:191], v[56:59], v[8:11]\n"
        "v_mfma_f32_16x16x32_bf16 v[76:79], a[196:199], v[92:95], v[76:79]\n"
        "v_mfma_f32_16x16x32_bf16 v[12:15], a[196:199], v[56:59], v[12:15]\n"
        "v_mfma_f32_16x16x32_bf16 v[80:83], a[204:207], v[92:95], v[80:83]\n"
        "v_mfma_f32_16x16x32_bf16 v[16:19], a[204:207], v[56:59], v[16:19]\n"
        "v_mfma_f32_16x16x32_bf16 v[84:87], a[212:215], v[92:95], v[84:87]\n"
        "v_mfma_f32_16x16x32_bf16 v[20:23], a[212:215], v[56:59], v[20:23]\n"
        "v_mfma_f32_16x16x32_bf16 v[88:91], a[220:223], v[92:95], v[88:91]\n"
        "v_mfma_f32_16x16x32_bf16 v[24:27], a[220:223], v[56:59], v[24:27]\n"
        "s_waitcnt vmcnt(28)\n"
        "s_waitcnt lgkmcnt(0)\n"
        "v_mfma_f32_16x16x32_bf16 v[64:67], a[224:227], v[28:31], v[64:67]\n"
        "v_cndmask_b32 v61, v99, v60, s[78:79]\n"
        "v_mfma_f32_16x16x32_bf16 v[0:3], a[224:227], v[116:119], v[0:3]\n"
        "ds_read_b128 v[92:95], v61 offset:18432\n"
        "v_mfma_f32_16x16x32_bf16 v[68:71], a[232:235], v[28:31], v[68:71]\n"
        "ds_read_b128 v[56:59], v61 offset:18688\n"
        "v_max3_u32 v62, v28, v29, v62\n"
        "v_mfma_f32_16x16x32_bf16 v[4:7], a[232:235], v[116:119], v[4:7]\n"
        "v_max3_u32 v62, v30, v31, v62\n"
        "v_max3_u32 v62, v116, v117, v62\n"
        "v_mfma_f32_16x16x32_bf16 v[72:75], a[240:243], v[28:31], v[72:75]\n"
        "v_max3_u32 v62, v118, v119, v62\n"
        "v_mfma_f32_16x16x32_bf16 v[8:11], a[240:243], v[116:119], v[8:11]\n"
        "v_mfma_f32_16x16x32_bf16 v[76:79], a[248:251], v[28:31], v[76:79]\n"
        "v_mfma_f32_16x16x32_bf16 v[12:15], a[248:251], v[116:119], v[12:15]\n"
        "v_mfma_f32_16x16x32_bf16 v[80:83], v[120:123], v[28:31], v[80:83]\n"
        "v_mfma_f32_16x16x32_bf16 v[16:19], v[120:123], v[116:119], v[16:19]\n"
        "v_mfma_f32_16x16x32_bf16 v[84:87], v[128:131], v[28:31], v[84:87]\n"
        "v_mfma_f32_16x16x32_bf16 v[20:23], v[128:131], v[116:119], v[20:23]\n"
        "v_mfma_f32_16x16x32_bf16 v[88:91], v[136:139], v[28:31], v[88:91]\n"
        "v_mfma_f32_16x16x32_bf16 v[24:27], v[136:139], v[116:119], v[24:27]\n"
        "s_waitcnt lgkmcnt(0)\n"
        "v_mfma_f32_16x16x32_bf16 v[64:67], a[228:231], v[92:95], v[64:67]\n"
        "v_cndmask_b32 v61, v99, v60, s[80:81]\n"
        "v_mfma_f32_16x16x32_bf16 v[0:3], a[228:231], v[56:59], v[0:3]\n"
        "ds_read_b128 v[28:31], v61 offset:20480\n"
        "v_mfma_f32_16x16x32_bf16 v[68:71], a[236:239], v[92:95], v[68:71]\n"
        "ds_read_b128 v[116:119], v61 offset:20736\n"
        "v_max3_u32 v62, v92, v93, v62\n"
        "v_mfma_f32_16x16x32_bf16 v[4:7], a[236:239], v[56:59], v[4:7]\n"
        "v_max3_u32 v62, v94, v95, v62\n"
        "v_max3_u32 v62, v56, v57, v62\n"
        "v_mfma_f32_16x16x32_bf16 v[72:75], a[244:247], v[92:95], v[72:75]\n"
        "v_max3_u32 v62, v58, v59, v62\n"
        "v_mfma_f32_16x16x32_bf16 v[8:11], a[244:247], v[56:59], v[8:11]\n"
        "v_mfma_f32_16x16x32_bf16 v[76:79], a[252:255], v[92:95], v[76:79]\n"
        "v_mfma_f32_16x16x32_bf16 v[12:15], a[252:255], v[56:59], v[12:15]\n"
        "v_mfma_f32_16x16x32_bf16 v[80:83], v[124:127], v[92:95], v[80:83]\n"
        "v_mfma_f32_16x16x32_bf16 v[16:19], v[124:127], v[56:59], v[16:19]\n"
        "v_mfma_f32_16x16x32_bf16 v[84:87], v[132:135], v[92:95], v[84:87]\n"
        "v_mfma_f32_16x16x32_bf16 v[20:23], v[132:135], v[56:59], v[20:23]\n"
        "v_mfma_f32_16x16x32_bf16 v[88:91], v[140:143], v[92:95], v[88:91]\n"
        "v_mfma_f32_16x16x32_bf16 v[24:27], v[140:143], v[56:59], v[24:27]\n"
        "s_waitcnt vmcnt(14)\n"
        "s_waitcnt lgkmcnt(0)\n"
        "v_mfma_f32_16x16x32_bf16 v[64:67], v[144:147], v[28:31], v[64:67]\n"
        "v_cndmask_b32 v61, v99, v60, s[82:83]\n"
        "v_mfma_f32_16x16x32_bf16 v[0:3], v[144:147], v[116:119], v[0:3]\n"
        "ds_read_b128 v[92:95], v61 offset:22528\n"
        "v_mfma_f32_16x16x32_bf16 v[68:71], v[152:155], v[28:31], v[68:71]\n"
        "ds_read_b128 v[56:59], v61 offset:22784\n"
        "v_max3_u32 v62, v28, v29, v62\n"
        "v_mfma_f32_16x16x32_bf16 v[4:7], v[152:155], v[116:119], v[4:7]\n"
        "v_max3_u32 v62, v30, v31, v62\n"
        "v_max3_u32 v62, v116, v117, v62\n"
        "v_mfma_f32_16x16x32_bf16 v[72:75], v[160:163], v[28:31], v[72:75]\n"
        "v_max3_u32 v62, v118, v119, v62\n"
        "v_mfma_f32_16x16x32_bf16 v[8:11], v[160:163], v[116:119], v[8:11]\n"
        "v_mfma_f32_16x16x32_bf16 v[76:79], v[168:171], v[28:31], v[76:79]\n"
        "v_mfma_f32_16x16x32_bf16 v[12:15], v[168:171], v[116:119], v[12:15]\n"
        "v_mfma_f32_16x16x32_bf16 v[80:83], v[176:179], v[28:31], v[80:83]\n"
        "v_mfma_f32_16x16x32_bf16 v[16:19], v[176:179], v[116:119], v[16:19]\n"
        "v_mfma_f32_16x16x32_bf16 v[84:87], v[184:187], v[28:31], v[84:87]\n"
        "v_mfma_f32_16x16x32_bf16 v[20:23], v[184:187], v[116:119], v[20:23]\n"
        "v_mfma_f32_16x16x32_bf16 v[88:91], v[192:195], v[28:31], v[88:91]\n"
        "v_mfma_f32_16x16x32_bf16 v[24:27], v[192:195], v[116:119], v[24:27]\n"
        "s_waitcnt lgkmcnt(0)\n"
        "v_mfma_f32_16x16x32_bf16 v[64:67], v[148:151], v[92:95], v[64:67]\n"
        "v_cndmask_b32 v61, v99, v60, s[84:85]\n"
        "v_mfma_f32_16x16x32_bf16 v[0:3], v[148:151], v[56:59], v[0:3]\n"
        "ds_read_b128 v[28:31], v61 offset:24576\n"
        "v_mfma_f32_16x16x32_bf16 v[68:71], v[156:159], v[92:95], v[68:71]\n"
        "ds_read_b128 v[116:119], v61 offset:24832\n"
        "v_max3_u32 v62, v92, v93, v62\n"
        "v_mfma_f32_16x16x32_bf16 v[4:7], v[156:159], v[56:59], v[4:7]\n"
        "v_max3_u32 v62, v94, v95, v62\n"
        "v_max3_u32 v62, v56, v57, v62\n"
        "v_mfma_f32_16x16x32_bf16 v[72:75], v[164:167], v[92:95], v[72:75]\n"
        "v_max3_u32 v62, v58, v59, v62\n"
        "v_mfma_f32_16x16x32_bf16 v[8:11], v[164:167], v[56:59], v[8:11]\n"
        "v_mfma_f32_16x16x32_bf16 v[76:79], v[172:175], v[92:95], v[76:79]\n"
        "v_mfma_f32_16x16x32_bf16 v[12:15], v[172:175], v[56:59], v[12:15]\n"
        "v_mfma_f32_16x16x32_bf16 v[80:83], v[180:183], v[92:95], v[80:83]\n"
        "v_mfma_f32_16x16x32_bf16 v[16:19], v[180:183], v[56:59], v[16:19]\n"
        "v_mfma_f32_16x16x32_bf16 v[84:87], v[188:191], v[92:95], v[84:87]\n"
        "v_mfma_f32_16x16x32_bf16 v[20:23], v[188:191], v[56:59], v[20:23]\n"
        "v_mfma_f32_16x16x32_bf16 v[88:91], v[196:199], v[92:95], v[88:91]\n"
        "v_mfma_f32_16x16x32_bf16 v[24:27], v[196:199], v[56:59], v[24:27]\n"
        "s_waitcnt vmcnt(0)\n"
        "s_waitcnt lgkmcnt(0)\n"
        "v_mfma_f32_16x16x32_bf16 v[64:67], v[200:203], v[28:31], v[64:67]\n"
        "v_cndmask_b32 v61, v99, v60, s[86:87]\n"
        "v_mfma_f32_16x16x32_bf16 v[0:3], v[200:203], v[116:119], v[0:3]\n"
        "ds_read_b128 v[92:95], v61 offset:26624\n"
        "v_mfma_f32_16x16x32_bf16 v[68:71], v[208:211], v[28:31], v[68:71]\n"
        "ds_read_b128 v[56:59], v61 offset:26880\n"
        "v_max3_u32 v62, v28, v29, v62\n"
        "v_mfma_f32_16x16x32_bf16 v[4:7], v[208:211], v[116:119], v[4:7]\n"
        "v_max3_u32 v62, v30, v31, v62\n"
        "v_max3_u32 v62, v116, v117, v62\n"
        "v_mfma_f32_16x16x32_bf16 v[72:75], v[216:219], v[28:31], v[72:75]\n"
        "v_max3_u32 v62, v118, v119, v62\n"
        "v_mfma_f32_16x16x32_bf16 v[8:11], v[216:219], v[116:119], v[8:11]\n"
        "v_mfma_f32_16x16x32_bf16 v[76:79], v[224:227], v[28:31], v[76:79]\n"
        "v_mfma_f32_16x16x32_bf16 v[12:15], v[224:227], v[116:119], v[12:15]\n"
        "v_mfma_f32_16x16x32_bf16 v[80:83], v[232:235], v[28:31], v[80:83]\n"
        "v_mfma_f32_16x16x32_bf16 v[16:19], v[232:235], v[116:119], v[16:19]\n"
        "v_mfma_f32_16x16x32_bf16 v[84:87], v[240:243], v[28:31], v[84:87]\n"
        "v_mfma_f32_16x16x32_bf16 v[20:23], v[240:243], v[116:119], v[20:23]\n"
        "v_mfma_f32_16x16x32_bf16 v[88:91], v[248:251], v[28:31], v[88:91]\n"
        "v_mfma_f32_16x16x32_bf16 v[24:27], v[248:251], v[116:119], v[24:27]\n"
        "s_waitcnt lgkmcnt(0)\n"
        "v_mfma_f32_16x16x32_bf16 v[64:67], v[204:207], v[92:95], v[64:67]\n"
        "v_max3_u32 v62, v92, v93, v62\n"
        "v_max3_u32 v62, v94, v95, v62\n"
        "v_mfma_f32_16x16x32_bf16 v[0:3], v[204:207], v[56:59], v[0:3]\n"
        "v_max3_u32 v62, v56, v57, v62\n"
        "v_max3_u32 v62, v58, v59, v62\n"
        "v_mfma_f32_16x16x32_bf16 v[68:71], v[212:215], v[92:95], v[68:71]\n"
        "v_mfma_f32_16x16x32_bf16 v[4:7], v[212:215], v[56:59], v[4:7]\n"
        "v_mfma_f32_16x16x32_bf16 v[72:75], v[220:223], v[92:95], v[72:75]\n"
        "v_mfma_f32_16x16x32_bf16 v[8:11], v[220:223], v[56:59], v[8:11]\n"
        "v_mfma_f32_16x16x32_bf16 v[76:79], v[228:231], v[92:95], v[76:79]\n"
        "v_mfma_f32_16x16x32_bf16 v[12:15], v[228:231], v[56:59], v[12:15]\n"
        "v_mfma_f32_16x16x32_bf16 v[80:83], v[236:239], v[92:95], v[80:83]\n"
        "v_mfma_f32_16x16x32_bf16 v[16:19], v[236:239], v[56:59], v[16:19]\n"
        "v_mfma_f32_16x16x32_bf16 v[84:87], v[244:247], v[92:95], v[84:87]\n"
        "v_mfma_f32_16x16x32_bf16 v[20:23], v[244:247], v[56:59], v[20:23]\n"
        "v_mfma_f32_16x16x32_bf16 v[88:91], v[252:255], v[92:95], v[88:91]\n"
        "v_mfma_f32_16x16x32_bf16 v[24:27], v[252:255], v[56:59], v[24:27]\n"
        "v_cmp_eq_u32 vcc, -1, v62\n"
        "s_cbranch_vccnz Lslow_%=\n"
        "v_cvt_pk_bf16_f32 v32, v64, v65\n"
        "v_cvt_pk_bf16_f32 v33, v66, v67\n"
        "v_cvt_pk_bf16_f32 v34, v68, v69\n"
        "v_cvt_pk_bf16_f32 v35, v70, v71\n"
        "v_cvt_pk_bf16_f32 v36, v72, v73\n"
        "v_cvt_pk_bf16_f32 v37, v74, v75\n"
        "v_cvt_pk_bf16_f32 v38, v76, v77\n"
        "v_cvt_pk_bf16_f32 v39, v78, v79\n"
        "v_cvt_pk_bf16_f32 v40, v80, v81\n"
        "v_cvt_pk_bf16_f32 v41, v82, v83\n"
        "v_cvt_pk_bf16_f32 v42, v84, v85\n"
        "v_cvt_pk_bf16_f32 v43, v86, v87\n"
        "v_cvt_pk_bf16_f32 v46, v88, v89\n"
        "v_cvt_pk_bf16_f32 v47, v90, v91\n"
        "v_add_u32 v58, -1024, v99\n"
        "v_cndmask_b32 v58, v58, v99, s[48:49]\n"
        "ds_read_b128 v[64:67], v99 offset:0\n"
        "ds_read_b128 v[80:83], v99 offset:256\n"
        "ds_read_b128 v[68:71], v99 offset:2048\n"
        "ds_read_b128 v[84:87], v99 offset:2304\n"
        "ds_read_b128 v[72:75], v99 offset:4096\n"
        "ds_read_b128 v[88:91], v99 offset:4352\n"
        "ds_read_b128 v[76:79], v58 offset:6144\n"
        "ds_read_b128 v[92:95], v58 offset:6400\n"
        "v_cndmask_b32 v44, 0, v46, s[48:49]\n"
        "v_cndmask_b32 v45, 0, v47, s[48:49]\n"
        "v_cndmask_b32 v46, v46, 0, s[48:49]\n"
        "v_cndmask_b32 v47, v47, 0, s[48:49]\n"
        "s_mov_b32 s91, -1\n"
        "s_waitcnt vmcnt(0) lgkmcnt(0)\n"
        "s_branch LB_%=\n"
        "Lplain_%=:\n"
        "v_add_u32 v60, -28672, v99\n"
        "v_mov_b32 v62, 0\n"
        "v_add_u32 v63, %[lxc], v103\n"
        "ds_read_b128 v[116:119], v99 offset:0\n"
        "ds_read_b32 v63, v63\n"
        "s_waitcnt lgkmcnt(1)\n"
        "v_mfma_f32_16x16x32_bf16 v[0:3], a[0:3], v[116:119], 0\n"
        "ds_read_b128 v[28:31], v99 offset:2048\n"
        "v_max3_u32 v62, v116, v117, v62\n"
        "v_mfma_f32_16x16x32_bf16 v[4:7], a[8:11], v[116:119], 0\n"
        "v_max3_u32 v62, v118, v119, v62\n"
        "v_mfma_f32_16x16x32_bf16 v[8:11], a[16:19], v[116:119], 0\n"
        "v_mfma_f32_16x16x32_bf16 v[12:15], a[24:27], v[116:119], 0\n"
        "v_mfma_f32_16x16x32_bf16 v[16:19], a[32:35], v[116:119], 0\n"
        "v_mfma_f32_16x16x32_bf16 v[20:23], a[40:43], v[116:119], 0\n"
        "v_mfma_f32_16x16x32_bf16 v[24:27], a[48:51], v[116:119], 0\n"
        "s_waitcnt lgkmcnt(0)\n"
        "v_mfma_f32_16x16x32_bf16 v[0:3], a[4:7], v[28:31], v[0:3]\n"
        "ds_read_b128 v[116:119], v99 offset:4096\n"
        "v_max3_u32 v62, v28, v29, v62\n"
        "v_mfma_f32_16x16x32_bf16 v[4:7], a[12:15], v[28:31], v[4:7]\n"
        "v_max3_u32 v62, v30, v31, v62\n"
        "v_cndmask_b32 v61, v99, v60, s[66:67]\n"
        "v_mfma_f32_16x16x32_bf16 v[8:11], a[20:23], v[28:31], v[8:11]\n"
        "v_cmp_eq_u32 vcc, -1, v63\n"
        "v_mfma_f32_16x16x32_bf16 v[12:15], a[28:31], v[28:31], v[12:15]\n"
        "s_and_b64 vcc, vcc, %[live]\n"
        "s_cbranch_vccnz Lslow_%=\n"
        "v_mfma_f32_16x16x32_bf16 v[16:19], a[36:39], v[28:31], v[16:19]\n"
        "v_mfma_f32_16x16x32_bf16 v[20:23], a[44:47], v[28:31], v[20:23]\n"
        "v_mfma_f32_16x16x32_bf16 v[24:27], a[52:55], v[28:31], v[24:27]\n"
        "s_waitcnt lgkmcnt(0)\n"
        "v_mfma_f32_16x16x32_bf16 v[0:3], a[56:59], v[116:119], v[0:3]\n"
        "ds_read_b128 v[28:31], v61 offset:6144\n"
        "v_max3_u32 v62, v116, v117, v62\n"
        "v_mfma_f32_16x16x32_bf16 v[4:7], a[64:67], v[116:119], v[4:7]\n"
        "v_max3_u32 v62, v118, v119, v62\n"
        "v_cndmask_b32 v61, v99, v60, s[68:69]\n"
        "v_mfma_f32_16x16x32_bf16 v[8:11], a[72:75], v[116:119], v[8:11]\n"
        "v_mfma_f32_16x16x32_bf16 v[12:15], a[80:83], v[116:119], v[12:15]\n"
        "v_mfma_f32_16x16x32_bf16 v[16:19], a[88:91], v[116:119], v[16:19]\n"
        "v_mfma_f32_16x16x32_bf16 v[20:23], a[96:99], v[116:119], v[20:23]\n"
        "v_mfma_f32_16x16x32_bf16 v[24:27], a[104:107], v[116:119], v[24:27]\n"
        "s_waitcnt lgkmcnt(0)\n"
        "v_mfma_f32_16x16x32_bf16 v[0:3], a[60:63], v[28:31], v[0:3]\n"
        "ds_read_b128 v[116:119], v61 offset:8192\n"
        "v_max3_u32 v62, v28, v29, v62\n"
        "v_mfma_f32_16x16x32_bf16 v[4:7], a[68:71], v[28:31], v[4:7]\n"
        "v_max3_u32 v62, v30, v31, v62\n"
        "v_cndmask_b32 v61, v99, v60, s[70:71]\n"
        "v_mfma_f32_16x16x32_bf16 v[8:11], a[76:79], v[28:31], v[8:11]\n"
        "v_mfma_f32_16x16x32_bf16 v[12:15], a[84:87], v[28:31], v[12:15]\n"
        "v_mfma_f32_16x16x32_bf16 v[16:19], a[92:95], v[28:31], v[16:19]\n"
        "v_mfma_f32_16x16x32_bf16 v[20:23], a[100:103], v[28:31], v[20:23]\n"
        "v_mfma_f32_16x16x32_bf16 v[24:27], a[108:111], v[28:31], v[24:27]\n"
        "s_waitcnt vmcnt(56)\n"
        "s_waitcnt lgkmcnt(0)\n"
        "v_mfma_f32_16x16x32_bf16 v[0:3], a[112:115], v[116:119], v[0:3]\n"
        "ds_read_b128 v[28:31], v61 offset:10240\n"
        "v_max3_u32 v62, v116, v117, v62\n"
        "v_mfma_f32_16x16x32_bf16 v[4:7], a[120:123], v[116:119], v[4:7]\n"
        "v_max3_u32 v62, v118, v119, v62\n"
        "v_cndmask_b32 v61, v99, v60, s[72:73]\n"
        "v_mfma_f32_16x16x32_bf16 v[8:11], a[128:131], v[116:119], v[8:11]\n"
        "v_mfma_f32_16x16x32_bf16 v[12:15], a[136:139], v[116:119], v[12:15]\n"
        "v_mfma_f32_16x16x32_bf16 v[16:19], a[144:147], v[116:119], v[16:19]\n"
        "v_mfma_f32_16x16x32_bf16 v[20:23], a[152:155], v[116:119], v[20:23]\n"
        "v_mfma_f32_16x16x32_bf16 v[24:27], a[160:163], v[116:119], v[24:27]\n"
        "s_waitcnt lgkmcnt(0)\n"
        "v_mfma_f32_16x16x32_bf16 v[0:3], a[116:119], v[28:31], v[0:3]\n"
        "ds_read_b128 v[116:119], v61 offset:12288\n"
        "v_max3_u32 v62, v28, v29, v62\n"
        "v_mfma_f32_16x16x32_bf16 v[4:7], a[124:127], v[28:31], v[4:7]\n"
        "v_max3_u32 v62, v30, v31, v62\n"
        "v_cndmask_b32 v61, v99, v60, s[74:75]\n"
        "v_mfma_f32_16x16x32_bf16 v[8:11], a[132:135], v[28:31], v[8:11]\n"
        "v_mfma_f32_16x16x32_bf16 v[12:15], a[140:143], v[28:31], v[12:15]\n"
        "v_mfma_f32_16x16x32_bf16 v[16:19], a[148:151], v[28:31], v[16:19]\n"
        "v_mfma_f32_16x16x32_bf16 v[20:23], a[156:159], v[28:31], v[20:23]\n"
        "v_mfma_f32_16x16x32_bf16 v[24:27], a[164:167], v[28:31], v[24:27]\n"
        "s_waitcnt vmcnt(42)\n"
        "s_waitcnt lgkmcnt(0)\n"
        "v_mfma_f32_16x16x32_bf16 v[0:3], a[168:171], v[116:119], v[0:3]\n"
        "ds_read_b128 v[28:31], v61 offset:14336\n"
        "v_max3_u32 v62, v116, v117, v62\n"
        "v_mfma_f32_16x16x32_bf16 v[4:7], a[176:179], v[116:119], v[4:7]\n"
        "v_max3_u32 v62, v118, v119, v62\n"
        "v_cndmask_b32 v61, v99, v60, s[76:77]\n"
        "v_mfma_f32_16x16x32_bf16 v[8:11], a[184:187], v[116:119], v[8:11]\n"
        "v_mfma_f32_16x16x32_bf16 v[12:15], a[192:195], v[116:119], v[12:15]\n"
        "v_mfma_f32_16x16x32_bf16 v[16:19], a[200:203], v[116:119], v[16:19]\n"
        "v_mfma_f32_16x16x32_bf16 v[20:23], a[208:211], v[116:119], v[20:23]\n"
        "v_mfma_f32_16x16x32_bf16 v[24:27], a[216:219], v[116:119], v[24:27]\n"
        "s_waitcnt lgkmcnt(0)\n"
        "v_mfma_f32_16x16x32_bf16 v[0:3], a[172:175], v[28:31], v[0:3]\n"
        "ds_read_b128 v[116:119], v61 offset:16384\n"
        "v_max3_u32 v62, v28, v29, v62\n"
        "v_mfma_f32_16x16x32_bf16 v[4:7], a[180:183], v[28:31], v[4:7]\n"
        "v_max3_u32 v62, v30, v31, v62\n"
        "v_cndmask_b32 v61, v99, v60, s[78:79]\n"
        "v_mfma_f32_16x16x32_bf16 v[8:11], a[188:191], v[28:31], v[8:11]\n"
        "v_mfma_f32_16x16x32_bf16 v[12:15], a[196:199], v[28:31], v[12:15]\n"
        "v_mfma_f32_16x16x32_bf16 v[16:19], a[204:207], v[28:31], v[16:19]\n"
        "v_mfma_f32_16x16x32_bf16 v[20:23], a[212:215], v[28:31], v[20:23]\n"
        "v_mfma_f32_16x16x32_bf16 v[24:27], a[220:223], v[28:31], v[24:27]\n"
        "s_waitcnt vmcnt(28)\n"
        "s_waitcnt lgkmcnt(0)\n"
        "v_mfma_f32_16x16x32_bf16 v[0:3], a[224:227], v[116:119], v[0:3]\n"
        "ds_read_b128 v[28:31], v61 offset:18432\n"
        "v_max3_u32 v62, v116, v117, v62\n"
        "v_mfma_f32_16x16x32_bf16 v[4:7], a[232:235], v[116:119], v[4:7]\n"
        "v_max3_u32 v62, v118, v119, v62\n"
        "v_cndmask_b32 v61, v99, v60, s[80:81]\n"
        "v_mfma_f32_16x16x32_bf16 v[8:11], a[240:243], v[116:119], v[8:11]\n"
        "v_mfma_f32_16x16x32_bf16 v[12:15], a[248:251], v[116:119], v[12:15]\n"
        "v_mfma_f32_16x16x32_bf16 v[16:19], v[120:123], v[116:119], v[16:19]\n"
        "v_mfma_f32_16x16x32_bf16 v[20:23], v[128:131], v[116:119], v[20:23]\n"
        "v_mfma_f32_16x16x32_bf16 v[24:27], v[136:139], v[116:119], v[24:27]\n"
        "s_waitcnt lgkmcnt(0)\n"
        "v_mfma_f32_16x16x32_bf16 v[0:3], a[228:231], v[28:31], v[0:3]\n"
        "ds_read_b128 v[116:119], v61 offset:20480\n"
        "v_max3_u32 v62, v28, v29, v62\n"
        "v_mfma_f32_16x16x32_bf16 v[4:7], a[236:239], v[28:31], v[4:7]\n"
        "v_max3_u32 v62, v30, v31, v62\n"
        "v_cndmask_b32 v61, v99, v60, s[82:83]\n"
        "v_mfma_f32_16x16x32_bf16 v[8:11], a[244:247], v[28:31], v[8:11]\n"
        "v_mfma_f32_16x16x32_bf16 v[12:15], a[252:255], v[28:31], v[12:15]\n"
        "v_mfma_f32_16x16x32_bf16 v[16:19], v[124:127], v[28:31], v[16:19]\n"
        "v_mfma_f32_16x16x32_bf16 v[20:23], v[132:135], v[28:31], v[20:23]\n"
        "v_mfma_f32_16x16x32_bf16 v[24:27], v[140:143], v[28:31], v[24:27]\n"
        "s_waitcnt vmcnt(14)\n"
        "s_waitcnt lgkmcnt(0)\n"
        "v_mfma_f32_16x16x32_bf16 v[0:3], v[144:147], v[116:119], v[0:3]\n"
        "ds_read_b128 v[28:31], v61 offset:22528\n"
        "v_max3_u32 v62, v116, v117, v62\n"
        "v_mfma_f32_16x16x32_bf16 v[4:7], v[152:155], v[116:119], v[4:7]\n"
        "v_max3_u32 v62, v118, v119, v62\n"
        "v_cndmask_b32 v61, v99, v60, s[84:85]\n"
        "v_mfma_f32_16x16x32_bf16 v[8:11], v[160:163], v[116:119], v[8:11]\n"
        "v_mfma_f32_16x16x32_bf16 v[12:15], v[168:171], v[116:119], v[12:15]\n"
        "v_mfma_f32_16x16x32_bf16 v[16:19], v[176:179], v[116:119], v[16:19]\n"
        "v_mfma_f32_16x16x32_bf16 v[20:23], v[184:187], v[116:119], v[20:23]\n"
        "v_mfma_f32_16x16x32_bf16 v[24:27], v[192:195], v[116:119], v[24:27]\n"
        "s_waitcnt lgkmcnt(0)\n"
        "v_mfma_f32_16x16x32_bf16 v[0:3], v[148:151], v[28:31], v[0:3]\n"
        "ds_read_b128 v[116:119], v61 offset:24576\n"
        "v_max3_u32 v62, v28, v29, v62\n"
        "v_mfma_f32_16x16x32_bf16 v[4:7], v[156:159], v[28:31], v[4:7]\n"
        "v_max3_u32 v62, v30, v31, v62\n"
        "v_cndmask_b32 v61, v99, v60, s[86:87]\n"
        "v_mfma_f32_16x16x32_bf16 v[8:11], v[164:167], v[28:31], v[8:11]\n"
        "v_mfma_f32_16x16x32_bf16 v[12:15], v[172:175], v[28:31], v[12:15]\n"
        "v_mfma_f32_16x16x32_bf16 v[16:19], v[180:183], v[28:31], v[16:19]\n"
        "v_mfma_f32_16x16x32_bf16 v[20:23], v[188:191], v[28:31], v[20:23]\n"
        "v_mfma_f32_16x16x32_bf16 v[24:27], v[196:199], v[28:31], v[24:27]\n"
        "s_waitcnt vmcnt(0)\n"
        "s_waitcnt lgkmcnt(0)\n"
        "v_mfma_f32_16x16x32_bf16 v[0:3], v[200:203], v[116:119], v[0:3]\n"
        "ds_read_b128 v[28:31], v61 offset:26624\n"
        "v_max3_u32 v62, v116, v117, v62\n"
        "v_mfma_f32_16x16x32_bf16 v[4:7], v[208:211], v[116:119], v[4:7]\n"
        "v_max3_u32 v62, v118, v119, v62\n"
        "v_mfma_f32_16x16x32_bf16 v[8:11], v[216:219], v[116:119], v[8:11]\n"
        "v_mfma_f32_16x16x32_bf16 v[12:15], v[224:227], v[116:119], v[12:15]\n"
        "v_mfma_f32_16x16x32_bf16 v[16:19], v[232:235], v[116:119], v[16:19]\n"
        "v_mfma_f32_16x16x32_bf16 v[20:23], v[240:243], v[116:119], v[20:23]\n"
        "v_mfma_f32_16x16x32_bf16 v[24:27], v[248:251], v[116:119], v[24:27]\n"
        "s_waitcnt lgkmcnt(0)\n"
        "v_mfma_f32_16x16x32_bf16 v[0:3], v[204:207], v[28:31], v[0:3]\n"
        "ds_read_b128 v[116:119], v99 offset:256\n"
        "v_max3_u32 v62, v28, v29, v62\n"
        "v_mfma_f32_16x16x32_bf16 v[4:7], v[212:215], v[28:31], v[4:7]\n"
        "v_max3_u32 v62, v30, v31, v62\n"
        "v_mfma_f32_16x16x32_bf16 v[8:11], v[220:223], v[28:31], v[8:11]\n"
        "v_mfma_f32_16x16x32_bf16 v[12:15], v[228:231], v[28:31], v[12:15]\n"
        "v_mfma_f32_16x16x32_bf16 v[16:19], v[236:239], v[28:31], v[16:19]\n"
        "v_mfma_f32_16x16x32_bf16 v[20:23], v[244:247], v[28:31], v[20:23]\n"
        "v_mfma_f32_16x16x32_bf16 v[24:27], v[252:255], v[28:31], v[24:27]\n"
        "s_waitcnt lgkmcnt(0)\n"
        "v_cvt_pk_bf16_f32 v32, v0, v1\n"
        "v_cvt_pk_bf16_f32 v33, v2, v3\n"
        "v_mfma_f32_16x16x32_bf16 v[0:3], a[0:3], v[116:119], 0\n"
        "ds_read_b128 v[28:31], v99 offset:2304\n"
        "v_cvt_pk_bf16_f32 v34, v4, v5\n"
        "v_cvt_pk_bf16_f32 v35, v6, v7\n"
        "v_mfma_f32_16x16x32_bf16 v[4:7], a[8:11], v[116:119], 0\n"
        "v_cvt_pk_bf16_f32 v36, v8, v9\n"
        "v_cvt_pk_bf16_f32 v37, v10, v11\n"
        "v_mfma_f32_16x16x32_bf16 v[8:11], a[16:19], v[116:119], 0\n"
        "v_cvt_pk_bf16_f32 v38, v12, v13\n"
        "v_cvt_pk_bf16_f32 v39, v14, v15\n"
        "v_mfma_f32_16x16x32_bf16 v[12:15], a[24:27], v[116:119], 0\n"
        "v_cvt_pk_bf16_f32 v40, v16, v17\n"
        "v_cvt_pk_bf16_f32 v41, v18, v19\n"
        "v_mfma_f32_16x16x32_bf16 v[16:19], a[32:35], v[116:119], 0\n"
        "v_cvt_pk_bf16_f32 v42, v20, v21\n"
        "v_cvt_pk_bf16_f32 v43, v22, v23\n"
        "v_mfma_f32_16x16x32_bf16 v[20:23], a[40:43], v[116:119], 0\n"
        "v_cvt_pk_bf16_f32 v46, v24, v25\n"
        "v_cvt_pk_bf16_f32 v47, v26, v27\n"
        "v_mfma_f32_16x16x32_bf16 v[24:27], a[48:51], v[116:119], 0\n"
        "v_max3_u32 v62, v116, v117, v62\n"
        "v_max3_u32 v62, v118, v119, v62\n"
        "s_waitcnt lgkmcnt(0)\n"
        "v_mfma_f32_16x16x32_bf16 v[0:3], a[4:7], v[28:31], v[0:3]\n"
        "ds_read_b128 v[116:119], v99 offset:4352\n"
        "v_max3_u32 v62, v28, v29, v62\n"
        "v_mfma_f32_16x16x32_bf16 v[4:7], a[12:15], v[28:31], v[4:7]\n"
        "v_max3_u32 v62, v30, v31, v62\n"
        "v_cndmask_b32 v61, v99, v60, s[66:67]\n"
        "v_mfma_f32_16x16x32_bf16 v[8:11], a[20:23], v[28:31], v[8:11]\n"
        "v_cndmask_b32 v44, 0, v46, s[48:49]\n"
        "v_cndmask_b32 v45, 0, v47, s[48:49]\n"
        "v_mfma_f32_16x16x32_bf16 v[12:15], a[28:31], v[28:31], v[12:15]\n"
        "v_cndmask_b32 v46, v46, 0, s[48:49]\n"
        "v_cndmask_b32 v47, v47, 0, s[48:49]\n"
        "v_mfma_f32_16x16x32_bf16 v[16:19], a[36:39], v[28:31], v[16:19]\n"
        "v_mov_b64 v[58:59], -1\n"
        "v_mfma_f32_16x16x32_bf16 v[20:23], a[44:47], v[28:31], v[20:23]\n"
        "s_add_u32 s95, %[bt], 131072\n"
        "buffer_store_dword v59, v109, %[rs], s95 offen sc1\n"
        "v_mfma_f32_16x16x32_bf16 v[24:27], a[52:55], v[28:31], v[24:27]\n"
        "s_add_u32 s95, %[bp], 65536\n"
        "buffer_store_dword v59, v110, %[rs], s95 offen sc1\n"
        "s_waitcnt lgkmcnt(0)\n"
        "v_mfma_f32_16x16x32_bf16 v[0:3], a[56:59], v[116:119], v[0:3]\n"
        "ds_read_b128 v[28:31], v61 offset:6400\n"
        "v_max3_u32 v62, v116, v117, v62\n"
        "v_mfma_f32_16x16x32_bf16 v[4:7], a[64:67], v[116:119], v[4:7]\n"
        "s_add_u32 s95, s95, %[d2]\n"
        "buffer_store_dword v59, v110, %[rs], s95 offen sc1\n"
        "v_max3_u32 v62, v118, v119, v62\n"
        "v_mfma_f32_16x16x32_bf16 v[8:11], a[72:75], v[116:119], v[8:11]\n"
        "s_add_u32 s95, %[bm], 138240\n"
        "s_mov_b64 exec, s[62:63]\n"
        "buffer_store_dwordx2 v[58:59], v100, %[rs], s95 offen sc1\n"
        "s_mov_b64 exec, -1\n"
        "v_cndmask_b32 v61, v99, v60, s[68:69]\n"
        "v_mfma_f32_16x16x32_bf16 v[12:15], a[80:83], v[116:119], v[12:15]\n"
        "s_add_u32 s92, %[bxc], 57344\n"
        "s_cmp_lt_u32 %[s], 62\n"
        "s_cselect_b32 s90, s92, %[bxc]\n"
        "s_xor_b32 s92, %[lxc], 32768\n"
        "s_mul_i32 s95, %[wq], 7168\n"
        "s_add_u32 %[lx], s95, s92\n"
        "s_add_u32 %[bx], s95, s90\n"
        "v_mfma_f32_16x16x32_bf16 v[16:19], a[88:91], v[116:119], v[16:19]\n"
        "v_mfma_f32_16x16x32_bf16 v[20:23], a[96:99], v[116:119], v[20:23]\n"
        "v_mfma_f32_16x16x32_bf16 v[24:27], a[104:107], v[116:119], v[24:27]\n"
        "s_waitcnt lgkmcnt(0)\n"
        "v_mfma_f32_16x16x32_bf16 v[0:3], a[60:63], v[28:31], v[0:3]\n"
        "ds_read_b128 v[116:119], v61 offset:8448\n"
        "v_max3_u32 v62, v28, v29, v62\n"
        "v_mfma_f32_16x16x32_bf16 v[4:7], a[68:71], v[28:31], v[4:7]\n"
        "s_mov_b32 m0, %[lx]\n"
        "s_nop 0\n"
        "buffer_load_dwordx4 v100, %[rs], %[bx] offen offset:0 sc1 lds\n"
        "v_max3_u32 v62, v30, v31, v62\n"
        "v_mfma_f32_16x16x32_bf16 v[8:11], a[76:79], v[28:31], v[8:11]\n"
        "v_cndmask_b32 v61, v99, v60, s[70:71]\n"
        "v_mfma_f32_16x16x32_bf16 v[12:15], a[84:87], v[28:31], v[12:15]\n"
        "v_mfma_f32_16x16x32_bf16 v[16:19], a[92:95], v[28:31], v[16:19]\n"
        "buffer_load_dwordx4 v100, %[rs], %[bx] offen offset:1024 sc1 lds\n"
        "v_mfma_f32_16x16x32_bf16 v[20:23], a[100:103], v[28:31], v[20:23]\n"
        "v_mfma_f32_16x16x32_bf16 v[24:27], a[108:111], v[28:31], v[24:27]\n"
        "buffer_load_dwordx4 v100, %[rs], %[bx] offen offset:2048 sc1 lds\n"
        "s_waitcnt lgkmcnt(0)\n"
        "v_mfma_f32_16x16x32_bf16 v[0:3], a[112:115], v[116:119], v[0:3]\n"
        "ds_read_b128 v[28:31], v61 offset:10496\n"
        "v_max3_u32 v62, v116, v117, v62\n"
        "v_mfma_f32_16x16x32_bf16 v[4:7], a[120:123], v[116:119], v[4:7]\n"
        "v_max3_u32 v62, v118, v119, v62\n"
        "v_cndmask_b32 v61, v99, v60, s[72:73]\n"
        "v_mfma_f32_16x16x32_bf16 v[8:11], a[128:131], v[116:119], v[8:11]\n"
        "v_mfma_f32_16x16x32_bf16 v[12:15], a[136:139], v[116:119], v[12:15]\n"
        "buffer_load_dwordx4 v100, %[rs], %[bx] offen offset:3072 sc1 lds\n"
        "v_mfma_f32_16x16x32_bf16 v[16:19], a[144:147], v[116:119], v[16:19]\n"
        "v_mfma_f32_16x16x32_bf16 v[20:23], a[152:155], v[116:119], v[20:23]\n"
        "s_add_u32 s95, %[lx], 4096\n"
        "s_mov_b32 m0, s95\n"
        "s_add_u32 s95, %[bx], 4096\n"
        "buffer_load_dwordx4 v100, %[rs], s95 offen offset:0 sc1 lds\n"
        "v_mfma_f32_16x16x32_bf16 v[24:27], a[160:163], v[116:119], v[24:27]\n"
        "s_waitcnt lgkmcnt(0)\n"
        "v_mfma_f32_16x16x32_bf16 v[0:3], a[116:119], v[28:31], v[0:3]\n"
        "ds_read_b128 v[116:119], v61 offset:12544\n"
        "v_max3_u32 v62, v28, v29, v62\n"
        "v_mfma_f32_16x16x32_bf16 v[4:7], a[124:127], v[28:31], v[4:7]\n"
        "buffer_load_dwordx4 v100, %[rs], s95 offen offset:1024 sc1 lds\n"
        "v_max3_u32 v62, v30, v31, v62\n"
        "v_mfma_f32_16x16x32_bf16 v[8:11], a[132:135], v[28:31], v[8:11]\n"
        "v_cndmask_b32 v61, v99, v60, s[74:75]\n"
        "v_mfma_f32_16x16x32_bf16 v[12:15], a[140:143], v[28:31], v[12:15]\n"
        "v_mfma_f32_16x16x32_bf16 v[16:19], a[148:151], v[28:31], v[16:19]\n"
        "buffer_load_dwordx4 v100, %[rs], s95 offen offset:2048 sc1 lds\n"
        "v_mfma_f32_16x16x32_bf16 v[20:23], a[156:159], v[28:31], v[20:23]\n"
        "v_mfma_f32_16x16x32_bf16 v[24:27], a[164:167], v[28:31], v[24:27]\n"
        "s_waitcnt lgkmcnt(0)\n"
        "v_mfma_f32_16x16x32_bf16 v[0:3], a[168:171], v[116:119], v[0:3]\n"
        "ds_read_b128 v[28:31], v61 offset:14592\n"
        "v_max3_u32 v62, v116, v117, v62\n"
        "v_mfma_f32_16x16x32_bf16 v[4:7], a[176:179], v[116:119], v[4:7]\n"
        "v_max3_u32 v62, v118, v119, v62\n"
        "v_cndmask_b32 v61, v99, v60, s[76:77]\n"
        "v_mfma_f32_16x16x32_bf16 v[8:11], a[184:187], v[116:119], v[8:11]\n"
        "v_mfma_f32_16x16x32_bf16 v[12:15], a[192:195], v[116:119], v[12:15]\n"
        "v_mfma_f32_16x16x32_bf16 v[16:19], a[200:203], v[116:119], v[16:19]\n"
        "v_mfma_f32_16x16x32_bf16 v[20:23], a[208:211], v[116:119], v[20:23]\n"
        "v_mfma_f32_16x16x32_bf16 v[24:27], a[216:219], v[116:119], v[24:27]\n"
        "s_waitcnt lgkmcnt(0)\n"
        "v_mfma_f32_16x16x32_bf16 v[0:3], a[172:175], v[28:31], v[0:3]\n"
        "ds_read_b128 v[116:119], v61 offset:16640\n"
        "v_max3_u32 v62, v28, v29, v62\n"
        "v_mfma_f32_16x16x32_bf16 v[4:7], a[180:183], v[28:31], v[4:7]\n"
        "v_max3_u32 v62, v30, v31, v62\n"
        "v_cndmask_b32 v61, v99, v60, s[78:79]\n"
        "v_mfma_f32_16x16x32_bf16 v[8:11], a[188:191], v[28:31], v[8:11]\n"
        "v_mfma_f32_16x16x32_bf16 v[12:15], a[196:199], v[28:31], v[12:15]\n"
        "v_mfma_f32_16x16x32_bf16 v[16:19], a[204:207], v[28:31], v[16:19]\n"
        "v_mfma_f32_16x16x32_bf16 v[20:23], a[212:215], v[28:31], v[20:23]\n"
        "v_mfma_f32_16x16x32_bf16 v[24:27], a[220:223], v[28:31], v[24:27]\n"
        "s_waitcnt lgkmcnt(0)\n"
        "v_mfma_f32_16x16x32_bf16 v[0:3], a[224:227], v[116:119], v[0:3]\n"
        "ds_read_b128 v[28:31], v61 offset:18688\n"
        "v_max3_u32 v62, v116, v117, v62\n"
        "v_mfma_f32_16x16x32_bf16 v[4:7], a[232:235], v[116:119], v[4:7]\n"
        "v_max3_u32 v62, v118, v119, v62\n"
        "v_cndmask_b32 v61, v99, v60, s[80:81]\n"
        "v_mfma_f32_16x16x32_bf16 v[8:11], a[240:243], v[116:119], v[8:11]\n"
        "v_mfma_f32_16x16x32_bf16 v[12:15], a[248:251], v[116:119], v[12:15]\n"
        "v_mfma_f32_16x16x32_bf16 v[16:19], v[120:123], v[116:119], v[16:19]\n"
        "v_mfma_f32_16x16x32_bf16 v[20:23], v[128:131], v[116:119], v[20:23]\n"
        "v_mfma_f32_16x16x32_bf16 v[24:27], v[136:139], v[116:119], v[24:27]\n"
        "s_waitcnt lgkmcnt(0)\n"
        "v_mfma_f32_16x16x32_bf16 v[0:3], a[228:231], v[28:31], v[0:3]\n"
        "ds_read_b128 v[116:119], v61 offset:20736\n"
        "v_max3_u32 v62, v28, v29, v62\n"
        "v_mfma_f32_16x16x32_bf16 v[4:7], a[236:239], v[28:31], v[4:7]\n"
        "s_sub_u32 s92, %[bm], 9212\n"
        "buffer_load_dword v57, off, %[rs], s92 sc1\n"
        "v_max3_u32 v62, v30, v31, v62\n"
        "v_mfma_f32_16x16x32_bf16 v[8:11], a[244:247], v[28:31], v[8:11]\n"
        "v_cndmask_b32 v61, v99, v60, s[82:83]\n"
        "v_mfma_f32_16x16x32_bf16 v[12:15], a[252:255], v[28:31], v[12:15]\n"
        "v_mfma_f32_16x16x32_bf16 v[16:19], v[124:127], v[28:31], v[16:19]\n"
        "v_mfma_f32_16x16x32_bf16 v[20:23], v[132:135], v[28:31], v[20:23]\n"
        "v_mfma_f32_16x16x32_bf16 v[24:27], v[140:143], v[28:31], v[24:27]\n"
        "s_waitcnt lgkmcnt(0)\n"
        "v_mfma_f32_16x16x32_bf16 v[0:3], v[144:147], v[116:119], v[0:3]\n"
        "ds_read_b128 v[28:31], v61 offset:22784\n"
        "v_max3_u32 v62, v116, v117, v62\n"
        "v_mfma_f32_16x16x32_bf16 v[4:7], v[152:155], v[116:119], v[4:7]\n"
        "v_max3_u32 v62, v118, v119, v62\n"
        "v_cndmask_b32 v61, v99, v60, s[84:85]\n"
        "v_mfma_f32_16x16x32_bf16 v[8:11], v[160:163], v[116:119], v[8:11]\n"
        "v_mfma_f32_16x16x32_bf16 v[12:15], v[168:171], v[116:119], v[12:15]\n"
        "v_mfma_f32_16x16x32_bf16 v[16:19], v[176:179], v[116:119], v[16:19]\n"
        "v_mfma_f32_16x16x32_bf16 v[20:23], v[184:187], v[116:119], v[20:23]\n"
        "v_mfma_f32_16x16x32_bf16 v[24:27], v[192:195], v[116:119], v[24:27]\n"
        "s_waitcnt lgkmcnt(0)\n"
        "v_mfma_f32_16x16x32_bf16 v[0:3], v[148:151], v[28:31], v[0:3]\n"
        "ds_read_b128 v[116:119], v61 offset:24832\n"
        "v_max3_u32 v62, v28, v29, v62\n"
        "v_mfma_f32_16x16x32_bf16 v[4:7], v[156:159], v[28:31], v[4:7]\n"
        "v_max3_u32 v62, v30, v31, v62\n"
        "v_cndmask_b32 v61, v99, v60, s[86:87]\n"
        "v_mfma_f32_16x16x32_bf16 v[8:11], v[164:167], v[28:31], v[8:11]\n"
        "v_add_u32 v58, -1024, v99\n"
        "v_cndmask_b32 v58, v58, v99, s[48:49]\n"
        "v_mfma_f32_16x16x32_bf16 v[12:15], v[172:175], v[28:31], v[12:15]\n"
        "v_mfma_f32_16x16x32_bf16 v[16:19], v[180:183], v[28:31], v[16:19]\n"
        "v_mfma_f32_16x16x32_bf16 v[20:23], v[188:191], v[28:31], v[20:23]\n"
        "v_mfma_f32_16x16x32_bf16 v[24:27], v[196:199], v[28:31], v[24:27]\n"
        "s_waitcnt lgkmcnt(0)\n"
        "v_mfma_f32_16x16x32_bf16 v[0:3], v[200:203], v[116:119], v[0:3]\n"
        "ds_read_b128 v[28:31], v61 offset:26880\n"
        "v_max3_u32 v62, v116, v117, v62\n"
        "v_mfma_f32_16x16x32_bf16 v[4:7], v[208:211], v[116:119], v[4:7]\n"
        "ds_read_b128 v[64:67], v99 offset:0\n"
        "v_max3_u32 v62, v118, v119, v62\n"
        "v_mfma_f32_16x16x32_bf16 v[8:11], v[216:219], v[116:119], v[8:11]\n"
        "ds_read_b128 v[80:83], v99 offset:256\n"
        "v_mfma_f32_16x16x32_bf16 v[12:15], v[224:227], v[116:119], v[12:15]\n"
        "ds_read_b128 v[68:71], v99 offset:2048\n"
        "v_mfma_f32_16x16x32_bf16 v[16:19], v[232:235], v[116:119], v[16:19]\n"
        "ds_read_b128 v[84:87], v99 offset:2304\n"
        "v_mfma_f32_16x16x32_bf16 v[20:23], v[240:243], v[116:119], v[20:23]\n"
        "ds_read_b128 v[72:75], v99 offset:4096\n"
        "v_mfma_f32_16x16x32_bf16 v[24:27], v[248:251], v[116:119], v[24:27]\n"
        "ds_read_b128 v[88:91], v99 offset:4352\n"
        "s_waitcnt lgkmcnt(6)\n"
        "v_mfma_f32_16x16x32_bf16 v[0:3], v[204:207], v[28:31], v[0:3]\n"
        "ds_read_b128 v[76:79], v58 offset:6144\n"
        "v_max3_u32 v62, v28, v29, v62\n"
        "v_mfma_f32_16x16x32_bf16 v[4:7], v[212:215], v[28:31], v[4:7]\n"
        "ds_read_b128 v[92:95], v58 offset:6400\n"
        "v_max3_u32 v62, v30, v31, v62\n"
        "v_mfma_f32_16x16x32_bf16 v[8:11], v[220:223], v[28:31], v[8:11]\n"
        "v_mfma_f32_16x16x32_bf16 v[12:15], v[228:231], v[28:31], v[12:15]\n"
        "v_mfma_f32_16x16x32_bf16 v[16:19], v[236:239], v[28:31], v[16:19]\n"
        "v_mfma_f32_16x16x32_bf16 v[20:23], v[244:247], v[28:31], v[20:23]\n"
        "v_mfma_f32_16x16x32_bf16 v[24:27], v[252:255], v[28:31], v[24:27]\n"
        "v_cmp_eq_u32 vcc, -1, v62\n"
        "s_waitcnt vmcnt(0)\n"
        "v_readfirstlane_b32 s91, v57\n"
        "s_and_b64 vcc, vcc, %[live]\n"
        "s_cbranch_vccnz Lslow_%=\n"
        "LB_%=:\n"
        "s_waitcnt lgkmcnt(6)\n"
        "v_mfma_f32_16x16x32_bf16 v[116:119], v[32:35], v[80:83], 0\n"
        "v_cvt_pk_bf16_f32 v48, v0, v1\n"
        "v_cvt_pk_bf16_f32 v49, v2, v3\n"
        "v_mfma_f32_16x16x32_bf16 v[28:31], v[32:35], v[64:67], 0\n"
        "v_cvt_pk_bf16_f32 v50, v4, v5\n"
        "v_cvt_pk_bf16_f32 v51, v6, v7\n"
        "s_waitcnt lgkmcnt(4)\n"
        "v_mfma_f32_16x16x32_bf16 v[116:119], v[36:39], v[84:87], v[116:119]\n"
        "v_cvt_pk_bf16_f32 v52, v8, v9\n"
        "v_cvt_pk_bf16_f32 v53, v10, v11\n"
        "v_mfma_f32_16x16x32_bf16 v[28:31], v[36:39], v[68:71], v[28:31]\n"
        "v_cvt_pk_bf16_f32 v54, v12, v13\n"
        "v_cvt_pk_bf16_f32 v55, v14, v15\n"
        "s_waitcnt lgkmcnt(2)\n"
        "v_mfma_f32_16x16x32_bf16 v[116:119], v[40:43], v[88:91], v[116:119]\n"
        "v_cvt_pk_bf16_f32 v56, v16, v17\n"
        "v_cvt_pk_bf16_f32 v57, v18, v19\n"
        "v_mfma_f32_16x16x32_bf16 v[28:31], v[40:43], v[72:75], v[28:31]\n"
        "v_cvt_pk_bf16_f32 v58, v20, v21\n"
        "v_cvt_pk_bf16_f32 v59, v22, v23\n"
        "s_waitcnt lgkmcnt(0)\n"
        "v_mfma_f32_16x16x32_bf16 v[116:119], v[44:47], v[92:95], v[116:119]\n"
        "v_cvt_pk_bf16_f32 v62, v24, v25\n"
        "v_cvt_pk_bf16_f32 v63, v26, v27\n"
        "v_mfma_f32_16x16x32_bf16 v[28:31], v[44:47], v[76:79], v[28:31]\n"
        "v_cndmask_b32 v60, 0, v62, s[48:49]\n"
        "v_cndmask_b32 v61, 0, v63, s[48:49]\n"
        "v_cndmask_b32 v62, v62, 0, s[48:49]\n"
        "v_cndmask_b32 v63, v63, 0, s[48:49]\n"
        "v_mfma_f32_16x16x32_bf16 v[76:79], v[48:51], v[80:83], 0\n"
        "v_add_u32 v72, %[w5], v100\n"
        "v_mov_b64 v[0:1], 0\n"
        "v_mfma_f32_16x16x32_bf16 v[76:79], v[52:55], v[84:87], v[76:79]\n"
        "v_mov_b64 v[2:3], 0\n"
        "v_mov_b64 v[4:5], 0\n"
        "v_mfma_f32_16x16x32_bf16 v[76:79], v[56:59], v[88:91], v[76:79]\n"
        "ds_write_b128 v72, v[116:119] offset:9216\n"
        "v_mov_b64 v[6:7], 0\n"
        "v_mfma_f32_16x16x32_bf16 v[76:79], v[60:63], v[92:95], v[76:79]\n"
        "ds_write_b128 v72, v[28:31] offset:8192\n"
        "v_mov_b64 v[8:9], 0\n"
        "v_mov_b64 v[10:11], 0\n"
        "v_mov_b64 v[12:13], 0\n"
        "v_mov_b64 v[14:15], 0\n"
        "v_mov_b64 v[16:17], 0\n"
        "s_min_u32 s92, %[wv], 2\n"
        "s_lshl_b32 s92, s92, 10\n"
        "v_add_u32 v73, s92, v100\n"
        "s_mov_b32 %[bxc], s90\n"
        "s_xor_b32 %[lxc], %[lxc], 32768\n"
        "v_add_u32 v85, %[lxc], v103\n"
        "ds_write_b128 v72, v[76:79] offset:10240\n"
        "s_memrealtime s[96:97]\n"
        "s_cmp_eq_u32 %[s], 0\n"
        "s_cselect_b32 s91, 0, s91\n"
        "v_mov_b64 v[18:19], 0\n"
        "v_mov_b64 v[20:21], 0\n"
        "v_mov_b64 v[22:23], 0\n"
        "v_mov_b64 v[24:25], 0\n"
        "v_mov_b64 v[26:27], 0\n"
        "s_waitcnt lgkmcnt(0)\n"
        "s_barrier\n"
        "s_lshl_b32 s96, s96, 4\n"
        "s_sub_u32 s96, s91, s96\n"
        "s_sub_u32 s96, s96, 64\n"
        "s_cmp_eq_u32 s91, -1\n"
        "s_cselect_b32 s97, 999, s96\n"
        "s_cmp_gt_i32 s97, 48\n"
        "s_cbranch_scc1 Lbw0_%=\n"
        "Lbw1_%=:\n"
        "buffer_load_dwordx4 v[64:67], v97, %[rs], %[bm] offen sc1\n"
        "buffer_load_dwordx4 v[68:71], v98, %[rs], %[bm] offen sc1\n"
        "s_memtime s[98:99]\n"
        "ds_read_b128 v[76:79], v73 offset:8192\n"
        "ds_read_b128 v[80:83], v73 offset:13312\n"
        "ds_read_b128 v[116:119], v73 offset:18432\n"
        "ds_read_b128 v[88:91], v73 offset:23552\n"
        "ds_read_b32 v85, v85\n"
        "v_mov_b32 v72, 0xf149f2ca\n"
        "v_mov_b64 v[28:29], 0\n"
        "v_mov_b64 v[30:31], 0\n"
        "v_add_u32 v92, 5120, v97\n"
        "v_add_u32 v93, -2048, v97\n"
        "v_min_u32 v74, v92, v93\n"
        "v_add_u32 v92, 6144, v97\n"
        "v_add_u32 v93, -1024, v97\n"
        "v_min_u32 v75, v92, v93\n"
        "v_add_u32 v92, 44032, v108\n"
        "v_mov_b32 v93, 55552\n"
        "v_cndmask_b32 v84, v93, v92, s[48:49]\n"
        "v_cndmask_b32 v86, v92, v93, s[48:49]\n"
        "v_mov_b64 v[92:93], 0\n"
        "v_mov_b64 v[94:95], 0\n"
        "s_waitcnt lgkmcnt(0)\n"
        "v_pk_add_f32 v[76:77], v[76:77], v[80:81]\n"
        "v_pk_add_f32 v[78:79], v[78:79], v[82:83]\n"
        "v_pk_add_f32 v[116:117], v[116:117], v[88:89]\n"
        "v_pk_add_f32 v[118:119], v[118:119], v[90:91]\n"
        "v_cmp_eq_u32 vcc, -1, v85\n"
        "v_pk_add_f32 v[76:77], v[76:77], v[116:117]\n"
        "v_pk_add_f32 v[78:79], v[78:79], v[118:119]\n"
        "v_cndmask_b32 v76, v72, v76, s[50:51]\n"
        "v_cndmask_b32 v77, v72, v77, s[52:53]\n"
        "v_cndmask_b32 v78, v72, v78, s[54:55]\n"
        "v_cndmask_b32 v79, v72, v79, s[56:57]\n"
        "ds_write_b128 v73, v[76:79] offset:57344\n"
        "v_mov_b64 v[88:89], 0\n"
        "v_mov_b64 v[90:91], 0\n"
        "s_and_b64 vcc, vcc, %[live]\n"
        "s_cselect_b32 s90, 99, 0\n"
        "s_bitcmp1_b32 s91, 0\n"
        "s_cselect_b32 s92, 48, -4\n"
        "s_cmp_lg_u32 s65, 0\n"
        "s_cselect_b32 s92, 0, s92\n"
        "s_add_u32 %[da], %[da], s92\n"
        "s_min_u32 %[da], %[da], 1088\n"
        "s_max_u32 %[da], %[da], 640\n"
        "s_sub_u32 s92, s91, s96\n"
        "s_add_u32 s92, s92, 80\n"
        "s_cmp_lt_u32 s96, 145\n"
        "s_cselect_b32 s91, s91, s92\n"
        "s_cmp_lt_u32 s96, 80\n"
        "s_cselect_b32 s91, s92, s91\n"
        "s_add_u32 s94, %[bm], 7168\n"
        "s_mov_b32 s64, s98\n"
        "s_add_u32 s91, s91, s60\n"
        "s_and_b32 s91, s91, -4\n"
        "s_cmp_lt_i32 s96, -48\n"
        "s_cselect_b32 s65, 1, 0\n"
        "s_cselect_b32 s99, 256, 0\n"
        "s_mov_b32 s93, -1\n"
        "s_and_b32 s97, %[da], -4\n"
        "Lpc_%=:\n"
        "s_waitcnt vmcnt(1)\n"
        "v_smfmac_f32_16x16x64_bf16 v[88:91], v[64:67], v[32:39], v96\n"
        "ds_write_b128 v97, v[64:67] offset:0\n"
        "v_smfmac_f32_16x16x64_bf16 v[92:95], v[64:67], v[48:55], v96\n"
        "v_max3_u32 v72, v64, v65, v66\n"
        "s_waitcnt vmcnt(0)\n"
        "v_smfmac_f32_16x16x64_bf16 v[88:91], v[68:71], v[40:47], v96\n"
        "ds_write_b128 v98, v[68:71] offset:0\n"
        "v_smfmac_f32_16x16x64_bf16 v[92:95], v[68:71], v[56:63], v96\n"
        "v_max3_u32 v73, v68, v69, v70\n"
        "v_max3_u32 v72, v72, v67, v71\n"
        "v_smfmac_f32_16x16x64_bf16 v[0:3], v[64:67], a[48:55], v96\n"
        "v_max_u32 v72, v72, v73\n"
        "v_cmp_eq_u32 vcc, -1, v72\n"
        "v_smfmac_f32_16x16x64_bf16 v[4:7], v[64:67], a[32:39], v96\n"
        "s_cbranch_vccnz Lretry_%=\n"
        "Lgo_%=:\n"
        "s_waitcnt lgkmcnt(0)\n"
        "s_barrier\n"
        "s_memtime s[88:89]\n"
        "ds_read_b128 v[32:35], v112 offset:0\n"
        "ds_read_b128 v[36:39], v113 offset:0\n"
        "v_smfmac_f32_16x16x64_bf16 v[8:11], v[64:67], a[40:47], v96\n"
        "ds_read_b128 v[40:43], v114 offset:0\n"
        "v_smfmac_f32_16x16x64_bf16 v[12:15], v[64:67], a[0:7], v96\n"
        "ds_read_b128 v[44:47], v115 offset:0\n"
        "v_smfmac_f32_16x16x64_bf16 v[16:19], v[64:67], a[8:15], v96\n"
        "ds_read_b128 v[48:51], v74 offset:0\n"
        "v_smfmac_f32_16x16x64_bf16 v[20:23], v[64:67], a[16:23], v96\n"
        "ds_read_b128 v[52:55], v75 offset:0\n"
        "v_smfmac_f32_16x16x64_bf16 v[24:27], v[64:67], a[24:31], v96\n"
        "v_mov_b32 v62, -2\n"
        "s_waitcnt lgkmcnt(5)\n"
        "v_smfmac_f32_16x16x64_bf16 v[0:3], v[32:35], a[104:111], v96\n"
        "s_nop 0\n"
        "ds_write2_b32 v105, v88, v92 offset0:0 offset1:16\n"
        "s_waitcnt lgkmcnt(5)\n"
        "v_smfmac_f32_16x16x64_bf16 v[0:3], v[36:39], a[160:167], v96\n"
        "s_waitcnt lgkmcnt(4)\n"
        "v_smfmac_f32_16x16x64_bf16 v[0:3], v[40:43], a[216:223], v96\n"
        "ds_write2_b32 v105, v89, v93 offset0:32 offset1:48\n"
        "s_waitcnt lgkmcnt(4)\n"
        "v_smfmac_f32_16x16x64_bf16 v[0:3], v[44:47], v[136:143], v96\n"
        "s_waitcnt lgkmcnt(3)\n"
        "v_smfmac_f32_16x16x64_bf16 v[0:3], v[48:51], v[192:199], v96\n"
        "s_sub_u32 s88, s88, s64\n"
        "s_cmp_gt_u32 s88, 1050\n"
        "s_cselect_b32 s65, 3, s65\n"
        "ds_write2_b32 v105, v90, v94 offset0:64 offset1:80\n"
        "s_waitcnt lgkmcnt(3)\n"
        "v_smfmac_f32_16x16x64_bf16 v[0:3], v[52:55], v[248:255], v96\n"
        "s_cselect_b32 s99, 256, s99\n"
        "s_mul_i32 s88, s88, 49\n"
        "s_lshr_b32 s88, s88, 8\n"
        "v_smfmac_f32_16x16x64_bf16 v[4:7], v[32:35], a[88:95], v96\n"
        "s_lshl2_add_u32 s61, s88, s91\n"
        "s_or_b32 s61, s61, s65\n"
        "ds_write2_b32 v105, v91, v95 offset0:96 offset1:112\n"
        "v_min_u32 v60, s61, v62\n"
        "v_smfmac_f32_16x16x64_bf16 v[4:7], v[36:39], a[144:151], v96\n"
        "s_add_u32 s88, s99, s97\n"
        "s_add_u32 s88, s88, s61\n"
        "v_min_u32 v61, s88, v62\n"
        "v_smfmac_f32_16x16x64_bf16 v[4:7], v[40:43], a[200:207], v96\n"
        "s_mov_b64 exec, s[62:63]\n"
        "buffer_store_dwordx2 v[60:61], v100, %[rs], s94 offen sc1\n"
        "s_mov_b64 exec, -1\n"
        "v_cvt_pk_bf16_f32 v0, v0, v1\n"
        "v_smfmac_f32_16x16x64_bf16 v[4:7], v[44:47], v[120:127], v96\n"
        "v_cvt_pk_bf16_f32 v1, v2, v3\n"
        "v_smfmac_f32_16x16x64_bf16 v[4:7], v[48:51], v[176:183], v96\n"
        "s_waitcnt lgkmcnt(0)\n"
        "s_barrier\n"
        "ds_read2st64_b32 v[88:89], v106 offset0:112 offset1:116\n"
        "ds_read2st64_b32 v[90:91], v106 offset0:120 offset1:124\n"
        "v_smfmac_f32_16x16x64_bf16 v[4:7], v[52:55], v[232:239], v96\n"
        "s_nop 0\n"
        "ds_read2st64_b32 v[92:93], v106 offset0:128 offset1:132\n"
        "v_smfmac_f32_16x16x64_bf16 v[8:11], v[32:35], a[96:103], v96\n"
        "s_nop 0\n"
        "ds_read2st64_b32 v[94:95], v106 offset0:136 offset1:140\n"
        "v_smfmac_f32_16x16x64_bf16 v[8:11], v[36:39], a[152:159], v96\n"
        "ds_write_b64 v107, v[0:1] offset:44032\n"
        "v_smfmac_f32_16x16x64_bf16 v[8:11], v[40:43], a[208:215], v96\n"
        "ds_read_b64_tr_b16 v[84:85], v84\n"
        "v_smfmac_f32_16x16x64_bf16 v[8:11], v[44:47], v[128:135], v96\n"
        "ds_read_b64_tr_b16 v[86:87], v86\n"
        "v_cvt_pk_bf16_f32 v4, v4, v5\n"
        "v_smfmac_f32_16x16x64_bf16 v[8:11], v[48:51], v[184:191], v96\n"
        "s_waitcnt lgkmcnt(5)\n"
        "v_cvt_pk_bf16_f32 v5, v6, v7\n"
        "v_smfmac_f32_16x16x64_bf16 v[8:11], v[52:55], v[240:247], v96\n"
        "ds_write_b64 v107, v[4:5] offset:43008\n"
        "v_add_f32 v88, v88, v89\n"
        "v_add_f32 v90, v90, v91\n"
        "v_smfmac_f32_16x16x64_bf16 v[12:15], v[32:35], a[56:63], v96\n"
        "s_nop 0\n"
        "ds_read_b64_tr_b16 v[80:81], v108 offset:43008\n"
        "v_smfmac_f32_16x16x64_bf16 v[12:15], v[36:39], a[112:119], v96\n"
        "s_waitcnt lgkmcnt(5)\n"
        "v_cvt_pk_bf16_f32 v8, v8, v9\n"
        "v_smfmac_f32_16x16x64_bf16 v[12:15], v[40:43], a[168:175], v96\n"
        "v_cvt_pk_bf16_f32 v9, v10, v11\n"
        "v_smfmac_f32_16x16x64_bf16 v[12:15], v[44:47], a[224:231], v96\n"
        "ds_write_b64 v107, v[8:9] offset:43520\n"
        "v_add_f32 v92, v92, v93\n"
        "v_add_f32 v94, v94, v95\n"
        "v_smfmac_f32_16x16x64_bf16 v[12:15], v[48:51], v[144:151], v96\n"
        "ds_read_b64_tr_b16 v[82:83], v108 offset:43520\n"
        "v_add_f32 v88, v88, v90\n"
        "v_add_f32 v92, v92, v94\n"
        "v_smfmac_f32_16x16x64_bf16 v[12:15], v[52:55], v[200:207], v96\n"
        "s_nop 1\n"
        "v_add_f32 v56, v88, v92\n"
        "v_smfmac_f32_16x16x64_bf16 v[16:19], v[32:35], a[64:71], v96\n"
        "s_nop 0\n"
        "v_max_f32_dpp v58, v56, v56 row_ror:8 row_mask:0xf bank_mask:0xf\n"
        "v_smfmac_f32_16x16x64_bf16 v[16:19], v[36:39], a[120:127], v96\n"
        "s_nop 0\n"
        "v_cvt_pk_bf16_f32 v12, v12, v13\n"
        "v_smfmac_f32_16x16x64_bf16 v[16:19], v[40:43], a[176:183], v96\n"
        "v_cvt_pk_bf16_f32 v13, v14, v15\n"
        "v_smfmac_f32_16x16x64_bf16 v[16:19], v[44:47], a[232:239], v96\n"
        "ds_write_b64 v107, v[12:13] offset:40960\n"
        "v_max_f32_dpp v58, v58, v58 row_ror:4 row_mask:0xf bank_mask:0xf\n"
        "v_smfmac_f32_16x16x64_bf16 v[16:19], v[48:51], v[152:159], v96\n"
        "ds_read_b64_tr_b16 v[72:73], v108 offset:40960\n"
        "v_max_f32_dpp v58, v58, v58 row_ror:2 row_mask:0xf bank_mask:0xf\n"
        "v_smfmac_f32_16x16x64_bf16 v[16:19], v[52:55], v[208:215], v96\n"
        "s_nop 0\n"
        "v_max_f32_dpp v58, v58, v58 row_ror:1 row_mask:0xf bank_mask:0xf\n"
        "v_mov_b32 v57, v58\n"
        "v_smfmac_f32_16x16x64_bf16 v[20:23], v[32:35], a[72:79], v96\n"
        "s_nop 0\n"
        "v_permlane16_swap_b32 v58, v57\n"
        "v_smfmac_f32_16x16x64_bf16 v[20:23], v[36:39], a[128:135], v96\n"
        "s_nop 0\n"
        "v_cvt_pk_bf16_f32 v16, v16, v17\n"
        "v_smfmac_f32_16x16x64_bf16 v[20:23], v[40:43], a[184:191], v96\n"
        "v_cvt_pk_bf16_f32 v17, v18, v19\n"
        "v_smfmac_f32_16x16x64_bf16 v[20:23], v[44:47], a[240:247], v96\n"
        "ds_write_b64 v107, v[16:17] offset:41472\n"
        "v_max_f32 v58, v58, v57\n"
        "v_sub_f32 v56, v56, v58\n"
        "v_smfmac_f32_16x16x64_bf16 v[20:23], v[48:51], v[160:167], v96\n"
        "ds_read_b64_tr_b16 v[74:75], v108 offset:41472\n"
        "v_exp_f32 v57, v56\n"
        "v_smfmac_f32_16x16x64_bf16 v[20:23], v[52:55], v[216:223], v96\n"
        "s_nop 0\n"
        "v_mov_b32_dpp v56, v57 quad_perm:[1,0,3,2] row_mask:0xf bank_mask:0xf\n"
        "v_smfmac_f32_16x16x64_bf16 v[24:27], v[32:35], a[80:87], v96\n"
        "v_cvt_pk_bf16_f32 v56, v57, v56\n"
        "v_min_u32 v56, -2, v56\n"
        "v_smfmac_f32_16x16x64_bf16 v[24:27], v[36:39], a[136:143], v96\n"
        "buffer_store_dword v56, v109, %[rs], %[bt] offen sc1\n"
        "v_add_f32_dpp v59, v57, v57 row_ror:8 row_mask:0xf bank_mask:0xf\n"
        "v_smfmac_f32_16x16x64_bf16 v[24:27], v[40:43], a[192:199], v96\n"
        "v_cvt_pk_bf16_f32 v20, v20, v21\n"
        "v_smfmac_f32_16x16x64_bf16 v[24:27], v[44:47], a[248:255], v96\n"
        "v_cvt_pk_bf16_f32 v21, v22, v23\n"
        "v_smfmac_f32_16x16x64_bf16 v[24:27], v[48:51], v[168:175], v96\n"
        "ds_write_b64 v107, v[20:21] offset:41984\n"
        "v_add_f32_dpp v59, v59, v59 row_ror:4 row_mask:0xf bank_mask:0xf\n"
        "v_smfmac_f32_16x16x64_bf16 v[24:27], v[52:55], v[224:231], v96\n"
        "ds_read_b64_tr_b16 v[76:77], v108 offset:41984\n"
        "v_add_f32_dpp v59, v59, v59 row_ror:2 row_mask:0xf bank_mask:0xf\n"
        "s_waitcnt lgkmcnt(6)\n"
        "v_smfmac_f32_16x16x64_bf16 v[28:31], v[68:71], v[80:87], v96\n"
        "s_nop 3\n"
        "v_cvt_pk_bf16_f32 v24, v24, v25\n"
        "v_cvt_pk_bf16_f32 v25, v26, v27\n"
        "ds_write_b64 v107, v[24:25] offset:42496\n"
        "ds_read_b64_tr_b16 v[78:79], v108 offset:42496\n"
        "v_add_f32_dpp v59, v59, v59 row_ror:1 row_mask:0xf bank_mask:0xf\n"
        "v_mov_b32 v57, v59\n"
        "s_nop 1\n"
        "v_permlane16_swap_b32 v59, v57\n"
        "v_add_f32 v59, v59, v57\n"
        "ds_write_b64 v111, v[58:59] offset:55296\n"
        "s_waitcnt lgkmcnt(1)\n"
        "v_smfmac_f32_16x16x64_bf16 v[28:31], v[64:67], v[72:79], v96\n"
        "s_nop 1\n"
        "v_add_u32 v63, -1024, v99\n"
        "v_xor_b32 v60, 32768, v99\n"
        "v_mov_b32 v56, v99\n"
        "v_cndmask_b32 v63, v63, v99, s[48:49]\n"
        "v_mov_b32 v62, 0\n"
        "s_nop 0\n"
        "v_permlane32_swap_b32 v28, v30\n"
        "v_permlane32_swap_b32 v29, v31\n"
        "v_add_f32 v28, v28, v30\n"
        "v_add_f32 v29, v29, v31\n"
        "s_nop 0\n"
        "v_add_f32_dpp v28, v28, v28 row_ror:8 row_mask:0xf bank_mask:0xf\n"
        "v_add_f32_dpp v28, v29, v29 row_ror:8 row_mask:0xf bank_mask:0xc\n"
        "ds_write_b32 v106, v28 offset:36864\n"
        "s_memrealtime s[88:89]\n"
        "s_cmp_lg_u32 %[wv], 0\n"
        "s_waitcnt lgkmcnt(0)\n"
        "s_barrier\n"
        "ds_read2st64_b32 v[30:31], v106 offset0:145 offset1:146\n"
        "ds_read_b32 v29, v106 offset:37632\n"
        "ds_read_b64 v[10:11], v104 offset:55296\n"
        "s_cbranch_scc1 Lnf_%=\n"
        "s_waitcnt lgkmcnt(1)\n"
        "v_add_f32 v28, v28, v29\n"
        "v_add_f32 v30, v30, v31\n"
        "s_waitcnt lgkmcnt(0)\n"
        "v_add_f32 v28, v28, v30\n"
        "v_max_f32 v29, v28, v10\n"
        "s_nop 1\n"
        "v_max_f32_dpp v29, v29, v29 quad_perm:[1,0,3,2] row_mask:0xf bank_mask:0xf\n"
        "s_nop 1\n"
        "v_max_f32_dpp v29, v29, v29 quad_perm:[2,3,0,1] row_mask:0xf bank_mask:0xf\n"
        "s_nop 1\n"
        "v_max_f32_dpp v29, v29, v29 row_half_mirror row_mask:0xf bank_mask:0xf\n"
        "v_sub_f32 v28, v28, v29\n"
        "v_exp_f32 v28, v28\n"
        "v_sub_f32 v10, v10, v29\n"
        "v_exp_f32 v10, v10\n"
        "v_add_f32_dpp v29, v28, v28 quad_perm:[1,0,3,2] row_mask:0xf bank_mask:0xf\n"
        "v_mov_b32_dpp v30, v28 quad_perm:[1,0,3,2] row_mask:0xf bank_mask:0xf\n"
        "v_cndmask_b32 v8, 0, v10, s[44:45]\n"
        "v_add_f32_dpp v29, v29, v29 quad_perm:[2,3,0,1] row_mask:0xf bank_mask:0xf\n"
        "v_cndmask_b32 v9, 0, v10, s[46:47]\n"
        "v_cndmask_b32 v28, v28, v8, s[42:43]\n"
        "v_add_f32_dpp v29, v29, v29 row_half_mirror row_mask:0xf bank_mask:0xf\n"
        "v_fmac_f32 v29, v10, v11\n"
        "v_rcp_f32 v29, v29\n"
        "v_cndmask_b32 v30, v30, v9, s[42:43]\n"
        "v_mul_f32 v28, v28, v29\n"
        "v_mul_f32 v30, v30, v29\n"
        "v_cvt_pk_bf16_f32 v28, v28, v30\n"
        "v_min_u32 v28, -2, v28\n"
        "buffer_store_dword v28, v110, %[rs], %[bp] offen sc1\n"
        "s_add_u32 s92, %[bp], %[d2]\n"
        "buffer_store_dword v28, v110, %[rs], s92 offen sc1\n"
        "Lnf_%=:\n"
        "Lnb_%=:\n"
        "ds_read_b128 v[116:119], v60\n"
        "s_add_u32 %[s], %[s], 2\n"
        "s_max_u32 s90, s90, %[s]\n"
        "s_cmp_ge_u32 s90, 64\n"
        "v_mov_b32 v99, v60\n"
        "s_cbranch_scc1 Lfin_%=\n"
        "s_cmp_ge_u32 %[wv], 2\n"
        "s_cbranch_scc1 Ltop_%=\n"
        "s_waitcnt lgkmcnt(0)\n"
        "v_mfma_f32_16x16x32_bf16 v[0:3], a[0:3], v[116:119], 0\n"
        "ds_read_b128 v[28:31], v99 offset:2048\n"
        "v_max3_u32 v62, v116, v117, v62\n"
        "v_mfma_f32_16x16x32_bf16 v[4:7], a[8:11], v[116:119], 0\n"
        "v_max3_u32 v62, v118, v119, v62\n"
        "v_add_u32 v60, -28672, v99\n"
        "v_mfma_f32_16x16x32_bf16 v[8:11], a[16:19], v[116:119], 0\n"
        "v_add_u32 v58, %[w5], v100\n"
        "v_mfma_f32_16x16x32_bf16 v[12:15], a[24:27], v[116:119], 0\n"
        "ds_read_b128 v[32:35], v56 offset:0\n"
        "v_mfma_f32_16x16x32_bf16 v[16:19], a[32:35], v[116:119], 0\n"
        "ds_read_b128 v[48:51], v56 offset:256\n"
        "v_mfma_f32_16x16x32_bf16 v[20:23], a[40:43], v[116:119], 0\n"
        "ds_read_b128 v[36:39], v56 offset:2048\n"
        "v_mfma_f32_16x16x32_bf16 v[24:27], a[48:51], v[116:119], 0\n"
        "ds_read_b128 v[52:55], v56 offset:2304\n"
        "s_waitcnt lgkmcnt(4)\n"
        "v_mfma_f32_16x16x32_bf16 v[0:3], a[4:7], v[28:31], v[0:3]\n"
        "ds_read_b128 v[116:119], v99 offset:4096\n"
        "v_max3_u32 v62, v28, v29, v62\n"
        "v_mfma_f32_16x16x32_bf16 v[4:7], a[12:15], v[28:31], v[4:7]\n"
        "ds_read_b128 v[40:43], v56 offset:4096\n"
        "v_max3_u32 v62, v30, v31, v62\n"
        "v_mfma_f32_16x16x32_bf16 v[8:11], a[20:23], v[28:31], v[8:11]\n"
        "ds_read_b128 v[88:91], v56 offset:4352\n"
        "v_cndmask_b32 v61, v99, v60, s[66:67]\n"
        "v_mfma_f32_16x16x32_bf16 v[12:15], a[28:31], v[28:31], v[12:15]\n"
        "ds_read_b128 v[44:47], v63 offset:6144\n"
        "v_mfma_f32_16x16x32_bf16 v[16:19], a[36:39], v[28:31], v[16:19]\n"
        "ds_read_b128 v[92:95], v63 offset:6400\n"
        "v_mfma_f32_16x16x32_bf16 v[20:23], a[44:47], v[28:31], v[20:23]\n"
        "v_mfma_f32_16x16x32_bf16 v[24:27], a[52:55], v[28:31], v[24:27]\n"
        "s_waitcnt lgkmcnt(8)\n"
        "v_mfma_f32_16x16x32_bf16 v[64:67], v[72:75], v[32:35], 0\n"
        "s_lshl_b32 s88, s88, 4\n"
        "s_sub_u32 s88, s88, s61\n"
        "s_waitcnt lgkmcnt(7)\n"
        "v_mfma_f32_16x16x32_bf16 v[68:71], v[72:75], v[48:51], 0\n"
        "s_add_u32 s88, s88, 304\n"
        "s_waitcnt lgkmcnt(6)\n"
        "v_mfma_f32_16x16x32_bf16 v[64:67], v[76:79], v[36:39], v[64:67]\n"
        "s_cmp_gt_i32 s88, 0\n"
        "s_cselect_b32 s92, 1, 3\n"
        "s_waitcnt lgkmcnt(5)\n"
        "v_mfma_f32_16x16x32_bf16 v[68:71], v[76:79], v[52:55], v[68:71]\n"
        "s_ashr_i32 s88, s88, s92\n"
        "s_min_i32 s88, s88, 48\n"
        "s_waitcnt lgkmcnt(3)\n"
        "v_mfma_f32_16x16x32_bf16 v[64:67], v[80:83], v[40:43], v[64:67]\n"
        "s_max_i32 s88, s88, -32\n"
        "s_add_u32 s60, s60, s88\n"
        "s_waitcnt lgkmcnt(2)\n"
        "v_mfma_f32_16x16x32_bf16 v[68:71], v[80:83], v[88:91], v[68:71]\n"
        "s_add_u32 %[bm], %[bm], 32768\n"
        "s_add_u32 %[bt], %[bt], 32768\n"
        "s_waitcnt lgkmcnt(1)\n"
        "v_mfma_f32_16x16x32_bf16 v[64:67], v[84:87], v[44:47], v[64:67]\n"
        "s_add_u32 %[bp], %[bp], 16384\n"
        "s_add_u32 %[pt], %[pt], 81920\n"
        "s_waitcnt lgkmcnt(0)\n"
        "v_mfma_f32_16x16x32_bf16 v[68:71], v[84:87], v[92:95], v[68:71]\n"
        "v_mfma_f32_16x16x32_bf16 v[0:3], a[56:59], v[116:119], v[0:3]\n"
        "ds_read_b128 v[28:31], v61 offset:6144\n"
        "v_max3_u32 v62, v116, v117, v62\n"
        "v_mfma_f32_16x16x32_bf16 v[4:7], a[64:67], v[116:119], v[4:7]\n"
        "v_max3_u32 v62, v118, v119, v62\n"
        "v_cndmask_b32 v61, v99, v60, s[68:69]\n"
        "v_mfma_f32_16x16x32_bf16 v[8:11], a[72:75], v[116:119], v[8:11]\n"
        "v_mfma_f32_16x16x32_bf16 v[12:15], a[80:83], v[116:119], v[12:15]\n"
        "v_mfma_f32_16x16x32_bf16 v[16:19], a[88:91], v[116:119], v[16:19]\n"
        "ds_write_b128 v58, v[64:67] offset:11264\n"
        "v_mfma_f32_16x16x32_bf16 v[20:23], a[96:99], v[116:119], v[20:23]\n"
        "ds_write_b128 v58, v[68:71] offset:12288\n"
        "v_mfma_f32_16x16x32_bf16 v[24:27], a[104:107], v[116:119], v[24:27]\n"
        "s_waitcnt lgkmcnt(2)\n"
        "s_waitcnt lgkmcnt(0)\n"
        "s_barrier\n"
        "v_mfma_f32_16x16x32_bf16 v[0:3], a[60:63], v[28:31], v[0:3]\n"
        "ds_read_b128 v[116:119], v61 offset:8192\n"
        "v_max3_u32 v62, v28, v29, v62\n"
        "v_mfma_f32_16x16x32_bf16 v[4:7], a[68:71], v[28:31], v[4:7]\n"
        "v_max3_u32 v62, v30, v31, v62\n"
        "v_cndmask_b32 v61, v99, v60, s[70:71]\n"
        "v_mfma_f32_16x16x32_bf16 v[8:11], a[76:79], v[28:31], v[8:11]\n"
        "v_mfma_f32_16x16x32_bf16 v[12:15], a[84:87], v[28:31], v[12:15]\n"
        "v_mfma_f32_16x16x32_bf16 v[16:19], a[92:95], v[28:31], v[16:19]\n"
        "v_mfma_f32_16x16x32_bf16 v[20:23], a[100:103], v[28:31], v[20:23]\n"
        "v_mfma_f32_16x16x32_bf16 v[24:27], a[108:111], v[28:31], v[24:27]\n"
        "s_waitcnt lgkmcnt(0)\n"
        "v_mfma_f32_16x16x32_bf16 v[0:3], a[112:115], v[116:119], v[0:3]\n"
        "ds_read_b128 v[28:31], v61 offset:10240\n"
        "v_max3_u32 v62, v116, v117, v62\n"
        "v_mfma_f32_16x16x32_bf16 v[4:7], a[120:123], v[116:119], v[4:7]\n"
        "v_max3_u32 v62, v118, v119, v62\n"
        "v_cndmask_b32 v61, v99, v60, s[72:73]\n"
        "v_mfma_f32_16x16x32_bf16 v[8:11], a[128:131], v[116:119], v[8:11]\n"
        "v_mfma_f32_16x16x32_bf16 v[12:15], a[136:139], v[116:119], v[12:15]\n"
        "v_mfma_f32_16x16x32_bf16 v[16:19], a[144:147], v[116:119], v[16:19]\n"
        "v_mfma_f32_16x16x32_bf16 v[20:23], a[152:155], v[116:119], v[20:23]\n"
        "v_mfma_f32_16x16x32_bf16 v[24:27], a[160:163], v[116:119], v[24:27]\n"
        "s_waitcnt lgkmcnt(0)\n"
        "v_mfma_f32_16x16x32_bf16 v[0:3], a[116:119], v[28:31], v[0:3]\n"
        "ds_read_b128 v[116:119], v61 offset:12288\n"
        "v_max3_u32 v62, v28, v29, v62\n"
        "v_mfma_f32_16x16x32_bf16 v[4:7], a[124:127], v[28:31], v[4:7]\n"
        "v_max3_u32 v62, v30, v31, v62\n"
        "v_cndmask_b32 v61, v99, v60, s[74:75]\n"
        "v_mfma_f32_16x16x32_bf16 v[8:11], a[132:135], v[28:31], v[8:11]\n"
        "v_mfma_f32_16x16x32_bf16 v[12:15], a[140:143], v[28:31], v[12:15]\n"
        "v_mfma_f32_16x16x32_bf16 v[16:19], a[148:151], v[28:31], v[16:19]\n"
        "v_mfma_f32_16x16x32_bf16 v[20:23], a[156:159], v[28:31], v[20:23]\n"
        "v_mfma_f32_16x16x32_bf16 v[24:27], a[164:167], v[28:31], v[24:27]\n"
        "s_waitcnt lgkmcnt(0)\n"
        "v_mfma_f32_16x16x32_bf16 v[0:3], a[168:171], v[116:119], v[0:3]\n"
        "ds_read_b128 v[28:31], v61 offset:14336\n"
        "v_max3_u32 v62, v116, v117, v62\n"
        "v_mfma_f32_16x16x32_bf16 v[4:7], a[176:179], v[116:119], v[4:7]\n"
        "v_max3_u32 v62, v118, v119, v62\n"
        "v_cndmask_b32 v61, v99, v60, s[76:77]\n"
        "v_mfma_f32_16x16x32_bf16 v[8:11], a[184:187], v[116:119], v[8:11]\n"
        "v_mfma_f32_16x16x32_bf16 v[12:15], a[192:195], v[116:119], v[12:15]\n"
        "v_mfma_f32_16x16x32_bf16 v[16:19], a[200:203], v[116:119], v[16:19]\n"
        "v_mfma_f32_16x16x32_bf16 v[20:23], a[208:211], v[116:119], v[20:23]\n"
        "v_mfma_f32_16x16x32_bf16 v[24:27], a[216:219], v[116:119], v[24:27]\n"
        "s_waitcnt lgkmcnt(0)\n"
        "v_mfma_f32_16x16x32_bf16 v[0:3], a[172:175], v[28:31], v[0:3]\n"
        "ds_read_b128 v[116:119], v61 offset:16384\n"
        "v_max3_u32 v62, v28, v29, v62\n"
        "v_mfma_f32_16x16x32_bf16 v[4:7], a[180:183], v[28:31], v[4:7]\n"
        "v_max3_u32 v62, v30, v31, v62\n"
        "v_cndmask_b32 v61, v99, v60, s[78:79]\n"
        "v_mfma_f32_16x16x32_bf16 v[8:11], a[188:191], v[28:31], v[8:11]\n"
        "v_mfma_f32_16x16x32_bf16 v[12:15], a[196:199], v[28:31], v[12:15]\n"
        "v_mfma_f32_16x16x32_bf16 v[16:19], a[204:207], v[28:31], v[16:19]\n"
        "v_mfma_f32_16x16x32_bf16 v[20:23], a[212:215], v[28:31], v[20:23]\n"
        "v_mfma_f32_16x16x32_bf16 v[24:27], a[220:223], v[28:31], v[24:27]\n"
        "s_waitcnt lgkmcnt(0)\n"
        "v_mfma_f32_16x16x32_bf16 v[0:3], a[224:227], v[116:119], v[0:3]\n"
        "ds_read_b128 v[28:31], v61 offset:18432\n"
        "v_max3_u32 v62, v116, v117, v62\n"
        "v_mfma_f32_16x16x32_bf16 v[4:7], a[232:235], v[116:119], v[4:7]\n"
        "v_max3_u32 v62, v118, v119, v62\n"
        "v_cndmask_b32 v61, v99, v60, s[80:81]\n"
        "v_mfma_f32_16x16x32_bf16 v[8:11], a[240:243], v[116:119], v[8:11]\n"
        "v_mfma_f32_16x16x32_bf16 v[12:15], a[248:251], v[116:119], v[12:15]\n"
        "v_mfma_f32_16x16x32_bf16 v[16:19], v[120:123], v[116:119], v[16:19]\n"
        "v_mfma_f32_16x16x32_bf16 v[20:23], v[128:131], v[116:119], v[20:23]\n"
        "v_mfma_f32_16x16x32_bf16 v[24:27], v[136:139], v[116:119], v[24:27]\n"
        "s_waitcnt lgkmcnt(0)\n"
        "v_mfma_f32_16x16x32_bf16 v[0:3], a[228:231], v[28:31], v[0:3]\n"
        "ds_read_b128 v[116:119], v61 offset:20480\n"
        "v_max3_u32 v62, v28, v29, v62\n"
        "v_mfma_f32_16x16x32_bf16 v[4:7], a[236:239], v[28:31], v[4:7]\n"
        "v_max3_u32 v62, v30, v31, v62\n"
        "v_cndmask_b32 v61, v99, v60, s[82:83]\n"
        "v_mfma_f32_16x16x32_bf16 v[8:11], a[244:247], v[28:31], v[8:11]\n"
        "v_mfma_f32_16x16x32_bf16 v[12:15], a[252:255], v[28:31], v[12:15]\n"
        "v_mfma_f32_16x16x32_bf16 v[16:19], v[124:127], v[28:31], v[16:19]\n"
        "v_mfma_f32_16x16x32_bf16 v[20:23], v[132:135], v[28:31], v[20:23]\n"
        "v_mfma_f32_16x16x32_bf16 v[24:27], v[140:143], v[28:31], v[24:27]\n"
        "s_waitcnt lgkmcnt(0)\n"
        "v_mfma_f32_16x16x32_bf16 v[0:3], v[144:147], v[116:119], v[0:3]\n"
        "ds_read_b128 v[28:31], v61 offset:22528\n"
        "v_max3_u32 v62, v116, v117, v62\n"
        "v_mfma_f32_16x16x32_bf16 v[4:7], v[152:155], v[116:119], v[4:7]\n"
        "v_max3_u32 v62, v118, v119, v62\n"
        "v_cndmask_b32 v61, v99, v60, s[84:85]\n"
        "v_mfma_f32_16x16x32_bf16 v[8:11], v[160:163], v[116:119], v[8:11]\n"
        "v_mfma_f32_16x16x32_bf16 v[12:15], v[168:171], v[116:119], v[12:15]\n"
        "v_mfma_f32_16x16x32_bf16 v[16:19], v[176:179], v[116:119], v[16:19]\n"
        "v_mfma_f32_16x16x32_bf16 v[20:23], v[184:187], v[116:119], v[20:23]\n"
        "v_mfma_f32_16x16x32_bf16 v[24:27], v[192:195], v[116:119], v[24:27]\n"
        "s_waitcnt lgkmcnt(0)\n"
        "v_mfma_f32_16x16x32_bf16 v[0:3], v[148:151], v[28:31], v[0:3]\n"
        "ds_read_b128 v[116:119], v61 offset:24576\n"
        "v_max3_u32 v62, v28, v29, v62\n"
        "v_mfma_f32_16x16x32_bf16 v[4:7], v[156:159], v[28:31], v[4:7]\n"
        "v_max3_u32 v62, v30, v31, v62\n"
        "v_cndmask_b32 v61, v99, v60, s[86:87]\n"
        "v_mfma_f32_16x16x32_bf16 v[8:11], v[164:167], v[28:31], v[8:11]\n"
        "v_mfma_f32_16x16x32_bf16 v[12:15], v[172:175], v[28:31], v[12:15]\n"
        "v_mfma_f32_16x16x32_bf16 v[16:19], v[180:183], v[28:31], v[16:19]\n"
        "v_mfma_f32_16x16x32_bf16 v[20:23], v[188:191], v[28:31], v[20:23]\n"
        "v_mfma_f32_16x16x32_bf16 v[24:27], v[196:199], v[28:31], v[24:27]\n"
        "s_waitcnt lgkmcnt(0)\n"
        "v_mfma_f32_16x16x32_bf16 v[0:3], v[200:203], v[116:119], v[0:3]\n"
        "ds_read_b128 v[28:31], v61 offset:26624\n"
        "v_max3_u32 v62, v116, v117, v62\n"
        "v_mfma_f32_16x16x32_bf16 v[4:7], v[208:211], v[116:119], v[4:7]\n"
        "v_max3_u32 v62, v118, v119, v62\n"
        "v_mfma_f32_16x16x32_bf16 v[8:11], v[216:219], v[116:119], v[8:11]\n"
        "v_mfma_f32_16x16x32_bf16 v[12:15], v[224:227], v[116:119], v[12:15]\n"
        "v_mfma_f32_16x16x32_bf16 v[16:19], v[232:235], v[116:119], v[16:19]\n"
        "v_mfma_f32_16x16x32_bf16 v[20:23], v[240:243], v[116:119], v[20:23]\n"
        "v_mfma_f32_16x16x32_bf16 v[24:27], v[248:251], v[116:119], v[24:27]\n"
        "s_waitcnt lgkmcnt(0)\n"
        "v_mfma_f32_16x16x32_bf16 v[0:3], v[204:207], v[28:31], v[0:3]\n"
        "ds_read_b128 v[116:119], v99 offset:256\n"
        "v_max3_u32 v62, v28, v29, v62\n"
        "v_mfma_f32_16x16x32_bf16 v[4:7], v[212:215], v[28:31], v[4:7]\n"
        "v_max3_u32 v62, v30, v31, v62\n"
        "v_mfma_f32_16x16x32_bf16 v[8:11], v[220:223], v[28:31], v[8:11]\n"
        "v_mfma_f32_16x16x32_bf16 v[12:15], v[228:231], v[28:31], v[12:15]\n"
        "v_mfma_f32_16x16x32_bf16 v[16:19], v[236:239], v[28:31], v[16:19]\n"
        "v_mfma_f32_16x16x32_bf16 v[20:23], v[244:247], v[28:31], v[20:23]\n"
        "v_mfma_f32_16x16x32_bf16 v[24:27], v[252:255], v[28:31], v[24:27]\n"
        "s_waitcnt lgkmcnt(0)\n"
        "v_cvt_pk_bf16_f32 v32, v0, v1\n"
        "v_cvt_pk_bf16_f32 v33, v2, v3\n"
        "v_mfma_f32_16x16x32_bf16 v[0:3], a[0:3], v[116:119], 0\n"
        "ds_read_b128 v[28:31], v99 offset:2304\n"
        "v_cvt_pk_bf16_f32 v34, v4, v5\n"
        "v_cvt_pk_bf16_f32 v35, v6, v7\n"
        "v_mfma_f32_16x16x32_bf16 v[4:7], a[8:11], v[116:119], 0\n"
        "v_cvt_pk_bf16_f32 v36, v8, v9\n"
        "v_cvt_pk_bf16_f32 v37, v10, v11\n"
        "v_mfma_f32_16x16x32_bf16 v[8:11], a[16:19], v[116:119], 0\n"
        "v_cvt_pk_bf16_f32 v38, v12, v13\n"
        "v_cvt_pk_bf16_f32 v39, v14, v15\n"
        "v_mfma_f32_16x16x32_bf16 v[12:15], a[24:27], v[116:119], 0\n"
        "v_cvt_pk_bf16_f32 v40, v16, v17\n"
        "v_cvt_pk_bf16_f32 v41, v18, v19\n"
        "v_mfma_f32_16x16x32_bf16 v[16:19], a[32:35], v[116:119], 0\n"
        "v_cvt_pk_bf16_f32 v42, v20, v21\n"
        "v_cvt_pk_bf16_f32 v43, v22, v23\n"
        "v_mfma_f32_16x16x32_bf16 v[20:23], a[40:43], v[116:119], 0\n"
        "v_cvt_pk_bf16_f32 v46, v24, v25\n"
        "v_cvt_pk_bf16_f32 v47, v26, v27\n"
        "v_mfma_f32_16x16x32_bf16 v[24:27], a[48:51], v[116:119], 0\n"
        "v_max3_u32 v62, v116, v117, v62\n"
        "v_max3_u32 v62, v118, v119, v62\n"
        "s_waitcnt lgkmcnt(0)\n"
        "v_mfma_f32_16x16x32_bf16 v[0:3], a[4:7], v[28:31], v[0:3]\n"
        "ds_read_b128 v[116:119], v99 offset:4352\n"
        "v_max3_u32 v62, v28, v29, v62\n"
        "v_mfma_f32_16x16x32_bf16 v[4:7], a[12:15], v[28:31], v[4:7]\n"
        "v_max3_u32 v62, v30, v31, v62\n"
        "v_cndmask_b32 v61, v99, v60, s[66:67]\n"
        "v_mfma_f32_16x16x32_bf16 v[8:11], a[20:23], v[28:31], v[8:11]\n"
        "v_cndmask_b32 v44, 0, v46, s[48:49]\n"
        "v_cndmask_b32 v45, 0, v47, s[48:49]\n"
        "v_mfma_f32_16x16x32_bf16 v[12:15], a[28:31], v[28:31], v[12:15]\n"
        "v_cndmask_b32 v46, v46, 0, s[48:49]\n"
        "v_cndmask_b32 v47, v47, 0, s[48:49]\n"
        "v_mfma_f32_16x16x32_bf16 v[16:19], a[36:39], v[28:31], v[16:19]\n"
        "v_mov_b64 v[58:59], -1\n"
        "v_mfma_f32_16x16x32_bf16 v[20:23], a[44:47], v[28:31], v[20:23]\n"
        "s_add_u32 s95, %[bt], 131072\n"
        "buffer_store_dword v59, v109, %[rs], s95 offen sc1\n"
        "v_mfma_f32_16x16x32_bf16 v[24:27], a[52:55], v[28:31], v[24:27]\n"
        "s_add_u32 s95, %[bp], 65536\n"
        "buffer_store_dword v59, v110, %[rs], s95 offen sc1\n"
        "s_waitcnt lgkmcnt(0)\n"
        "v_mfma_f32_16x16x32_bf16 v[0:3], a[56:59], v[116:119], v[0:3]\n"
        "ds_read_b128 v[28:31], v61 offset:6400\n"
        "v_max3_u32 v62, v116, v117, v62\n"
        "v_mfma_f32_16x16x32_bf16 v[4:7], a[64:67], v[116:119], v[4:7]\n"
        "s_add_u32 s95, s95, %[d2]\n"
        "buffer_store_dword v59, v110, %[rs], s95 offen sc1\n"
        "v_max3_u32 v62, v118, v119, v62\n"
        "v_mfma_f32_16x16x32_bf16 v[8:11], a[72:75], v[116:119], v[8:11]\n"
        "s_add_u32 s95, %[bm], 138240\n"
        "s_mov_b64 exec, s[62:63]\n"
        "buffer_store_dwordx2 v[58:59], v100, %[rs], s95 offen sc1\n"
        "s_mov_b64 exec, -1\n"
        "v_cndmask_b32 v61, v99, v60, s[68:69]\n"
        "v_mfma_f32_16x16x32_bf16 v[12:15], a[80:83], v[116:119], v[12:15]\n"
        "s_add_u32 s92, %[bxc], 57344\n"
        "s_cmp_lt_u32 %[s], 62\n"
        "s_cselect_b32 s90, s92, %[bxc]\n"
        "s_xor_b32 s92, %[lxc], 32768\n"
        "s_mul_i32 s95, %[wq], 7168\n"
        "s_add_u32 %[lx], s95, s92\n"
        "s_add_u32 %[bx], s95, s90\n"
        "v_mfma_f32_16x16x32_bf16 v[16:19], a[88:91], v[116:119], v[16:19]\n"
        "v_mfma_f32_16x16x32_bf16 v[20:23], a[96:99], v[116:119], v[20:23]\n"
        "v_mfma_f32_16x16x32_bf16 v[24:27], a[104:107], v[116:119], v[24:27]\n"
        "s_waitcnt lgkmcnt(0)\n"
        "v_mfma_f32_16x16x32_bf16 v[0:3], a[60:63], v[28:31], v[0:3]\n"
        "ds_read_b128 v[116:119], v61 offset:8448\n"
        "v_max3_u32 v62, v28, v29, v62\n"
        "v_mfma_f32_16x16x32_bf16 v[4:7], a[68:71], v[28:31], v[4:7]\n"
        "s_mov_b32 m0, %[lx]\n"
        "s_nop 0\n"
        "buffer_load_dwordx4 v100, %[rs], %[bx] offen offset:0 sc1 lds\n"
        "v_max3_u32 v62, v30, v31, v62\n"
        "v_mfma_f32_16x16x32_bf16 v[8:11], a[76:79], v[28:31], v[8:11]\n"
        "v_cndmask_b32 v61, v99, v60, s[70:71]\n"
        "v_mfma_f32_16x16x32_bf16 v[12:15], a[84:87], v[28:31], v[12:15]\n"
        "v_mfma_f32_16x16x32_bf16 v[16:19], a[92:95], v[28:31], v[16:19]\n"
        "buffer_load_dwordx4 v100, %[rs], %[bx] offen offset:1024 sc1 lds\n"
        "v_mfma_f32_16x16x32_bf16 v[20:23], a[100:103], v[28:31], v[20:23]\n"
        "v_mfma_f32_16x16x32_bf16 v[24:27], a[108:111], v[28:31], v[24:27]\n"
        "buffer_load_dwordx4 v100, %[rs], %[bx] offen offset:2048 sc1 lds\n"
        "s_waitcnt lgkmcnt(0)\n"
        "v_mfma_f32_16x16x32_bf16 v[0:3], a[112:115], v[116:119], v[0:3]\n"
        "ds_read_b128 v[28:31], v61 offset:10496\n"
        "v_max3_u32 v62, v116, v117, v62\n"
        "v_mfma_f32_16x16x32_bf16 v[4:7], a[120:123], v[116:119], v[4:7]\n"
        "v_max3_u32 v62, v118, v119, v62\n"
        "v_cndmask_b32 v61, v99, v60, s[72:73]\n"
        "v_mfma_f32_16x16x32_bf16 v[8:11], a[128:131], v[116:119], v[8:11]\n"
        "v_mfma_f32_16x16x32_bf16 v[12:15], a[136:139], v[116:119], v[12:15]\n"
        "buffer_load_dwordx4 v100, %[rs], %[bx] offen offset:3072 sc1 lds\n"
        "v_mfma_f32_16x16x32_bf16 v[16:19], a[144:147], v[116:119], v[16:19]\n"
        "v_mfma_f32_16x16x32_bf16 v[20:23], a[152:155], v[116:119], v[20:23]\n"
        "s_add_u32 s95, %[lx], 4096\n"
        "s_mov_b32 m0, s95\n"
        "s_add_u32 s95, %[bx], 4096\n"
        "buffer_load_dwordx4 v100, %[rs], s95 offen offset:0 sc1 lds\n"
        "v_mfma_f32_16x16x32_bf16 v[24:27], a[160:163], v[116:119], v[24:27]\n"
        "s_waitcnt lgkmcnt(0)\n"
        "v_mfma_f32_16x16x32_bf16 v[0:3], a[116:119], v[28:31], v[0:3]\n"
        "ds_read_b128 v[116:119], v61 offset:12544\n"
        "v_max3_u32 v62, v28, v29, v62\n"
        "v_mfma_f32_16x16x32_bf16 v[4:7], a[124:127], v[28:31], v[4:7]\n"
        "buffer_load_dwordx4 v100, %[rs], s95 offen offset:1024 sc1 lds\n"
        "v_max3_u32 v62, v30, v31, v62\n"
        "v_mfma_f32_16x16x32_bf16 v[8:11], a[132:135], v[28:31], v[8:11]\n"
        "v_cndmask_b32 v61, v99, v60, s[74:75]\n"
        "v_mfma_f32_16x16x32_bf16 v[12:15], a[140:143], v[28:31], v[12:15]\n"
        "v_mfma_f32_16x16x32_bf16 v[16:19], a[148:151], v[28:31], v[16:19]\n"
        "buffer_load_dwordx4 v100, %[rs], s95 offen offset:2048 sc1 lds\n"
        "v_mfma_f32_16x16x32_bf16 v[20:23], a[156:159], v[28:31], v[20:23]\n"
        "v_mfma_f32_16x16x32_bf16 v[24:27], a[164:167], v[28:31], v[24:27]\n"
        "s_waitcnt lgkmcnt(0)\n"
        "v_mfma_f32_16x16x32_bf16 v[0:3], a[168:171], v[116:119], v[0:3]\n"
        "ds_read_b128 v[28:31], v61 offset:14592\n"
        "v_max3_u32 v62, v116, v117, v62\n"
        "v_mfma_f32_16x16x32_bf16 v[4:7], a[176:179], v[116:119], v[4:7]\n"
        "v_max3_u32 v62, v118, v119, v62\n"
        "v_cndmask_b32 v61, v99, v60, s[76:77]\n"
        "v_mfma_f32_16x16x32_bf16 v[8:11], a[184:187], v[116:119], v[8:11]\n"
        "v_mfma_f32_16x16x32_bf16 v[12:15], a[192:195], v[116:119], v[12:15]\n"
        "v_mfma_f32_16x16x32_bf16 v[16:19], a[200:203], v[116:119], v[16:19]\n"
        "v_mfma_f32_16x16x32_bf16 v[20:23], a[208:211], v[116:119], v[20:23]\n"
        "v_mfma_f32_16x16x32_bf16 v[24:27], a[216:219], v[116:119], v[24:27]\n"
        "s_waitcnt lgkmcnt(0)\n"
        "v_mfma_f32_16x16x32_bf16 v[0:3], a[172:175], v[28:31], v[0:3]\n"
        "ds_read_b128 v[116:119], v61 offset:16640\n"
        "v_max3_u32 v62, v28, v29, v62\n"
        "v_mfma_f32_16x16x32_bf16 v[4:7], a[180:183], v[28:31], v[4:7]\n"
        "v_max3_u32 v62, v30, v31, v62\n"
        "v_cndmask_b32 v61, v99, v60, s[78:79]\n"
        "v_mfma_f32_16x16x32_bf16 v[8:11], a[188:191], v[28:31], v[8:11]\n"
        "v_mfma_f32_16x16x32_bf16 v[12:15], a[196:199], v[28:31], v[12:15]\n"
        "v_mfma_f32_16x16x32_bf16 v[16:19], a[204:207], v[28:31], v[16:19]\n"
        "v_mfma_f32_16x16x32_bf16 v[20:23], a[212:215], v[28:31], v[20:23]\n"
        "v_mfma_f32_16x16x32_bf16 v[24:27], a[220:223], v[28:31], v[24:27]\n"
        "s_waitcnt lgkmcnt(0)\n"
        "v_mfma_f32_16x16x32_bf16 v[0:3], a[224:227], v[116:119], v[0:3]\n"
        "ds_read_b128 v[28:31], v61 offset:18688\n"
        "v_max3_u32 v62, v116, v117, v62\n"
        "v_mfma_f32_16x16x32_bf16 v[4:7], a[232:235], v[116:119], v[4:7]\n"
        "v_max3_u32 v62, v118, v119, v62\n"
        "v_cndmask_b32 v61, v99, v60, s[80:81]\n"
        "v_mfma_f32_16x16x32_bf16 v[8:11], a[240:243], v[116:119], v[8:11]\n"
        "v_mfma_f32_16x16x32_bf16 v[12:15], a[248:251], v[116:119], v[12:15]\n"
        "v_mfma_f32_16x16x32_bf16 v[16:19], v[120:123], v[116:119], v[16:19]\n"
        "v_mfma_f32_16x16x32_bf16 v[20:23], v[128:131], v[116:119], v[20:23]\n"
        "v_mfma_f32_16x16x32_bf16 v[24:27], v[136:139], v[116:119], v[24:27]\n"
        "s_waitcnt lgkmcnt(0)\n"
        "v_mfma_f32_16x16x32_bf16 v[0:3], a[228:231], v[28:31], v[0:3]\n"
        "ds_read_b128 v[116:119], v61 offset:20736\n"
        "v_max3_u32 v62, v28, v29, v62\n"
        "v_mfma_f32_16x16x32_bf16 v[4:7], a[236:239], v[28:31], v[4:7]\n"
        "s_sub_u32 s92, %[bm], 9212\n"
        "buffer_load_dword v57, off, %[rs], s92 sc1\n"
        "v_max3_u32 v62, v30, v31, v62\n"
        "v_mfma_f32_16x16x32_bf16 v[8:11], a[244:247], v[28:31], v[8:11]\n"
        "v_cndmask_b32 v61, v99, v60, s[82:83]\n"
        "v_mfma_f32_16x16x32_bf16 v[12:15], a[252:255], v[28:31], v[12:15]\n"
        "v_mfma_f32_16x16x32_bf16 v[16:19], v[124:127], v[28:31], v[16:19]\n"
        "v_mfma_f32_16x16x32_bf16 v[20:23], v[132:135], v[28:31], v[20:23]\n"
        "v_mfma_f32_16x16x32_bf16 v[24:27], v[140:143], v[28:31], v[24:27]\n"
        "s_waitcnt lgkmcnt(0)\n"
        "v_mfma_f32_16x16x32_bf16 v[0:3], v[144:147], v[116:119], v[0:3]\n"
        "ds_read_b128 v[28:31], v61 offset:22784\n"
        "v_max3_u32 v62, v116, v117, v62\n"
        "v_mfma_f32_16x16x32_bf16 v[4:7], v[152:155], v[116:119], v[4:7]\n"
        "v_max3_u32 v62, v118, v119, v62\n"
        "v_cndmask_b32 v61, v99, v60, s[84:85]\n"
        "v_mfma_f32_16x16x32_bf16 v[8:11], v[160:163], v[116:119], v[8:11]\n"
        "v_mfma_f32_16x16x32_bf16 v[12:15], v[168:171], v[116:119], v[12:15]\n"
        "v_mfma_f32_16x16x32_bf16 v[16:19], v[176:179], v[116:119], v[16:19]\n"
        "v_mfma_f32_16x16x32_bf16 v[20:23], v[184:187], v[116:119], v[20:23]\n"
        "v_mfma_f32_16x16x32_bf16 v[24:27], v[192:195], v[116:119], v[24:27]\n"
        "s_waitcnt lgkmcnt(0)\n"
        "v_mfma_f32_16x16x32_bf16 v[0:3], v[148:151], v[28:31], v[0:3]\n"
        "ds_read_b128 v[116:119], v61 offset:24832\n"
        "v_max3_u32 v62, v28, v29, v62\n"
        "v_mfma_f32_16x16x32_bf16 v[4:7], v[156:159], v[28:31], v[4:7]\n"
        "v_max3_u32 v62, v30, v31, v62\n"
        "v_cndmask_b32 v61, v99, v60, s[86:87]\n"
        "v_mfma_f32_16x16x32_bf16 v[8:11], v[164:167], v[28:31], v[8:11]\n"
        "v_add_u32 v58, -1024, v99\n"
        "v_cndmask_b32 v58, v58, v99, s[48:49]\n"
        "v_mfma_f32_16x16x32_bf16 v[12:15], v[172:175], v[28:31], v[12:15]\n"
        "v_mfma_f32_16x16x32_bf16 v[16:19], v[180:183], v[28:31], v[16:19]\n"
        "v_mfma_f32_16x16x32_bf16 v[20:23], v[188:191], v[28:31], v[20:23]\n"
        "v_mfma_f32_16x16x32_bf16 v[24:27], v[196:199], v[28:31], v[24:27]\n"
        "s_waitcnt lgkmcnt(0)\n"
        "v_mfma_f32_16x16x32_bf16 v[0:3], v[200:203], v[116:119], v[0:3]\n"
        "ds_read_b128 v[28:31], v61 offset:26880\n"
        "v_max3_u32 v62, v116, v117, v62\n"
        "v_mfma_f32_16x16x32_bf16 v[4:7], v[208:211], v[116:119], v[4:7]\n"
        "ds_read_b128 v[64:67], v99 offset:0\n"
        "v_max3_u32 v62, v118, v119, v62\n"
        "v_mfma_f32_16x16x32_bf16 v[8:11], v[216:219], v[116:119], v[8:11]\n"
        "ds_read_b128 v[80:83], v99 offset:256\n"
        "v_mfma_f32_16x16x32_bf16 v[12:15], v[224:227], v[116:119], v[12:15]\n"
        "ds_read_b128 v[68:71], v99 offset:2048\n"
        "v_mfma_f32_16x16x32_bf16 v[16:19], v[232:235], v[116:119], v[16:19]\n"
        "ds_read_b128 v[84:87], v99 offset:2304\n"
        "v_mfma_f32_16x16x32_bf16 v[20:23], v[240:243], v[116:119], v[20:23]\n"
        "ds_read_b128 v[72:75], v99 offset:4096\n"
        "v_mfma_f32_16x16x32_bf16 v[24:27], v[248:251], v[116:119], v[24:27]\n"
        "ds_read_b128 v[88:91], v99 offset:4352\n"
        "s_waitcnt lgkmcnt(6)\n"
        "v_mfma_f32_16x16x32_bf16 v[0:3], v[204:207], v[28:31], v[0:3]\n"
        "ds_read_b128 v[76:79], v58 offset:6144\n"
        "v_max3_u32 v62, v28, v29, v62\n"
        "v_mfma_f32_16x16x32_bf16 v[4:7], v[212:215], v[28:31], v[4:7]\n"
        "ds_read_b128 v[92:95], v58 offset:6400\n"
        "v_max3_u32 v62, v30, v31, v62\n"
        "v_mfma_f32_16x16x32_bf16 v[8:11], v[220:223], v[28:31], v[8:11]\n"
        "v_mfma_f32_16x16x32_bf16 v[12:15], v[228:231], v[28:31], v[12:15]\n"
        "v_mfma_f32_16x16x32_bf16 v[16:19], v[236:239], v[28:31], v[16:19]\n"
        "v_mfma_f32_16x16x32_bf16 v[20:23], v[244:247], v[28:31], v[20:23]\n"
        "v_mfma_f32_16x16x32_bf16 v[24:27], v[252:255], v[28:31], v[24:27]\n"
        "v_cmp_eq_u32 vcc, -1, v62\n"
        "s_waitcnt vmcnt(0)\n"
        "v_readfirstlane_b32 s91, v57\n"
        "s_and_b64 vcc, vcc, %[live]\n"
        "s_cbranch_vccnz Lslow_%=\n"
        "s_branch LB_%=\n"
        "Ltop_%=:\n"
        "s_waitcnt lgkmcnt(0)\n"
        "v_mfma_f32_16x16x32_bf16 v[0:3], a[0:3], v[116:119], 0\n"
        "ds_read_b128 v[28:31], v99 offset:2048\n"
        "v_max3_u32 v62, v116, v117, v62\n"
        "v_mfma_f32_16x16x32_bf16 v[4:7], a[8:11], v[116:119], 0\n"
        "v_max3_u32 v62, v118, v119, v62\n"
        "v_add_u32 v60, -28672, v99\n"
        "v_mfma_f32_16x16x32_bf16 v[8:11], a[16:19], v[116:119], 0\n"
        "v_add_u32 v58, %[w5], v100\n"
        "v_mfma_f32_16x16x32_bf16 v[12:15], a[24:27], v[116:119], 0\n"
        "ds_read_b128 v[32:35], v56 offset:0\n"
        "v_mfma_f32_16x16x32_bf16 v[16:19], a[32:35], v[116:119], 0\n"
        "ds_read_b128 v[48:51], v56 offset:256\n"
        "v_mfma_f32_16x16x32_bf16 v[20:23], a[40:43], v[116:119], 0\n"
        "ds_read_b128 v[36:39], v56 offset:2048\n"
        "v_mfma_f32_16x16x32_bf16 v[24:27], a[48:51], v[116:119], 0\n"
        "ds_read_b128 v[52:55], v56 offset:2304\n"
        "s_waitcnt lgkmcnt(4)\n"
        "v_mfma_f32_16x16x32_bf16 v[0:3], a[4:7], v[28:31], v[0:3]\n"
        "ds_read_b128 v[116:119], v99 offset:4096\n"
        "v_max3_u32 v62, v28, v29, v62\n"
        "v_mfma_f32_16x16x32_bf16 v[4:7], a[12:15], v[28:31], v[4:7]\n"
        "ds_read_b128 v[40:43], v56 offset:4096\n"
        "v_max3_u32 v62, v30, v31, v62\n"
        "v_mfma_f32_16x16x32_bf16 v[8:11], a[20:23], v[28:31], v[8:11]\n"
        "ds_read_b128 v[88:91], v56 offset:4352\n"
        "v_cndmask_b32 v61, v99, v60, s[66:67]\n"
        "v_mfma_f32_16x16x32_bf16 v[12:15], a[28:31], v[28:31], v[12:15]\n"
        "ds_read_b128 v[44:47], v63 offset:6144\n"
        "v_mfma_f32_16x16x32_bf16 v[16:19], a[36:39], v[28:31], v[16:19]\n"
        "ds_read_b128 v[92:95], v63 offset:6400\n"
        "v_mfma_f32_16x16x32_bf16 v[20:23], a[44:47], v[28:31], v[20:23]\n"
        "v_mfma_f32_16x16x32_bf16 v[24:27], a[52:55], v[28:31], v[24:27]\n"
        "s_waitcnt lgkmcnt(8)\n"
        "v_mfma_f32_16x16x32_bf16 v[64:67], v[72:75], v[32:35], 0\n"
        "s_lshl_b32 s88, s88, 4\n"
        "s_sub_u32 s88, s88, s61\n"
        "s_waitcnt lgkmcnt(7)\n"
        "v_mfma_f32_16x16x32_bf16 v[68:71], v[72:75], v[48:51], 0\n"
        "s_add_u32 s88, s88, 304\n"
        "s_waitcnt lgkmcnt(6)\n"
        "v_mfma_f32_16x16x32_bf16 v[64:67], v[76:79], v[36:39], v[64:67]\n"
        "s_cmp_gt_i32 s88, 0\n"
        "s_cselect_b32 s92, 1, 3\n"
        "s_waitcnt lgkmcnt(5)\n"
        "v_mfma_f32_16x16x32_bf16 v[68:71], v[76:79], v[52:55], v[68:71]\n"
        "s_ashr_i32 s88, s88, s92\n"
        "s_min_i32 s88, s88, 48\n"
        "s_waitcnt lgkmcnt(3)\n"
        "v_mfma_f32_16x16x32_bf16 v[64:67], v[80:83], v[40:43], v[64:67]\n"
        "s_max_i32 s88, s88, -32\n"
        "s_add_u32 s60, s60, s88\n"
        "s_waitcnt lgkmcnt(2)\n"
        "v_mfma_f32_16x16x32_bf16 v[68:71], v[80:83], v[88:91], v[68:71]\n"
        "s_add_u32 %[bm], %[bm], 32768\n"
        "s_add_u32 %[bt], %[bt], 32768\n"
        "s_waitcnt lgkmcnt(1)\n"
        "v_mfma_f32_16x16x32_bf16 v[64:67], v[84:87], v[44:47], v[64:67]\n"
        "s_add_u32 %[bp], %[bp], 16384\n"
        "s_add_u32 %[pt], %[pt], 81920\n"
        "s_waitcnt lgkmcnt(0)\n"
        "v_mfma_f32_16x16x32_bf16 v[68:71], v[84:87], v[92:95], v[68:71]\n"
        "v_mfma_f32_16x16x32_bf16 v[0:3], a[56:59], v[116:119], v[0:3]\n"
        "ds_read_b128 v[28:31], v61 offset:6144\n"
        "v_max3_u32 v62, v116, v117, v62\n"
        "v_mfma_f32_16x16x32_bf16 v[4:7], a[64:67], v[116:119], v[4:7]\n"
        "v_max3_u32 v62, v118, v119, v62\n"
        "v_cndmask_b32 v61, v99, v60, s[68:69]\n"
        "v_mfma_f32_16x16x32_bf16 v[8:11], a[72:75], v[116:119], v[8:11]\n"
        "v_mfma_f32_16x16x32_bf16 v[12:15], a[80:83], v[116:119], v[12:15]\n"
        "v_mfma_f32_16x16x32_bf16 v[16:19], a[88:91], v[116:119], v[16:19]\n"
        "ds_write_b128 v58, v[64:67] offset:11264\n"
        "v_mfma_f32_16x16x32_bf16 v[20:23], a[96:99], v[116:119], v[20:23]\n"
        "ds_write_b128 v58, v[68:71] offset:12288\n"
        "v_mfma_f32_16x16x32_bf16 v[24:27], a[104:107], v[116:119], v[24:27]\n"
        "s_waitcnt lgkmcnt(2)\n"
        "v_mfma_f32_16x16x32_bf16 v[0:3], a[60:63], v[28:31], v[0:3]\n"
        "ds_read_b128 v[116:119], v61 offset:8192\n"
        "v_max3_u32 v62, v28, v29, v62\n"
        "v_mfma_f32_16x16x32_bf16 v[4:7], a[68:71], v[28:31], v[4:7]\n"
        "v_max3_u32 v62, v30, v31, v62\n"
        "v_cndmask_b32 v61, v99, v60, s[70:71]\n"
        "v_mfma_f32_16x16x32_bf16 v[8:11], a[76:79], v[28:31], v[8:11]\n"
        "v_mfma_f32_16x16x32_bf16 v[12:15], a[84:87], v[28:31], v[12:15]\n"
        "v_mfma_f32_16x16x32_bf16 v[16:19], a[92:95], v[28:31], v[16:19]\n"
        "v_mfma_f32_16x16x32_bf16 v[20:23], a[100:103], v[28:31], v[20:23]\n"
        "v_mfma_f32_16x16x32_bf16 v[24:27], a[108:111], v[28:31], v[24:27]\n"
        "s_waitcnt lgkmcnt(0)\n"
        "v_mfma_f32_16x16x32_bf16 v[0:3], a[112:115], v[116:119], v[0:3]\n"
        "ds_read_b128 v[28:31], v61 offset:10240\n"
        "v_max3_u32 v62, v116, v117, v62\n"
        "v_mfma_f32_16x16x32_bf16 v[4:7], a[120:123], v[116:119], v[4:7]\n"
        "v_max3_u32 v62, v118, v119, v62\n"
        "v_cndmask_b32 v61, v99, v60, s[72:73]\n"
        "v_mfma_f32_16x16x32_bf16 v[8:11], a[128:131], v[116:119], v[8:11]\n"
        "v_mfma_f32_16x16x32_bf16 v[12:15], a[136:139], v[116:119], v[12:15]\n"
        "v_mfma_f32_16x16x32_bf16 v[16:19], a[144:147], v[116:119], v[16:19]\n"
        "v_mfma_f32_16x16x32_bf16 v[20:23], a[152:155], v[116:119], v[20:23]\n"
        "v_mfma_f32_16x16x32_bf16 v[24:27], a[160:163], v[116:119], v[24:27]\n"
        "s_waitcnt lgkmcnt(0)\n"
        "s_barrier\n"
        "v_mfma_f32_16x16x32_bf16 v[0:3], a[116:119], v[28:31], v[0:3]\n"
        "ds_read_b128 v[116:119], v61 offset:12288\n"
        "v_max3_u32 v62, v28, v29, v62\n"
        "v_max3_u32 v62, v30, v31, v62\n"
        "v_cndmask_b32 v61, v99, v60, s[74:75]\n"
        "v_mfma_f32_16x16x32_bf16 v[4:7], a[124:127], v[28:31], v[4:7]\n"
        "ds_read_b128 v[80:83], v101 offset:11264\n"
        "v_mfma_f32_16x16x32_bf16 v[8:11], a[132:135], v[28:31], v[8:11]\n"
        "ds_read_b128 v[84:87], v101 offset:16384\n"
        "v_mfma_f32_16x16x32_bf16 v[12:15], a[140:143], v[28:31], v[12:15]\n"
        "ds_read_b128 v[88:91], v101 offset:21504\n"
        "v_mfma_f32_16x16x32_bf16 v[16:19], a[148:151], v[28:31], v[16:19]\n"
        "ds_read_b128 v[92:95], v101 offset:26624\n"
        "v_xor_b32 v48, 1024, v101\n"
        "v_bfrev_b32 v56, -2\n"
        "v_mfma_f32_16x16x32_bf16 v[20:23], a[156:159], v[28:31], v[20:23]\n"
        "ds_read_b128 v[64:67], v101 offset:57344\n"
        "v_cndmask_b32 v56, v56, v101, s[58:59]\n"
        "v_mfma_f32_16x16x32_bf16 v[24:27], a[164:167], v[28:31], v[24:27]\n"
        "ds_read_b128 v[68:71], v48 offset:59392\n"
        "s_waitcnt lgkmcnt(6)\n"
        "v_mfma_f32_16x16x32_bf16 v[0:3], a[168:171], v[116:119], v[0:3]\n"
        "ds_read_b128 v[28:31], v61 offset:14336\n"
        "v_max3_u32 v62, v116, v117, v62\n"
        "v_max3_u32 v62, v118, v119, v62\n"
        "v_cndmask_b32 v61, v99, v60, s[76:77]\n"
        "v_mfma_f32_16x16x32_bf16 v[4:7], a[176:179], v[116:119], v[4:7]\n"
        "s_waitcnt lgkmcnt(5)\n"
        "v_add_f32 v80, v80, v84\n"
        "v_add_f32 v81, v81, v85\n"
        "v_add_f32 v82, v82, v86\n"
        "v_add_f32 v83, v83, v87\n"
        "v_mfma_f32_16x16x32_bf16 v[8:11], a[184:187], v[116:119], v[8:11]\n"
        "s_waitcnt lgkmcnt(3)\n"
        "v_add_f32 v88, v88, v92\n"
        "v_add_f32 v89, v89, v93\n"
        "v_add_f32 v90, v90, v94\n"
        "v_add_f32 v91, v91, v95\n"
        "v_mfma_f32_16x16x32_bf16 v[12:15], a[192:195], v[116:119], v[12:15]\n"
        "v_add_f32 v80, v80, v88\n"
        "v_add_f32 v81, v81, v89\n"
        "v_add_f32 v82, v82, v90\n"
        "v_add_f32 v83, v83, v91\n"
        "v_mfma_f32_16x16x32_bf16 v[16:19], a[200:203], v[116:119], v[16:19]\n"
        "s_waitcnt lgkmcnt(1)\n"
        "v_max3_f32 v49, v64, v65, v66\n"
        "v_max3_f32 v50, v67, v68, v69\n"
        "v_permlane32_swap_b32 v80, v82\n"
        "v_mfma_f32_16x16x32_bf16 v[20:23], a[208:211], v[116:119], v[20:23]\n"
        "v_permlane32_swap_b32 v81, v83\n"
        "v_add_f32 v80, v80, v82\n"
        "v_add_f32 v81, v81, v83\n"
        "v_mfma_f32_16x16x32_bf16 v[24:27], a[216:219], v[116:119], v[24:27]\n"
        "v_max3_f32 v51, v70, v71, v80\n"
        "v_max3_f32 v49, v49, v50, v81\n"
        "v_max_f32 v49, v49, v51\n"
        "v_mov_b32 v50, v49\n"
        "s_waitcnt lgkmcnt(0)\n"
        "v_mfma_f32_16x16x32_bf16 v[0:3], a[172:175], v[28:31], v[0:3]\n"
        "ds_read_b128 v[116:119], v61 offset:16384\n"
        "v_max3_u32 v62, v28, v29, v62\n"
        "v_max3_u32 v62, v30, v31, v62\n"
        "v_cndmask_b32 v61, v99, v60, s[78:79]\n"
        "v_mfma_f32_16x16x32_bf16 v[4:7], a[180:183], v[28:31], v[4:7]\n"
        "v_permlane16_swap_b32 v49, v50\n"
        "v_max_f32 v49, v49, v50\n"
        "v_mov_b32 v50, v49\n"
        "v_mfma_f32_16x16x32_bf16 v[8:11], a[188:191], v[28:31], v[8:11]\n"
        "s_nop 0\n"
        "v_permlane32_swap_b32 v49, v50\n"
        "v_max_f32 v49, v49, v50\n"
        "v_sub_f32 v64, v64, v49\n"
        "v_mfma_f32_16x16x32_bf16 v[12:15], a[196:199], v[28:31], v[12:15]\n"
        "v_sub_f32 v65, v65, v49\n"
        "v_sub_f32 v66, v66, v49\n"
        "v_sub_f32 v67, v67, v49\n"
        "v_sub_f32 v68, v68, v49\n"
        "v_mfma_f32_16x16x32_bf16 v[16:19], a[204:207], v[28:31], v[16:19]\n"
        "v_sub_f32 v69, v69, v49\n"
        "v_sub_f32 v70, v70, v49\n"
        "v_sub_f32 v71, v71, v49\n"
        "v_sub_f32 v80, v80, v49\n"
        "v_mfma_f32_16x16x32_bf16 v[20:23], a[212:215], v[28:31], v[20:23]\n"
        "v_sub_f32 v81, v81, v49\n"
        "v_exp_f32 v64, v64\n"
        "v_mfma_f32_16x16x32_bf16 v[24:27], a[220:223], v[28:31], v[24:27]\n"
        "v_exp_f32 v65, v65\n"
        "v_exp_f32 v66, v66\n"
        "s_waitcnt lgkmcnt(0)\n"
        "v_mfma_f32_16x16x32_bf16 v[0:3], a[224:227], v[116:119], v[0:3]\n"
        "ds_read_b128 v[28:31], v61 offset:18432\n"
        "v_max3_u32 v62, v116, v117, v62\n"
        "v_max3_u32 v62, v118, v119, v62\n"
        "v_cndmask_b32 v61, v99, v60, s[80:81]\n"
        "v_mfma_f32_16x16x32_bf16 v[4:7], a[232:235], v[116:119], v[4:7]\n"
        "v_exp_f32 v67, v67\n"
        "v_exp_f32 v68, v68\n"
        "v_mfma_f32_16x16x32_bf16 v[8:11], a[240:243], v[116:119], v[8:11]\n"
        "v_exp_f32 v69, v69\n"
        "v_exp_f32 v70, v70\n"
        "v_mfma_f32_16x16x32_bf16 v[12:15], a[248:251], v[116:119], v[12:15]\n"
        "v_exp_f32 v71, v71\n"
        "v_exp_f32 v80, v80\n"
        "v_mfma_f32_16x16x32_bf16 v[16:19], v[120:123], v[116:119], v[16:19]\n"
        "v_exp_f32 v81, v81\n"
        "v_add_f32 v49, v64, v65\n"
        "v_add_f32 v50, v66, v67\n"
        "v_mfma_f32_16x16x32_bf16 v[20:23], v[128:131], v[116:119], v[20:23]\n"
        "v_add_f32 v51, v68, v69\n"
        "v_add_f32 v52, v70, v71\n"
        "v_add_f32 v53, v80, v81\n"
        "v_add_f32 v49, v49, v50\n"
        "v_mfma_f32_16x16x32_bf16 v[24:27], v[136:139], v[116:119], v[24:27]\n"
        "v_add_f32 v51, v51, v52\n"
        "v_add_f32 v49, v49, v53\n"
        "v_add_f32 v49, v49, v51\n"
        "v_mov_b32 v50, v49\n"
        "s_waitcnt lgkmcnt(0)\n"
        "v_mfma_f32_16x16x32_bf16 v[0:3], a[228:231], v[28:31], v[0:3]\n"
        "ds_read_b128 v[116:119], v61 offset:20480\n"
        "v_max3_u32 v62, v28, v29, v62\n"
        "v_max3_u32 v62, v30, v31, v62\n"
        "v_cndmask_b32 v61, v99, v60, s[82:83]\n"
        "v_mfma_f32_16x16x32_bf16 v[4:7], a[236:239], v[28:31], v[4:7]\n"
        "v_permlane16_swap_b32 v49, v50\n"
        "v_add_f32 v49, v49, v50\n"
        "v_mov_b32 v50, v49\n"
        "v_mfma_f32_16x16x32_bf16 v[8:11], a[244:247], v[28:31], v[8:11]\n"
        "s_nop 0\n"
        "v_permlane32_swap_b32 v49, v50\n"
        "v_add_f32 v49, v49, v50\n"
        "v_mfma_f32_16x16x32_bf16 v[12:15], a[252:255], v[28:31], v[12:15]\n"
        "v_rcp_f32 v49, v49\n"
        "v_mfma_f32_16x16x32_bf16 v[16:19], v[124:127], v[28:31], v[16:19]\n"
        "v_mul_f32 v64, v64, v49\n"
        "v_mul_f32 v65, v65, v49\n"
        "v_mul_f32 v66, v66, v49\n"
        "v_mul_f32 v67, v67, v49\n"
        "v_mfma_f32_16x16x32_bf16 v[20:23], v[132:135], v[28:31], v[20:23]\n"
        "v_mul_f32 v68, v68, v49\n"
        "v_mul_f32 v69, v69, v49\n"
        "v_mul_f32 v70, v70, v49\n"
        "v_mul_f32 v71, v71, v49\n"
        "v_mfma_f32_16x16x32_bf16 v[24:27], v[140:143], v[28:31], v[24:27]\n"
        "v_mul_f32 v80, v80, v49\n"
        "v_mul_f32 v81, v81, v49\n"
        "v_cvt_pk_bf16_f32 v72, v64, v65\n"
        "v_cvt_pk_bf16_f32 v73, v66, v67\n"
        "s_waitcnt lgkmcnt(0)\n"
        "v_mfma_f32_16x16x32_bf16 v[0:3], v[144:147], v[116:119], v[0:3]\n"
        "ds_read_b128 v[28:31], v61 offset:22528\n"
        "v_max3_u32 v62, v116, v117, v62\n"
        "v_max3_u32 v62, v118, v119, v62\n"
        "v_cndmask_b32 v61, v99, v60, s[84:85]\n"
        "v_mfma_f32_16x16x32_bf16 v[4:7], v[152:155], v[116:119], v[4:7]\n"
        "v_cvt_pk_bf16_f32 v74, v68, v69\n"
        "v_cvt_pk_bf16_f32 v75, v70, v71\n"
        "v_cvt_pk_bf16_f32 v76, v80, v81\n"
        "v_min_u32 v72, -2, v72\n"
        "v_mfma_f32_16x16x32_bf16 v[8:11], v[160:163], v[116:119], v[8:11]\n"
        "v_min_u32 v73, -2, v73\n"
        "v_min_u32 v74, -2, v74\n"
        "v_min_u32 v75, -2, v75\n"
        "v_min_u32 v76, -2, v76\n"
        "v_mfma_f32_16x16x32_bf16 v[12:15], v[168:171], v[116:119], v[12:15]\n"
        "buffer_store_dwordx4 v[72:75], v56, %[rs], %[pt] offen sc1\n"
        "v_mov_b64 v[52:53], -1\n"
        "v_mfma_f32_16x16x32_bf16 v[16:19], v[176:179], v[116:119], v[16:19]\n"
        "buffer_store_dword v76, v102, %[rs], %[pt] offen sc1\n"
        "v_mov_b64 v[54:55], -1\n"
        "v_mfma_f32_16x16x32_bf16 v[20:23], v[184:187], v[116:119], v[20:23]\n"
        "s_add_u32 s95, %[pt], 327680\n"
        "buffer_store_dwordx4 v[52:55], v56, %[rs], s95 offen sc1\n"
        "v_mfma_f32_16x16x32_bf16 v[24:27], v[192:195], v[116:119], v[24:27]\n"
        "buffer_store_dword v52, v102, %[rs], s95 offen sc1\n"
        "s_waitcnt lgkmcnt(0)\n"
        "v_mfma_f32_16x16x32_bf16 v[0:3], v[148:151], v[28:31], v[0:3]\n"
        "ds_read_b128 v[116:119], v61 offset:24576\n"
        "v_max3_u32 v62, v28, v29, v62\n"
        "v_max3_u32 v62, v30, v31, v62\n"
        "v_cndmask_b32 v61, v99, v60, s[86:87]\n"
        "v_mfma_f32_16x16x32_bf16 v[4:7], v[156:159], v[28:31], v[4:7]\n"
        "v_mfma_f32_16x16x32_bf16 v[8:11], v[164:167], v[28:31], v[8:11]\n"
        "v_mfma_f32_16x16x32_bf16 v[12:15], v[172:175], v[28:31], v[12:15]\n"
        "v_mfma_f32_16x16x32_bf16 v[16:19], v[180:183], v[28:31], v[16:19]\n"
        "v_mfma_f32_16x16x32_bf16 v[20:23], v[188:191], v[28:31], v[20:23]\n"
        "v_mfma_f32_16x16x32_bf16 v[24:27], v[196:199], v[28:31], v[24:27]\n"
        "s_waitcnt lgkmcnt(0)\n"
        "v_mfma_f32_16x16x32_bf16 v[0:3], v[200:203], v[116:119], v[0:3]\n"
        "ds_read_b128 v[28:31], v61 offset:26624\n"
        "v_max3_u32 v62, v116, v117, v62\n"
        "v_max3_u32 v62, v118, v119, v62\n"
        "v_mfma_f32_16x16x32_bf16 v[4:7], v[208:211], v[116:119], v[4:7]\n"
        "v_mfma_f32_16x16x32_bf16 v[8:11], v[216:219], v[116:119], v[8:11]\n"
        "v_mfma_f32_16x16x32_bf16 v[12:15], v[224:227], v[116:119], v[12:15]\n"
        "v_mfma_f32_16x16x32_bf16 v[16:19], v[232:235], v[116:119], v[16:19]\n"
        "v_mfma_f32_16x16x32_bf16 v[20:23], v[240:243], v[116:119], v[20:23]\n"
        "v_mfma_f32_16x16x32_bf16 v[24:27], v[248:251], v[116:119], v[24:27]\n"
        "s_waitcnt lgkmcnt(0)\n"
        "v_mfma_f32_16x16x32_bf16 v[0:3], v[204:207], v[28:31], v[0:3]\n"
        "ds_read_b128 v[116:119], v99 offset:256\n"
        "v_max3_u32 v62, v28, v29, v62\n"
        "v_max3_u32 v62, v30, v31, v62\n"
        "v_mfma_f32_16x16x32_bf16 v[4:7], v[212:215], v[28:31], v[4:7]\n"
        "v_mfma_f32_16x16x32_bf16 v[8:11], v[220:223], v[28:31], v[8:11]\n"
        "v_mfma_f32_16x16x32_bf16 v[12:15], v[228:231], v[28:31], v[12:15]\n"
        "v_mfma_f32_16x16x32_bf16 v[16:19], v[236:239], v[28:31], v[16:19]\n"
        "v_mfma_f32_16x16x32_bf16 v[20:23], v[244:247], v[28:31], v[20:23]\n"
        "v_mfma_f32_16x16x32_bf16 v[24:27], v[252:255], v[28:31], v[24:27]\n"
        "s_waitcnt lgkmcnt(0)\n"
        "v_cvt_pk_bf16_f32 v32, v0, v1\n"
        "v_cvt_pk_bf16_f32 v33, v2, v3\n"
        "v_mfma_f32_16x16x32_bf16 v[0:3], a[0:3], v[116:119], 0\n"
        "ds_read_b128 v[28:31], v99 offset:2304\n"
        "v_cvt_pk_bf16_f32 v34, v4, v5\n"
        "v_cvt_pk_bf16_f32 v35, v6, v7\n"
        "v_mfma_f32_16x16x32_bf16 v[4:7], a[8:11], v[116:119], 0\n"
        "v_cvt_pk_bf16_f32 v36, v8, v9\n"
        "v_cvt_pk_bf16_f32 v37, v10, v11\n"
        "v_mfma_f32_16x16x32_bf16 v[8:11], a[16:19], v[116:119], 0\n"
        "v_cvt_pk_bf16_f32 v38, v12, v13\n"
        "v_cvt_pk_bf16_f32 v39, v14, v15\n"
        "v_mfma_f32_16x16x32_bf16 v[12:15], a[24:27], v[116:119], 0\n"
        "v_cvt_pk_bf16_f32 v40, v16, v17\n"
        "v_cvt_pk_bf16_f32 v41, v18, v19\n"
        "v_mfma_f32_16x16x32_bf16 v[16:19], a[32:35], v[116:119], 0\n"
        "v_cvt_pk_bf16_f32 v42, v20, v21\n"
        "v_cvt_pk_bf16_f32 v43, v22, v23\n"
        "v_mfma_f32_16x16x32_bf16 v[20:23], a[40:43], v[116:119], 0\n"
        "v_cvt_pk_bf16_f32 v46, v24, v25\n"
        "v_cvt_pk_bf16_f32 v47, v26, v27\n"
        "v_mfma_f32_16x16x32_bf16 v[24:27], a[48:51], v[116:119], 0\n"
        "v_max3_u32 v62, v116, v117, v62\n"
        "v_max3_u32 v62, v118, v119, v62\n"
        "s_waitcnt lgkmcnt(0)\n"
        "v_mfma_f32_16x16x32_bf16 v[0:3], a[4:7], v[28:31], v[0:3]\n"
        "ds_read_b128 v[116:119], v99 offset:4352\n"
        "v_max3_u32 v62, v28, v29, v62\n"
        "v_mfma_f32_16x16x32_bf16 v[4:7], a[12:15], v[28:31], v[4:7]\n"
        "v_max3_u32 v62, v30, v31, v62\n"
        "v_cndmask_b32 v61, v99, v60, s[66:67]\n"
        "v_mfma_f32_16x16x32_bf16 v[8:11], a[20:23], v[28:31], v[8:11]\n"
        "v_cndmask_b32 v44, 0, v46, s[48:49]\n"
        "v_cndmask_b32 v45, 0, v47, s[48:49]\n"
        "v_mfma_f32_16x16x32_bf16 v[12:15], a[28:31], v[28:31], v[12:15]\n"
        "v_cndmask_b32 v46, v46, 0, s[48:49]\n"
        "v_cndmask_b32 v47, v47, 0, s[48:49]\n"
        "v_mfma_f32_16x16x32_bf16 v[16:19], a[36:39], v[28:31], v[16:19]\n"
        "v_mov_b64 v[58:59], -1\n"
        "v_mfma_f32_16x16x32_bf16 v[20:23], a[44:47], v[28:31], v[20:23]\n"
        "s_add_u32 s95, %[bt], 131072\n"
        "buffer_store_dword v59, v109, %[rs], s95 offen sc1\n"
        "v_mfma_f32_16x16x32_bf16 v[24:27], a[52:55], v[28:31], v[24:27]\n"
        "s_add_u32 s95, %[bp], 65536\n"
        "buffer_store_dword v59, v110, %[rs], s95 offen sc1\n"
        "s_waitcnt lgkmcnt(0)\n"
        "v_mfma_f32_16x16x32_bf16 v[0:3], a[56:59], v[116:119], v[0:3]\n"
        "ds_read_b128 v[28:31], v61 offset:6400\n"
        "v_max3_u32 v62, v116, v117, v62\n"
        "v_mfma_f32_16x16x32_bf16 v[4:7], a[64:67], v[116:119], v[4:7]\n"
        "s_add_u32 s95, s95, %[d2]\n"
        "buffer_store_dword v59, v110, %[rs], s95 offen sc1\n"
        "v_max3_u32 v62, v118, v119, v62\n"
        "v_mfma_f32_16x16x32_bf16 v[8:11], a[72:75], v[116:119], v[8:11]\n"
        "s_add_u32 s95, %[bm], 138240\n"
        "s_mov_b64 exec, s[62:63]\n"
        "buffer_store_dwordx2 v[58:59], v100, %[rs], s95 offen sc1\n"
        "s_mov_b64 exec, -1\n"
        "v_cndmask_b32 v61, v99, v60, s[68:69]\n"
        "v_mfma_f32_16x16x32_bf16 v[12:15], a[80:83], v[116:119], v[12:15]\n"
        "s_add_u32 s92, %[bxc], 57344\n"
        "s_cmp_lt_u32 %[s], 62\n"
        "s_cselect_b32 s90, s92, %[bxc]\n"
        "s_xor_b32 s92, %[lxc], 32768\n"
        "s_mul_i32 s95, %[wq], 7168\n"
        "s_add_u32 %[lx], s95, s92\n"
        "s_add_u32 %[bx], s95, s90\n"
        "v_mfma_f32_16x16x32_bf16 v[16:19], a[88:91], v[116:119], v[16:19]\n"
        "v_mfma_f32_16x16x32_bf16 v[20:23], a[96:99], v[116:119], v[20:23]\n"
        "v_mfma_f32_16x16x32_bf16 v[24:27], a[104:107], v[116:119], v[24:27]\n"
        "s_waitcnt lgkmcnt(0)\n"
        "v_mfma_f32_16x16x32_bf16 v[0:3], a[60:63], v[28:31], v[0:3]\n"
        "ds_read_b128 v[116:119], v61 offset:8448\n"
        "v_max3_u32 v62, v28, v29, v62\n"
        "v_mfma_f32_16x16x32_bf16 v[4:7], a[68:71], v[28:31], v[4:7]\n"
        "s_mov_b32 m0, %[lx]\n"
        "s_nop 0\n"
        "buffer_load_dwordx4 v100, %[rs], %[bx] offen offset:0 sc1 lds\n"
        "v_max3_u32 v62, v30, v31, v62\n"
        "v_mfma_f32_16x16x32_bf16 v[8:11], a[76:79], v[28:31], v[8:11]\n"
        "v_cndmask_b32 v61, v99, v60, s[70:71]\n"
        "v_mfma_f32_16x16x32_bf16 v[12:15], a[84:87], v[28:31], v[12:15]\n"
        "v_mfma_f32_16x16x32_bf16 v[16:19], a[92:95], v[28:31], v[16:19]\n"
        "buffer_load_dwordx4 v100, %[rs], %[bx] offen offset:1024 sc1 lds\n"
        "v_mfma_f32_16x16x32_bf16 v[20:23], a[100:103], v[28:31], v[20:23]\n"
        "v_mfma_f32_16x16x32_bf16 v[24:27], a[108:111], v[28:31], v[24:27]\n"
        "buffer_load_dwordx4 v100, %[rs], %[bx] offen offset:2048 sc1 lds\n"
        "s_waitcnt lgkmcnt(0)\n"
        "v_mfma_f32_16x16x32_bf16 v[0:3], a[112:115], v[116:119], v[0:3]\n"
        "ds_read_b128 v[28:31], v61 offset:10496\n"
        "v_max3_u32 v62, v116, v117, v62\n"
        "v_mfma_f32_16x16x32_bf16 v[4:7], a[120:123], v[116:119], v[4:7]\n"
        "v_max3_u32 v62, v118, v119, v62\n"
        "v_cndmask_b32 v61, v99, v60, s[72:73]\n"
        "v_mfma_f32_16x16x32_bf16 v[8:11], a[128:131], v[116:119], v[8:11]\n"
        "v_mfma_f32_16x16x32_bf16 v[12:15], a[136:139], v[116:119], v[12:15]\n"
        "buffer_load_dwordx4 v100, %[rs], %[bx] offen offset:3072 sc1 lds\n"
        "v_mfma_f32_16x16x32_bf16 v[16:19], a[144:147], v[116:119], v[16:19]\n"
        "v_mfma_f32_16x16x32_bf16 v[20:23], a[152:155], v[116:119], v[20:23]\n"
        "s_add_u32 s95, %[lx], 4096\n"
        "s_mov_b32 m0, s95\n"
        "s_add_u32 s95, %[bx], 4096\n"
        "buffer_load_dwordx4 v100, %[rs], s95 offen offset:0 sc1 lds\n"
        "v_mfma_f32_16x16x32_bf16 v[24:27], a[160:163], v[116:119], v[24:27]\n"
        "s_waitcnt lgkmcnt(0)\n"
        "v_mfma_f32_16x16x32_bf16 v[0:3], a[116:119], v[28:31], v[0:3]\n"
        "ds_read_b128 v[116:119], v61 offset:12544\n"
        "v_max3_u32 v62, v28, v29, v62\n"
        "v_mfma_f32_16x16x32_bf16 v[4:7], a[124:127], v[28:31], v[4:7]\n"
        "buffer_load_dwordx4 v100, %[rs], s95 offen offset:1024 sc1 lds\n"
        "v_max3_u32 v62, v30, v31, v62\n"
        "v_mfma_f32_16x16x32_bf16 v[8:11], a[132:135], v[28:31], v[8:11]\n"
        "v_cndmask_b32 v61, v99, v60, s[74:75]\n"
        "v_mfma_f32_16x16x32_bf16 v[12:15], a[140:143], v[28:31], v[12:15]\n"
        "v_mfma_f32_16x16x32_bf16 v[16:19], a[148:151], v[28:31], v[16:19]\n"
        "buffer_load_dwordx4 v100, %[rs], s95 offen offset:2048 sc1 lds\n"
        "v_mfma_f32_16x16x32_bf16 v[20:23], a[156:159], v[28:31], v[20:23]\n"
        "v_mfma_f32_16x16x32_bf16 v[24:27], a[164:167], v[28:31], v[24:27]\n"
        "s_waitcnt lgkmcnt(0)\n"
        "v_mfma_f32_16x16x32_bf16 v[0:3], a[168:171], v[116:119], v[0:3]\n"
        "ds_read_b128 v[28:31], v61 offset:14592\n"
        "v_max3_u32 v62, v116, v117, v62\n"
        "v_mfma_f32_16x16x32_bf16 v[4:7], a[176:179], v[116:119], v[4:7]\n"
        "v_max3_u32 v62, v118, v119, v62\n"
        "v_cndmask_b32 v61, v99, v60, s[76:77]\n"
        "v_mfma_f32_16x16x32_bf16 v[8:11], a[184:187], v[116:119], v[8:11]\n"
        "v_mfma_f32_16x16x32_bf16 v[12:15], a[192:195], v[116:119], v[12:15]\n"
        "v_mfma_f32_16x16x32_bf16 v[16:19], a[200:203], v[116:119], v[16:19]\n"
        "v_mfma_f32_16x16x32_bf16 v[20:23], a[208:211], v[116:119], v[20:23]\n"
        "v_mfma_f32_16x16x32_bf16 v[24:27], a[216:219], v[116:119], v[24:27]\n"
        "s_waitcnt lgkmcnt(0)\n"
        "v_mfma_f32_16x16x32_bf16 v[0:3], a[172:175], v[28:31], v[0:3]\n"
        "ds_read_b128 v[116:119], v61 offset:16640\n"
        "v_max3_u32 v62, v28, v29, v62\n"
        "v_mfma_f32_16x16x32_bf16 v[4:7], a[180:183], v[28:31], v[4:7]\n"
        "v_max3_u32 v62, v30, v31, v62\n"
        "v_cndmask_b32 v61, v99, v60, s[78:79]\n"
        "v_mfma_f32_16x16x32_bf16 v[8:11], a[188:191], v[28:31], v[8:11]\n"
        "v_mfma_f32_16x16x32_bf16 v[12:15], a[196:199], v[28:31], v[12:15]\n"
        "v_mfma_f32_16x16x32_bf16 v[16:19], a[204:207], v[28:31], v[16:19]\n"
        "v_mfma_f32_16x16x32_bf16 v[20:23], a[212:215], v[28:31], v[20:23]\n"
        "v_mfma_f32_16x16x32_bf16 v[24:27], a[220:223], v[28:31], v[24:27]\n"
        "s_waitcnt lgkmcnt(0)\n"
        "v_mfma_f32_16x16x32_bf16 v[0:3], a[224:227], v[116:119], v[0:3]\n"
        "ds_read_b128 v[28:31], v61 offset:18688\n"
        "v_max3_u32 v62, v116, v117, v62\n"
        "v_mfma_f32_16x16x32_bf16 v[4:7], a[232:235], v[116:119], v[4:7]\n"
        "v_max3_u32 v62, v118, v119, v62\n"
        "v_cndmask_b32 v61, v99, v60, s[80:81]\n"
        "v_mfma_f32_16x16x32_bf16 v[8:11], a[240:243], v[116:119], v[8:11]\n"
        "v_mfma_f32_16x16x32_bf16 v[12:15], a[248:251], v[116:119], v[12:15]\n"
        "v_mfma_f32_16x16x32_bf16 v[16:19], v[120:123], v[116:119], v[16:19]\n"
        "v_mfma_f32_16x16x32_bf16 v[20:23], v[128:131], v[116:119], v[20:23]\n"
        "v_mfma_f32_16x16x32_bf16 v[24:27], v[136:139], v[116:119], v[24:27]\n"
        "s_waitcnt lgkmcnt(0)\n"
        "v_mfma_f32_16x16x32_bf16 v[0:3], a[228:231], v[28:31], v[0:3]\n"
        "ds_read_b128 v[116:119], v61 offset:20736\n"
        "v_max3_u32 v62, v28, v29, v62\n"
        "v_mfma_f32_16x16x32_bf16 v[4:7], a[236:239], v[28:31], v[4:7]\n"
        "s_sub_u32 s92, %[bm], 9212\n"
        "buffer_load_dword v57, off, %[rs], s92 sc1\n"
        "v_max3_u32 v62, v30, v31, v62\n"
        "v_mfma_f32_16x16x32_bf16 v[8:11], a[244:247], v[28:31], v[8:11]\n"
        "v_cndmask_b32 v61, v99, v60, s[82:83]\n"
        "v_mfma_f32_16x16x32_bf16 v[12:15], a[252:255], v[28:31], v[12:15]\n"
        "v_mfma_f32_16x16x32_bf16 v[16:19], v[124:127], v[28:31], v[16:19]\n"
        "v_mfma_f32_16x16x32_bf16 v[20:23], v[132:135], v[28:31], v[20:23]\n"
        "v_mfma_f32_16x16x32_bf16 v[24:27], v[140:143], v[28:31], v[24:27]\n"
        "s_waitcnt lgkmcnt(0)\n"
        "v_mfma_f32_16x16x32_bf16 v[0:3], v[144:147], v[116:119], v[0:3]\n"
        "ds_read_b128 v[28:31], v61 offset:22784\n"
        "v_max3_u32 v62, v116, v117, v62\n"
        "v_mfma_f32_16x16x32_bf16 v[4:7], v[152:155], v[116:119], v[4:7]\n"
        "v_max3_u32 v62, v118, v119, v62\n"
        "v_cndmask_b32 v61, v99, v60, s[84:85]\n"
        "v_mfma_f32_16x16x32_bf16 v[8:11], v[160:163], v[116:119], v[8:11]\n"
        "v_mfma_f32_16x16x32_bf16 v[12:15], v[168:171], v[116:119], v[12:15]\n"
        "v_mfma_f32_16x16x32_bf16 v[16:19], v[176:179], v[116:119], v[16:19]\n"
        "v_mfma_f32_16x16x32_bf16 v[20:23], v[184:187], v[116:119], v[20:23]\n"
        "v_mfma_f32_16x16x32_bf16 v[24:27], v[192:195], v[116:119], v[24:27]\n"
        "s_waitcnt lgkmcnt(0)\n"
        "v_mfma_f32_16x16x32_bf16 v[0:3], v[148:151], v[28:31], v[0:3]\n"
        "ds_read_b128 v[116:119], v61 offset:24832\n"
        "v_max3_u32 v62, v28, v29, v62\n"
        "v_mfma_f32_16x16x32_bf16 v[4:7], v[156:159], v[28:31], v[4:7]\n"
        "v_max3_u32 v62, v30, v31, v62\n"
        "v_cndmask_b32 v61, v99, v60, s[86:87]\n"
        "v_mfma_f32_16x16x32_bf16 v[8:11], v[164:167], v[28:31], v[8:11]\n"
        "v_add_u32 v58, -1024, v99\n"
        "v_cndmask_b32 v58, v58, v99, s[48:49]\n"
        "v_mfma_f32_16x16x32_bf16 v[12:15], v[172:175], v[28:31], v[12:15]\n"
        "v_mfma_f32_16x16x32_bf16 v[16:19], v[180:183], v[28:31], v[16:19]\n"
        "v_mfma_f32_16x16x32_bf16 v[20:23], v[188:191], v[28:31], v[20:23]\n"
        "v_mfma_f32_16x16x32_bf16 v[24:27], v[196:199], v[28:31], v[24:27]\n"
        "s_waitcnt lgkmcnt(0)\n"
        "v_mfma_f32_16x16x32_bf16 v[0:3], v[200:203], v[116:119], v[0:3]\n"
        "ds_read_b128 v[28:31], v61 offset:26880\n"
        "v_max3_u32 v62, v116, v117, v62\n"
        "v_mfma_f32_16x16x32_bf16 v[4:7], v[208:211], v[116:119], v[4:7]\n"
        "ds_read_b128 v[64:67], v99 offset:0\n"
        "v_max3_u32 v62, v118, v119, v62\n"
        "v_mfma_f32_16x16x32_bf16 v[8:11], v[216:219], v[116:119], v[8:11]\n"
        "ds_read_b128 v[80:83], v99 offset:256\n"
        "v_mfma_f32_16x16x32_bf16 v[12:15], v[224:227], v[116:119], v[12:15]\n"
        "ds_read_b128 v[68:71], v99 offset:2048\n"
        "v_mfma_f32_16x16x32_bf16 v[16:19], v[232:235], v[116:119], v[16:19]\n"
        "ds_read_b128 v[84:87], v99 offset:2304\n"
        "v_mfma_f32_16x16x32_bf16 v[20:23], v[240:243], v[116:119], v[20:23]\n"
        "ds_read_b128 v[72:75], v99 offset:4096\n"
        "v_mfma_f32_16x16x32_bf16 v[24:27], v[248:251], v[116:119], v[24:27]\n"
        "ds_read_b128 v[88:91], v99 offset:4352\n"
        "s_waitcnt lgkmcnt(6)\n"
        "v_mfma_f32_16x16x32_bf16 v[0:3], v[204:207], v[28:31], v[0:3]\n"
        "ds_read_b128 v[76:79], v58 offset:6144\n"
        "v_max3_u32 v62, v28, v29, v62\n"
        "v_mfma_f32_16x16x32_bf16 v[4:7], v[212:215], v[28:31], v[4:7]\n"
        "ds_read_b128 v[92:95], v58 offset:6400\n"
        "v_max3_u32 v62, v30, v31, v62\n"
        "v_mfma_f32_16x16x32_bf16 v[8:11], v[220:223], v[28:31], v[8:11]\n"
        "v_mfma_f32_16x16x32_bf16 v[12:15], v[228:231], v[28:31], v[12:15]\n"
        "v_mfma_f32_16x16x32_bf16 v[16:19], v[236:239], v[28:31], v[16:19]\n"
        "v_mfma_f32_16x16x32_bf16 v[20:23], v[244:247], v[28:31], v[20:23]\n"
        "v_mfma_f32_16x16x32_bf16 v[24:27], v[252:255], v[28:31], v[24:27]\n"
        "v_cmp_eq_u32 vcc, -1, v62\n"
        "s_waitcnt vmcnt(0)\n"
        "v_readfirstlane_b32 s91, v57\n"
        "s_and_b64 vcc, vcc, %[live]\n"
        "s_cbranch_vccnz Lslow_%=\n"
        "s_branch LB_%=\n"
        "Lretry_%=:\n"
        "s_cmp_lg_u32 s93, -1\n"
        "s_cbranch_scc1 Lpoll_%=\n"
        "s_mov_b32 s93, %[capv]\n"
        "s_branch Lpoll_%=\n"
        "s_lshl_b32 s92, %[wv], 11\n"
        "s_add_u32 s92, s92, 126976\n"
        "v_add_u32 v72, s92, v100\n"
        "s_mov_b32 s93, %[capv]\n"
        "s_nop 3\n"
        "v_mov_b64 v[88:89], 0\n"
        "v_mov_b64 v[90:91], 0\n"
        "v_mov_b64 v[92:93], 0\n"
        "v_mov_b64 v[94:95], 0\n"
        "v_mov_b64 v[0:1], 0\n"
        "v_mov_b64 v[2:3], 0\n"
        "v_mov_b64 v[4:5], 0\n"
        "v_mov_b64 v[6:7], 0\n"
        "v_mov_b64 v[8:9], 0\n"
        "v_mov_b64 v[10:11], 0\n"
        "s_waitcnt vmcnt(0) lgkmcnt(0)\n"
        "ds_read_b128 v[64:67], v72\n"
        "ds_read_b128 v[68:71], v72 offset:1024\n"
        "s_waitcnt lgkmcnt(0)\n"
        "s_branch Lpc_%=\n"
        "Lpoll_%=:\n"
        "s_sub_u32 s93, s93, 1\n"
        "s_cbranch_scc1 Lto_%=\n"
        "buffer_load_dwordx4 v[64:67], v97, %[rs], %[bm] offen sc1\n"
        "buffer_load_dwordx4 v[68:71], v98, %[rs], %[bm] offen sc1\n"
        "s_nop 3\n"
        "v_mov_b64 v[88:89], 0\n"
        "v_mov_b64 v[90:91], 0\n"
        "v_mov_b64 v[92:93], 0\n"
        "v_mov_b64 v[94:95], 0\n"
        "v_mov_b64 v[0:1], 0\n"
        "v_mov_b64 v[2:3], 0\n"
        "v_mov_b64 v[4:5], 0\n"
        "v_mov_b64 v[6:7], 0\n"
        "v_mov_b64 v[8:9], 0\n"
        "v_mov_b64 v[10:11], 0\n"
        "s_waitcnt vmcnt(0)\n"
        "s_branch Lpc_%=\n"
        "Lbw0_%=:\n"
        "s_cmp_eq_u32 %[s], 0\n"
        "s_cselect_b32 s91, 0, s91\n"
        "s_sub_u32 s92, %[bm], 9212\n"
        "s_cmp_lg_u32 s91, -1\n"
        "s_cbranch_scc1 Lbcd_%=\n"
        "s_mov_b32 s93, %[capv]\n"
        "Lbc_%=:\n"
        "buffer_load_dword v64, off, %[rs], s92 sc1\n"
        "s_sub_u32 s93, s93, 1\n"
        "s_cbranch_scc1 Lbcd_%=\n"
        "s_waitcnt vmcnt(0)\n"
        "v_readfirstlane_b32 s91, v64\n"
        "s_cmp_lg_u32 s91, -1\n"
        "s_cbranch_scc1 Lbcd_%=\n"
        "s_sleep 3\n"
        "s_branch Lbc_%=\n"
        "Lbcd_%=:\n"
        "Lbw_%=:\n"
        "s_memrealtime s[96:97]\n"
        "s_waitcnt lgkmcnt(0)\n"
        "s_lshl_b32 s96, s96, 4\n"
        "s_sub_u32 s96, s91, s96\n"
        "s_sub_u32 s97, s96, 144\n"
        "s_cmp_lt_u32 s97, 9600\n"
        "s_cbranch_scc1 Lbw_%=\n"
        "s_min_i32 s97, s96, 144\n"
        "Lbwf_%=:\n"
        "s_sub_i32 s97, s97, 21\n"
        "s_cmp_gt_i32 s97, 60\n"
        "s_cbranch_scc1 Lbwf_%=\n"
        "s_branch Lbw1_%=\n"
        "Lto_%=:\n"
        "s_mov_b32 %[capv], 0\n"
        "v_mov_b32 v72, 1\n"
        "s_mov_b32 s94, 4227328\n"
        "buffer_store_dword v72, off, %[rs], s94 sc1\n"
        "s_mov_b64 %[live], 0\n"
        "s_nop 1\n"
        "s_branch Lgo_%=\n"
        "Lfin_%=:\n"
        "ds_read_b128 v[32:35], v56 offset:0\n"
        "ds_read_b128 v[48:51], v56 offset:256\n"
        "ds_read_b128 v[36:39], v56 offset:2048\n"
        "ds_read_b128 v[52:55], v56 offset:2304\n"
        "ds_read_b128 v[40:43], v56 offset:4096\n"
        "ds_read_b128 v[88:91], v56 offset:4352\n"
        "ds_read_b128 v[44:47], v63 offset:6144\n"
        "ds_read_b128 v[92:95], v63 offset:6400\n"
        "v_add_u32 v58, %[w5], v100\n"
        "s_lshl_b32 s88, s88, 4\n"
        "s_sub_u32 s88, s88, s61\n"
        "s_add_u32 s88, s88, 304\n"
        "s_cmp_gt_i32 s88, 0\n"
        "s_cselect_b32 s92, 1, 3\n"
        "s_ashr_i32 s88, s88, s92\n"
        "s_min_i32 s88, s88, 48\n"
        "s_max_i32 s88, s88, -32\n"
        "s_add_u32 s60, s60, s88\n"
        "s_add_u32 %[bm], %[bm], 32768\n"
        "s_add_u32 %[bt], %[bt], 32768\n"
        "s_add_u32 %[bp], %[bp], 16384\n"
        "s_add_u32 %[pt], %[pt], 81920\n"
        "s_waitcnt lgkmcnt(7)\n"
        "v_mfma_f32_16x16x32_bf16 v[64:67], v[72:75], v[32:35], 0\n"
        "s_waitcnt lgkmcnt(6)\n"
        "v_mfma_f32_16x16x32_bf16 v[68:71], v[72:75], v[48:51], 0\n"
        "s_waitcnt lgkmcnt(5)\n"
        "v_mfma_f32_16x16x32_bf16 v[64:67], v[76:79], v[36:39], v[64:67]\n"
        "s_waitcnt lgkmcnt(4)\n"
        "v_mfma_f32_16x16x32_bf16 v[68:71], v[76:79], v[52:55], v[68:71]\n"
        "s_waitcnt lgkmcnt(3)\n"
        "v_mfma_f32_16x16x32_bf16 v[64:67], v[80:83], v[40:43], v[64:67]\n"
        "s_waitcnt lgkmcnt(2)\n"
        "v_mfma_f32_16x16x32_bf16 v[68:71], v[80:83], v[88:91], v[68:71]\n"
        "s_waitcnt lgkmcnt(1)\n"
        "v_mfma_f32_16x16x32_bf16 v[64:67], v[84:87], v[44:47], v[64:67]\n"
        "s_waitcnt lgkmcnt(0)\n"
        "v_mfma_f32_16x16x32_bf16 v[68:71], v[84:87], v[92:95], v[68:71]\n"
        "s_nop 5\n"
        "ds_write_b128 v58, v[64:67] offset:11264\n"
        "s_nop 0\n"
        "ds_write_b128 v58, v[68:71] offset:12288\n"
        "s_cmp_ge_u32 %[s], 64\n"
        "s_cbranch_scc1 Lf2_%=\n"
        "s_mul_i32 s92, %[wq], 7168\n"
        "s_add_u32 s93, s92, %[lxc]\n"
        "s_mov_b32 m0, s93\n"
        "s_add_u32 s92, s92, %[bxc]\n"
        "buffer_load_dwordx4 v100, %[rs], s92 offen offset:0 sc1 lds\n"
        "buffer_load_dwordx4 v100, %[rs], s92 offen offset:1024 sc1 lds\n"
        "buffer_load_dwordx4 v100, %[rs], s92 offen offset:2048 sc1 lds\n"
        "buffer_load_dwordx4 v100, %[rs], s92 offen offset:3072 sc1 lds\n"
        "s_add_u32 s93, s93, 4096\n"
        "s_mov_b32 m0, s93\n"
        "s_add_u32 s92, s92, 4096\n"
        "buffer_load_dwordx4 v100, %[rs], s92 offen offset:0 sc1 lds\n"
        "buffer_load_dwordx4 v100, %[rs], s92 offen offset:1024 sc1 lds\n"
        "buffer_load_dwordx4 v100, %[rs], s92 offen offset:2048 sc1 lds\n"
        "Lf2_%=:\n"
        "s_waitcnt lgkmcnt(0)\n"
        "s_barrier\n"
        "ds_read_b128 v[80:83], v101 offset:11264\n"
        "ds_read_b128 v[84:87], v101 offset:16384\n"
        "ds_read_b128 v[88:91], v101 offset:21504\n"
        "ds_read_b128 v[92:95], v101 offset:26624\n"
        "v_xor_b32 v48, 1024, v101\n"
        "v_bfrev_b32 v56, -2\n"
        "ds_read_b128 v[64:67], v101 offset:57344\n"
        "v_cndmask_b32 v56, v56, v101, s[58:59]\n"
        "ds_read_b128 v[68:71], v48 offset:59392\n"
        "s_waitcnt lgkmcnt(4)\n"
        "v_add_f32 v80, v80, v84\n"
        "v_add_f32 v81, v81, v85\n"
        "v_add_f32 v82, v82, v86\n"
        "v_add_f32 v83, v83, v87\n"
        "s_waitcnt lgkmcnt(2)\n"
        "v_add_f32 v88, v88, v92\n"
        "v_add_f32 v89, v89, v93\n"
        "v_add_f32 v90, v90, v94\n"
        "v_add_f32 v91, v91, v95\n"
        "v_add_f32 v80, v80, v88\n"
        "v_add_f32 v81, v81, v89\n"
        "v_add_f32 v82, v82, v90\n"
        "v_add_f32 v83, v83, v91\n"
        "s_waitcnt lgkmcnt(0)\n"
        "v_max3_f32 v49, v64, v65, v66\n"
        "v_max3_f32 v50, v67, v68, v69\n"
        "v_permlane32_swap_b32 v80, v82\n"
        "v_permlane32_swap_b32 v81, v83\n"
        "v_add_f32 v80, v80, v82\n"
        "v_add_f32 v81, v81, v83\n"
        "v_max3_f32 v51, v70, v71, v80\n"
        "v_max3_f32 v49, v49, v50, v81\n"
        "v_max_f32 v49, v49, v51\n"
        "v_mov_b32 v50, v49\n"
        "s_nop 1\n"
        "v_permlane16_swap_b32 v49, v50\n"
        "v_max_f32 v49, v49, v50\n"
        "v_mov_b32 v50, v49\n"
        "s_nop 1\n"
        "v_permlane32_swap_b32 v49, v50\n"
        "v_max_f32 v49, v49, v50\n"
        "v_sub_f32 v64, v64, v49\n"
        "v_sub_f32 v65, v65, v49\n"
        "v_sub_f32 v66, v66, v49\n"
        "v_sub_f32 v67, v67, v49\n"
        "v_sub_f32 v68, v68, v49\n"
        "v_sub_f32 v69, v69, v49\n"
        "v_sub_f32 v70, v70, v49\n"
        "v_sub_f32 v71, v71, v49\n"
        "v_sub_f32 v80, v80, v49\n"
        "v_sub_f32 v81, v81, v49\n"
        "v_exp_f32 v64, v64\n"
        "v_exp_f32 v65, v65\n"
        "v_exp_f32 v66, v66\n"
        "v_exp_f32 v67, v67\n"
        "v_exp_f32 v68, v68\n"
        "v_exp_f32 v69, v69\n"
        "v_exp_f32 v70, v70\n"
        "v_exp_f32 v71, v71\n"
        "v_exp_f32 v80, v80\n"
        "v_exp_f32 v81, v81\n"
        "v_add_f32 v49, v64, v65\n"
        "v_add_f32 v50, v66, v67\n"
        "v_add_f32 v51, v68, v69\n"
        "v_add_f32 v52, v70, v71\n"
        "v_add_f32 v53, v80, v81\n"
        "v_add_f32 v49, v49, v50\n"
        "v_add_f32 v51, v51, v52\n"
        "v_add_f32 v49, v49, v53\n"
        "v_add_f32 v49, v49, v51\n"
        "v_mov_b32 v50, v49\n"
        "s_nop 1\n"
        "v_permlane16_swap_b32 v49, v50\n"
        "v_add_f32 v49, v49, v50\n"
        "v_mov_b32 v50, v49\n"
        "s_nop 1\n"
        "v_permlane32_swap_b32 v49, v50\n"
        "v_add_f32 v49, v49, v50\n"
        "v_rcp_f32 v49, v49\n"
        "s_nop 0\n"
        "v_mul_f32 v64, v64, v49\n"
        "v_mul_f32 v65, v65, v49\n"
        "v_mul_f32 v66, v66, v49\n"
        "v_mul_f32 v67, v67, v49\n"
        "v_mul_f32 v68, v68, v49\n"
        "v_mul_f32 v69, v69, v49\n"
        "v_mul_f32 v70, v70, v49\n"
        "v_mul_f32 v71, v71, v49\n"
        "v_mul_f32 v80, v80, v49\n"
        "v_mul_f32 v81, v81, v49\n"
        "v_cvt_pk_bf16_f32 v72, v64, v65\n"
        "v_cvt_pk_bf16_f32 v73, v66, v67\n"
        "v_cvt_pk_bf16_f32 v74, v68, v69\n"
        "v_cvt_pk_bf16_f32 v75, v70, v71\n"
        "v_cvt_pk_bf16_f32 v76, v80, v81\n"
        "v_min_u32 v72, -2, v72\n"
        "v_min_u32 v73, -2, v73\n"
        "v_min_u32 v74, -2, v74\n"
        "v_min_u32 v75, -2, v75\n"
        "v_min_u32 v76, -2, v76\n"
        "buffer_store_dwordx4 v[72:75], v56, %[rs], %[pt] offen sc1\n"
        "v_mov_b64 v[52:53], -1\n"
        "buffer_store_dword v76, v102, %[rs], %[pt] offen sc1\n"
        "v_mov_b64 v[54:55], -1\n"
        "s_add_u32 s95, %[pt], 327680\n"
        "buffer_store_dwordx4 v[52:55], v56, %[rs], s95 offen sc1\n"
        "buffer_store_dword v52, v102, %[rs], s95 offen sc1\n"
        "s_cmp_ge_u32 %[s], 64\n"
        "s_cbranch_scc1 Lend_%=\n"
        "s_waitcnt vmcnt(4)\n"
        "s_barrier\n"
        "s_branch Lplain_%=\n"
        "Lslow_%=:\n"
        "s_nop 7\n"
        "s_mov_b32 s93, %[capv]\n"
        "s_sub_u32 %[slowc], %[slowc], 1\n"
        "s_cbranch_scc1 Lslow_%=t\n"
        "Lslow_%=p:\n"
        "buffer_load_dword v63, v103, %[rs], %[bxc] offen sc1\n"
        "s_sub_u32 s93, s93, 1\n"
        "s_waitcnt vmcnt(0)\n"
        "v_cmp_eq_u32 vcc, -1, v63\n"
        "s_cbranch_scc1 Lslow_%=t\n"
        "s_cbranch_vccz Lslow_%=d\n"
        "s_sleep 1\n"
        "s_branch Lslow_%=p\n"
        "Lslow_%=t:\n"
        "s_mov_b32 %[capv], 0\n"
        "v_mov_b32 v63, 1\n"
        "s_mov_b32 s94, 4227328\n"
        "buffer_store_dword v63, off, %[rs], s94 sc1\n"
        "Lslow_%=d:\n"
        "s_waitcnt lgkmcnt(0)\n"
        "s_barrier\n"
        "s_mul_i32 s92, %[wq], 7168\n"
        "s_add_u32 s93, s92, %[lxc]\n"
        "s_mov_b32 m0, s93\n"
        "s_add_u32 s92, s92, %[bxc]\n"
        "buffer_load_dwordx4 v100, %[rs], s92 offen offset:0 sc1 lds\n"
        "buffer_load_dwordx4 v100, %[rs], s92 offen offset:1024 sc1 lds\n"
        "buffer_load_dwordx4 v100, %[rs], s92 offen offset:2048 sc1 lds\n"
        "buffer_load_dwordx4 v100, %[rs], s92 offen offset:3072 sc1 lds\n"
        "s_add_u32 s93, s93, 4096\n"
        "s_mov_b32 m0, s93\n"
        "s_add_u32 s92, s92, 4096\n"
        "buffer_load_dwordx4 v100, %[rs], s92 offen offset:0 sc1 lds\n"
        "buffer_load_dwordx4 v100, %[rs], s92 offen offset:1024 sc1 lds\n"
        "buffer_load_dwordx4 v100, %[rs], s92 offen offset:2048 sc1 lds\n"
        "s_waitcnt vmcnt(0)\n"
        "s_barrier\n"
        "s_cmp_eq_u32 %[capv], 0\n"
        "s_cselect_b64 %[live], 0, -1\n"
        "s_branch Lplain_%=\n"
        "Lend_%=:\n"
        "s_waitcnt vmcnt(0) lgkmcnt(0)\n"
        "s_endpgm\n"
        : "+{v[96:111]}"(A1), [s] "+s"(s), [bm] "+s"(bm), [bt] "+s"(bt), [bp] "+s"(bp), [bx] "+s"(bx), [lx] "+s"(lx), [bxc] "+s"(bxc), [lxc] "+s"(lxc), [pt] "+s"(pt), [capv] "+s"(capv), [slowc] "+s"(slowc), [da] "+s"(da), [live] "=&s"(live)
        : "{v[112:115]}"(A2), "{s[40:55]}"(M0), "{s[56:71]}"(M1), "{s[72:87]}"(M2), [rs] "s"(rs), [wb] "s"(wb), [wv] "s"(wv), [wq] "s"(wq), [w5] "s"(w5), [d2] "s"(d2)
        : "vcc", "scc", "memory", "v255", "a255", "s88", "s89", "s90", "s91", "s92", "s93", "s94", "s95", "s96", "s97", "s98", "s99");
    __builtin_amdgcn_endpgm();
}
DI void setm(u32x16& m, int i, bool c) { const unsigned long long b = __ballot(c); m[2 * i] = (unsigned)b; m[2 * i + 1] = (unsigned)(b >> 32); }
constexpr unsigned SL_SMT = 28672, SL_ZERO = 55552, SL_X = 65536;
DI void role_S(Ctx& C, int h, int rep) {
    const int lane = C.lane, w = C.w, l15 = lane & 15, g = lane >> 4, qt = w & 1;
    const int fi = 4 * (g & 1) + 2 * (g >> 1) + ((lane >> 3) & 1), fj = lane & 7;
    u32x16 A1, M0, M1, M2; u32x4 A2;
    A1[0] = (lane & 8) ? 0xEEEE : 0x4444;
    A1[1] = 7 * w * 256 + lane * 16;
    A1[2] = (7 * w + 4 + (g < 2 ? g : 2)) * 256 + l15 * 16;
    A1[3] = SL_X + (14 * w + g) * 512 + l15 * 16;
    A1[4] = lane * 16;
    A1[5] = qt * 1024 + lane * 16;
    A1[6] = w < 2 ? OOB : 2048 + (qt * 16 + l15) * 16 + (g & 1) * 8 + (g >> 1) * 4;
    A1[7] = (14 * (lane & 3) + 4 * ((lane >> 2) & 3) + (lane >> 5)) * 512 + ((lane >> 4) & 1) * 256 + l15 * 16 + g * 4;
    A1[8] = fi * 8;
    A1[9] = (((2 * w + (g >> 1)) * 8 + 4 * (g & 1)) * 32 + l15) * 4 + SL_SMT;
    A1[10] = w * 256 + lane * 4;
    A1[11] = w * 3584 + lane * 8;
    A1[12] = w * 3584 + ((lane & 3) * 16 + (lane >> 2)) * 8;
    A1[13] = (lane & 1) ? OOB : (unsigned)(h * 512 + w * 128 + lane * 2);
    A1[14] = w ? OOB : (unsigned)(h * 256 + (fj & 1) * 128 + fi * 16 + (fj >> 1) * 4);
    A1[15] = (lane & 31) ? 64 : (2 * w + (lane >> 5)) * 8;
#pragma unroll
    for (int k = 1; k < 5; ++k) A2[k - 1] = (A1[1] + k * 1024) % 7168;
    setm(M0, 0, (lane & 8) != 0); setm(M0, 1, (fj & 1) != 0); setm(M0, 2, fi == fj - 1); setm(M0, 3, fi == fj); setm(M0, 4, g < 2);
#pragma unroll
    for (int r = 0; r < 4; ++r) {
        const bool vis = 4 * g + r <= l15;
        if (r < 3) setm(M0, 5 + r, vis || w == 1); else setm(M1, 0, vis || w == 1);
        setm(M1, 1 + r, r == 2 ? (h == 0 && w == 3 && lane < 24 && (lane & 7) == 0) : w >= 2);
    }
#pragma unroll
    for (int ks = 3; ks < 14; ++ks) { if (ks < 6) setm(M1, 2 + ks, 14 * w + 4 * ks + g >= 56); else setm(M2, ks - 6, 14 * w + 4 * ks + g >= 56); }
    if (threadIdx.x < 4) sts16(C.sm, SL_ZERO + threadIdx.x * 16, (u32x4){0, 0, 0, 0});
    sts4f(C.sm, 57344 + 3072 + threadIdx.x * 4, -1e30f);
    const unsigned wq = (w + h) & 3, bxc = O_XN + C.layer * XSZ + rep * SEGB;
    s_asm(A1, A2, M0, M1, M2, C.rw, rep, C.hop0 + rep * 16384u, C.hop1 + rep * 16384u, C.hop0 + J_PM * 16384u + rep * 4096u, bxc + wq * 7168u, SL_X + wq * 7168u,
          bxc, SL_X, O_PT + C.layer * PTSZ + ((rep - 2) * 16 + h) * 2560, O_WS + (h * 4 + w) * 100352u, w, wq, w * 5120, O_HOP2 - O_HOP0, O_DBG + C.id * 8192u);
}

DI void v_asm(u32x16 A1, i32x4 rs, unsigned s, unsigned bm, unsigned bt, unsigned bp, unsigned bx, unsigned lx, unsigned bxc, unsigned lxc, unsigned pt, unsigned xr, unsigned x1,
              unsigned wb, unsigned wv, unsigned wq, unsigned d2, unsigned dbg) {
    unsigned capv = SPIN_CAP, slowc = 1u << 16, L = 1520; unsigned long long live;
    asm volatile(
        "s_mov_b64 %[live], -1\n"
        "s_mov_b32 s84, 0xaaaaaaaa\n"
        "s_mov_b32 s85, s84\n"
        "s_mov_b32 m0, %[lx]\n"
        "s_nop 0\n"
        "buffer_load_dwordx4 v100, %[rs], %[bx] offen offset:0 sc1 lds\n"
        "buffer_load_dwordx4 v100, %[rs], %[bx] offen offset:1024 sc1 lds\n"
        "buffer_load_dwordx4 v100, %[rs], %[bx] offen offset:2048 sc1 lds\n"
        "buffer_load_dwordx4 v100, %[rs], %[bx] offen offset:3072 sc1 lds\n"
        "s_add_u32 s95, %[lx], 4096\n"
        "s_mov_b32 m0, s95\n"
        "s_add_u32 s95, %[bx], 4096\n"
        "buffer_load_dwordx4 v100, %[rs], s95 offen offset:0 sc1 lds\n"
        "buffer_load_dwordx4 v100, %[rs], s95 offen offset:1024 sc1 lds\n"
        "buffer_load_dwordx4 v100, %[rs], s95 offen offset:2048 sc1 lds\n"
        "s_mov_b32 s95, %[wb]\n"
        "buffer_load_dwordx4 a[0:3], v100, %[rs], s95 offen offset:0\n"
        "buffer_load_dwordx4 a[4:7], v100, %[rs], s95 offen offset:1024\n"
        "buffer_load_dwordx4 a[8:11], v100, %[rs], s95 offen offset:2048\n"
        "buffer_load_dwordx4 a[12:15], v100, %[rs], s95 offen offset:3072\n"
        "s_add_u32 s95, s95, 4096\n"
        "buffer_load_dwordx4 a[16:19], v100, %[rs], s95 offen offset:0\n"
        "buffer_load_dwordx4 a[20:23], v100, %[rs], s95 offen offset:1024\n"
        "buffer_load_dwordx4 a[24:27], v100, %[rs], s95 offen offset:2048\n"
        "buffer_load_dwordx4 a[28:31], v100, %[rs], s95 offen offset:3072\n"
        "s_add_u32 s95, s95, 4096\n"
        "buffer_load_dwordx4 a[32:35], v100, %[rs], s95 offen offset:0\n"
        "buffer_load_dwordx4 a[36:39], v100, %[rs], s95 offen offset:1024\n"
        "buffer_load_dwordx4 a[40:43], v100, %[rs], s95 offen offset:2048\n"
        "buffer_load_dwordx4 a[44:47], v100, %[rs], s95 offen offset:3072\n"
        "s_add_u32 s95, s95, 4096\n"
        "buffer_load_dwordx4 a[48:51], v100, %[rs], s95 offen offset:0\n"
        "buffer_load_dwordx4 a[52:55], v100, %[rs], s95 offen offset:1024\n"
        "buffer_load_dwordx4 a[56:59], v100, %[rs], s95 offen offset:2048\n"
        "buffer_load_dwordx4 a[60:63], v100, %[rs], s95 offen offset:3072\n"
        "s_add_u32 s95, s95, 4096\n"
        "buffer_load_dwordx4 a[64:67], v100, %[rs], s95 offen offset:0\n"
        "buffer_load_dwordx4 a[68:71], v100, %[rs], s95 offen offset:1024\n"
        "buffer_load_dwordx4 a[72:75], v100, %[rs], s95 offen offset:2048\n"
        "buffer_load_dwordx4 a[76:79], v100, %[rs], s95 offen offset:3072\n"
        "s_add_u32 s95, s95, 4096\n"
        "buffer_load_dwordx4 a[80:83], v100, %[rs], s95 offen offset:0\n"
        "buffer_load_dwordx4 a[84:87], v100, %[rs], s95 offen offset:1024\n"
        "buffer_load_dwordx4 a[88:91], v100, %[rs], s95 offen offset:2048\n"
        "buffer_load_dwordx4 a[92:95], v100, %[rs], s95 offen offset:3072\n"
        "s_add_u32 s95, s95, 4096\n"
        "buffer_load_dwordx4 a[96:99], v100, %[rs], s95 offen offset:0\n"
        "buffer_load_dwordx4 a[100:103], v100, %[rs], s95 offen offset:1024\n"
        "buffer_load_dwordx4 a[104:107], v100, %[rs], s95 offen offset:2048\n"
        "buffer_load_dwordx4 a[108:111], v100, %[rs], s95 offen offset:3072\n"
        "s_add_u32 s95, s95, 4096\n"
        "buffer_load_dwordx4 a[112:115], v100, %[rs], s95 offen offset:0\n"
        "buffer_load_dwordx4 a[116:119], v100, %[rs], s95 offen offset:1024\n"
        "buffer_load_dwordx4 a[120:123], v100, %[rs], s95 offen offset:2048\n"
        "buffer_load_dwordx4 a[124:127], v100, %[rs], s95 offen offset:3072\n"
        "s_add_u32 s95, s95, 4096\n"
        "buffer_load_dwordx4 a[128:131], v100, %[rs], s95 offen offset:0\n"
        "buffer_load_dwordx4 a[132:135], v100, %[rs], s95 offen offset:1024\n"
        "buffer_load_dwordx4 a[136:139], v100, %[rs], s95 offen offset:2048\n"
        "buffer_load_dwordx4 a[140:143], v100, %[rs], s95 offen offset:3072\n"
        "s_add_u32 s95, s95, 4096\n"
        "buffer_load_dwordx4 a[144:147], v100, %[rs], s95 offen offset:0\n"
        "buffer_load_dwordx4 a[148:151], v100, %[rs], s95 offen offset:1024\n"
        "buffer_load_dwordx4 a[152:155], v100, %[rs], s95 offen offset:2048\n"
        "buffer_load_dwordx4 a[156:159], v100, %[rs], s95 offen offset:3072\n"
        "s_add_u32 s95, s95, 4096\n"
        "buffer_load_dwordx4 a[160:163], v100, %[rs], s95 offen offset:0\n"
        "buffer_load_dwordx4 a[164:167], v100, %[rs], s95 offen offset:1024\n"
        "buffer_load_dwordx4 a[168:171], v100, %[rs], s95 offen offset:2048\n"
        "buffer_load_dwordx4 a[172:175], v100, %[rs], s95 offen offset:3072\n"
        "s_add_u32 s95, s95, 4096\n"
        "buffer_load_dwordx4 a[176:179], v100, %[rs], s95 offen offset:0\n"
        "buffer_load_dwordx4 a[180:183], v100, %[rs], s95 offen offset:1024\n"
        "buffer_load_dwordx4 a[184:187], v100, %[rs], s95 offen offset:2048\n"
        "buffer_load_dwordx4 a[188:191], v100, %[rs], s95 offen offset:3072\n"
        "s_add_u32 s95, s95, 4096\n"
        "buffer_load_dwordx4 a[192:195], v100, %[rs], s95 offen offset:0\n"
        "buffer_load_dwordx4 a[196:199], v100, %[rs], s95 offen offset:1024\n"
        "buffer_load_dwordx4 a[200:203], v100, %[rs], s95 offen offset:2048\n"
        "buffer_load_dwordx4 a[204:207], v100, %[rs], s95 offen offset:3072\n"
        "s_add_u32 s95, s95, 4096\n"
        "buffer_load_dwordx4 a[208:211], v100, %[rs], s95 offen offset:0\n"
        "buffer_load_dwordx4 a[212:215], v100, %[rs], s95 offen offset:1024\n"
        "buffer_load_dwordx4 a[216:219], v100, %[rs], s95 offen offset:2048\n"
        "buffer_load_dwordx4 a[220:223], v100, %[rs], s95 offen offset:3072\n"
        "s_add_u32 s95, s95, 4096\n"
        "s_waitcnt vmcnt(56)\n"
        "s_barrier\n"
        "Lfirst_%=:\n"
        "v_add_u32 v223, %[lxc], v103\n"
        "v_mov_b32 v222, 0\n"
        "ds_read_b128 v[116:119], v99 offset:0\n"
        "ds_read_b128 v[120:123], v99 offset:256\n"
        "ds_read_b32 v223, v223\n"
        "s_waitcnt vmcnt(48)\n"
        "s_waitcnt lgkmcnt(1)\n"
        "v_mfma_f32_16x16x32_bf16 v[32:35], v[116:119], a[0:3], 0\n"
        "ds_read_b128 v[124:127], v99 offset:2048\n"
        "v_mov_b64 v[192:193], 0\n"
        "v_mfma_f32_16x16x32_bf16 v[36:39], v[120:123], a[0:3], 0\n"
        "ds_read_b128 v[128:131], v99 offset:2304\n"
        "v_max3_u32 v222, v116, v117, v222\n"
        "v_mfma_f32_16x16x32_bf16 v[40:43], v[116:119], a[8:11], 0\n"
        "v_max3_u32 v222, v118, v119, v222\n"
        "v_max3_u32 v222, v120, v121, v222\n"
        "v_mfma_f32_16x16x32_bf16 v[44:47], v[120:123], a[8:11], 0\n"
        "v_max3_u32 v222, v122, v123, v222\n"
        "v_mov_b64 v[194:195], 0\n"
        "v_mfma_f32_16x16x32_bf16 v[48:51], v[116:119], a[16:19], 0\n"
        "s_waitcnt lgkmcnt(2)\n"
        "v_cmp_eq_u32 vcc, -1, v223\n"
        "v_mov_b64 v[196:197], 0\n"
        "v_mfma_f32_16x16x32_bf16 v[52:55], v[120:123], a[16:19], 0\n"
        "s_and_b64 vcc, vcc, %[live]\n"
        "s_cbranch_vccnz Lslowa_%=\n"
        "v_mov_b64 v[198:199], 0\n"
        "v_mov_b64 v[200:201], 0\n"
        "v_mfma_f32_16x16x32_bf16 v[56:59], v[116:119], a[24:27], 0\n"
        "v_mov_b64 v[202:203], 0\n"
        "v_mov_b64 v[204:205], 0\n"
        "v_mfma_f32_16x16x32_bf16 v[60:63], v[120:123], a[24:27], 0\n"
        "v_mov_b64 v[206:207], 0\n"
        "v_mov_b64 v[208:209], 0\n"
        "s_waitcnt lgkmcnt(0)\n"
        "v_mfma_f32_16x16x32_bf16 v[32:35], v[124:127], a[4:7], v[32:35]\n"
        "ds_read_b128 v[116:119], v99 offset:4096\n"
        "v_mov_b64 v[210:211], 0\n"
        "v_mfma_f32_16x16x32_bf16 v[36:39], v[128:131], a[4:7], v[36:39]\n"
        "ds_read_b128 v[120:123], v99 offset:4352\n"
        "v_max3_u32 v222, v124, v125, v222\n"
        "v_mfma_f32_16x16x32_bf16 v[40:43], v[124:127], a[12:15], v[40:43]\n"
        "v_max3_u32 v222, v126, v127, v222\n"
        "v_max3_u32 v222, v128, v129, v222\n"
        "v_mfma_f32_16x16x32_bf16 v[44:47], v[128:131], a[12:15], v[44:47]\n"
        "v_max3_u32 v222, v130, v131, v222\n"
        "v_mov_b64 v[212:213], 0\n"
        "v_mfma_f32_16x16x32_bf16 v[48:51], v[124:127], a[20:23], v[48:51]\n"
        "v_mov_b64 v[214:215], 0\n"
        "v_mfma_f32_16x16x32_bf16 v[52:55], v[128:131], a[20:23], v[52:55]\n"
        "s_add_u32 s92, %[bxc], 57344\n"
        "s_cmp_lt_u32 %[s], 62\n"
        "s_cselect_b32 s87, s92, %[bxc]\n"
        "s_xor_b32 s92, %[lxc], 32768\n"
        "s_mul_i32 s95, %[wq], 7168\n"
        "s_add_u32 %[lx], s95, s92\n"
        "s_add_u32 %[bx], s95, s87\n"
        "v_mfma_f32_16x16x32_bf16 v[56:59], v[124:127], a[28:31], v[56:59]\n"
        "v_mfma_f32_16x16x32_bf16 v[60:63], v[128:131], a[28:31], v[60:63]\n"
        "s_waitcnt vmcnt(40)\n"
        "s_waitcnt lgkmcnt(0)\n"
        "v_mfma_f32_16x16x32_bf16 v[32:35], v[116:119], a[32:35], v[32:35]\n"
        "ds_read_b128 v[124:127], v99 offset:6144\n"
        "v_mfma_f32_16x16x32_bf16 v[36:39], v[120:123], a[32:35], v[36:39]\n"
        "ds_read_b128 v[128:131], v99 offset:6400\n"
        "v_max3_u32 v222, v116, v117, v222\n"
        "v_mfma_f32_16x16x32_bf16 v[40:43], v[116:119], a[40:43], v[40:43]\n"
        "v_max3_u32 v222, v118, v119, v222\n"
        "v_max3_u32 v222, v120, v121, v222\n"
        "v_mfma_f32_16x16x32_bf16 v[44:47], v[120:123], a[40:43], v[44:47]\n"
        "v_max3_u32 v222, v122, v123, v222\n"
        "v_mfma_f32_16x16x32_bf16 v[48:51], v[116:119], a[48:51], v[48:51]\n"
        "v_mfma_f32_16x16x32_bf16 v[52:55], v[120:123], a[48:51], v[52:55]\n"
        "v_mfma_f32_16x16x32_bf16 v[56:59], v[116:119], a[56:59], v[56:59]\n"
        "v_mfma_f32_16x16x32_bf16 v[60:63], v[120:123], a[56:59], v[60:63]\n"
        "s_waitcnt lgkmcnt(0)\n"
        "v_mfma_f32_16x16x32_bf16 v[32:35], v[124:127], a[36:39], v[32:35]\n"
        "ds_read_b128 v[116:119], v99 offset:8192\n"
        "v_mfma_f32_16x16x32_bf16 v[36:39], v[128:131], a[36:39], v[36:39]\n"
        "ds_read_b128 v[120:123], v99 offset:8448\n"
        "v_max3_u32 v222, v124, v125, v222\n"
        "v_mfma_f32_16x16x32_bf16 v[40:43], v[124:127], a[44:47], v[40:43]\n"
        "v_max3_u32 v222, v126, v127, v222\n"
        "v_max3_u32 v222, v128, v129, v222\n"
        "v_mfma_f32_16x16x32_bf16 v[44:47], v[128:131], a[44:47], v[44:47]\n"
        "v_max3_u32 v222, v130, v131, v222\n"
        "v_mfma_f32_16x16x32_bf16 v[48:51], v[124:127], a[52:55], v[48:51]\n"
        "v_mfma_f32_16x16x32_bf16 v[52:55], v[128:131], a[52:55], v[52:55]\n"
        "v_mfma_f32_16x16x32_bf16 v[56:59], v[124:127], a[60:63], v[56:59]\n"
        "v_mfma_f32_16x16x32_bf16 v[60:63], v[128:131], a[60:63], v[60:63]\n"
        "s_waitcnt vmcnt(32)\n"
        "s_waitcnt lgkmcnt(0)\n"
        "v_mfma_f32_16x16x32_bf16 v[32:35], v[116:119], a[64:67], v[32:35]\n"
        "ds_read_b128 v[124:127], v99 offset:10240\n"
        "v_mfma_f32_16x16x32_bf16 v[36:39], v[120:123], a[64:67], v[36:39]\n"
        "ds_read_b128 v[128:131], v99 offset:10496\n"
        "v_max3_u32 v222, v116, v117, v222\n"
        "v_mfma_f32_16x16x32_bf16 v[40:43], v[116:119], a[72:75], v[40:43]\n"
        "v_max3_u32 v222, v118, v119, v222\n"
        "v_max3_u32 v222, v120, v121, v222\n"
        "v_mfma_f32_16x16x32_bf16 v[44:47], v[120:123], a[72:75], v[44:47]\n"
        "v_max3_u32 v222, v122, v123, v222\n"
        "v_mfma_f32_16x16x32_bf16 v[48:51], v[116:119], a[80:83], v[48:51]\n"
        "v_mfma_f32_16x16x32_bf16 v[52:55], v[120:123], a[80:83], v[52:55]\n"
        "v_mfma_f32_16x16x32_bf16 v[56:59], v[116:119], a[88:91], v[56:59]\n"
        "v_mfma_f32_16x16x32_bf16 v[60:63], v[120:123], a[88:91], v[60:63]\n"
        "s_waitcnt lgkmcnt(0)\n"
        "v_mfma_f32_16x16x32_bf16 v[32:35], v[124:127], a[68:71], v[32:35]\n"
        "ds_read_b128 v[116:119], v99 offset:12288\n"
        "v_mfma_f32_16x16x32_bf16 v[36:39], v[128:131], a[68:71], v[36:39]\n"
        "ds_read_b128 v[120:123], v99 offset:12544\n"
        "v_max3_u32 v222, v124, v125, v222\n"
        "v_mfma_f32_16x16x32_bf16 v[40:43], v[124:127], a[76:79], v[40:43]\n"
        "v_max3_u32 v222, v126, v127, v222\n"
        "v_max3_u32 v222, v128, v129, v222\n"
        "v_mfma_f32_16x16x32_bf16 v[44:47], v[128:131], a[76:79], v[44:47]\n"
        "v_max3_u32 v222, v130, v131, v222\n"
        "v_mfma_f32_16x16x32_bf16 v[48:51], v[124:127], a[84:87], v[48:51]\n"
        "v_mfma_f32_16x16x32_bf16 v[52:55], v[128:131], a[84:87], v[52:55]\n"
        "v_mfma_f32_16x16x32_bf16 v[56:59], v[124:127], a[92:95], v[56:59]\n"
        "v_mfma_f32_16x16x32_bf16 v[60:63], v[128:131], a[92:95], v[60:63]\n"
        "s_waitcnt vmcnt(24)\n"
        "s_waitcnt lgkmcnt(0)\n"
        "v_mfma_f32_16x16x32_bf16 v[32:35], v[116:119], a[96:99], v[32:35]\n"
        "ds_read_b128 v[124:127], v99 offset:14336\n"
        "v_mfma_f32_16x16x32_bf16 v[36:39], v[120:123], a[96:99], v[36:39]\n"
        "ds_read_b128 v[128:131], v99 offset:14592\n"
        "v_max3_u32 v222, v116, v117, v222\n"
        "v_mfma_f32_16x16x32_bf16 v[40:43], v[116:119], a[104:107], v[40:43]\n"
        "v_max3_u32 v222, v118, v119, v222\n"
        "v_max3_u32 v222, v120, v121, v222\n"
        "v_mfma_f32_16x16x32_bf16 v[44:47], v[120:123], a[104:107], v[44:47]\n"
        "v_max3_u32 v222, v122, v123, v222\n"
        "v_mfma_f32_16x16x32_bf16 v[48:51], v[116:119], a[112:115], v[48:51]\n"
        "v_mfma_f32_16x16x32_bf16 v[52:55], v[120:123], a[112:115], v[52:55]\n"
        "v_mfma_f32_16x16x32_bf16 v[56:59], v[116:119], a[120:123], v[56:59]\n"
        "v_mfma_f32_16x16x32_bf16 v[60:63], v[120:123], a[120:123], v[60:63]\n"
        "s_waitcnt lgkmcnt(0)\n"
        "v_mfma_f32_16x16x32_bf16 v[32:35], v[124:127], a[100:103], v[32:35]\n"
        "ds_read_b128 v[116:119], v99 offset:16384\n"
        "v_mfma_f32_16x16x32_bf16 v[36:39], v[128:131], a[100:103], v[36:39]\n"
        "ds_read_b128 v[120:123], v99 offset:16640\n"
        "v_max3_u32 v222, v124, v125, v222\n"
        "v_mfma_f32_16x16x32_bf16 v[40:43], v[124:127], a[108:111], v[40:43]\n"
        "v_max3_u32 v222, v126, v127, v222\n"
        "v_max3_u32 v222, v128, v129, v222\n"
        "v_mfma_f32_16x16x32_bf16 v[44:47], v[128:131], a[108:111], v[44:47]\n"
        "v_max3_u32 v222, v130, v131, v222\n"
        "v_mfma_f32_16x16x32_bf16 v[48:51], v[124:127], a[116:119], v[48:51]\n"
        "v_mfma_f32_16x16x32_bf16 v[52:55], v[128:131], a[116:119], v[52:55]\n"
        "v_mfma_f32_16x16x32_bf16 v[56:59], v[124:127], a[124:127], v[56:59]\n"
        "v_mfma_f32_16x16x32_bf16 v[60:63], v[128:131], a[124:127], v[60:63]\n"
        "s_waitcnt vmcnt(16)\n"
        "s_waitcnt lgkmcnt(0)\n"
        "v_mfma_f32_16x16x32_bf16 v[32:35], v[116:119], a[128:131], v[32:35]\n"
        "ds_read_b128 v[124:127], v99 offset:18432\n"
        "v_mfma_f32_16x16x32_bf16 v[36:39], v[120:123], a[128:131], v[36:39]\n"
        "ds_read_b128 v[128:131], v99 offset:18688\n"
        "v_max3_u32 v222, v116, v117, v222\n"
        "v_mfma_f32_16x16x32_bf16 v[40:43], v[116:119], a[136:139], v[40:43]\n"
        "v_max3_u32 v222, v118, v119, v222\n"
        "v_max3_u32 v222, v120, v121, v222\n"
        "v_mfma_f32_16x16x32_bf16 v[44:47], v[120:123], a[136:139], v[44:47]\n"
        "v_max3_u32 v222, v122, v123, v222\n"
        "v_mfma_f32_16x16x32_bf16 v[48:51], v[116:119], a[144:147], v[48:51]\n"
        "v_mfma_f32_16x16x32_bf16 v[52:55], v[120:123], a[144:147], v[52:55]\n"
        "v_mfma_f32_16x16x32_bf16 v[56:59], v[116:119], a[152:155], v[56:59]\n"
        "v_mfma_f32_16x16x32_bf16 v[60:63], v[120:123], a[152:155], v[60:63]\n"
        "s_waitcnt lgkmcnt(0)\n"
        "v_mfma_f32_16x16x32_bf16 v[32:35], v[124:127], a[132:135], v[32:35]\n"
        "ds_read_b128 v[116:119], v99 offset:20480\n"
        "v_mfma_f32_16x16x32_bf16 v[36:39], v[128:131], a[132:135], v[36:39]\n"
        "ds_read_b128 v[120:123], v99 offset:20736\n"
        "v_max3_u32 v222, v124, v125, v222\n"
        "v_mfma_f32_16x16x32_bf16 v[40:43], v[124:127], a[140:143], v[40:43]\n"
        "v_max3_u32 v222, v126, v127, v222\n"
        "v_max3_u32 v222, v128, v129, v222\n"
        "v_mfma_f32_16x16x32_bf16 v[44:47], v[128:131], a[140:143], v[44:47]\n"
        "v_max3_u32 v222, v130, v131, v222\n"
        "v_mfma_f32_16x16x32_bf16 v[48:51], v[124:127], a[148:151], v[48:51]\n"
        "v_mfma_f32_16x16x32_bf16 v[52:55], v[128:131], a[148:151], v[52:55]\n"
        "v_mfma_f32_16x16x32_bf16 v[56:59], v[124:127], a[156:159], v[56:59]\n"
        "v_mfma_f32_16x16x32_bf16 v[60:63], v[128:131], a[156:159], v[60:63]\n"
        "s_waitcnt vmcnt(8)\n"
        "s_waitcnt lgkmcnt(0)\n"
        "v_mfma_f32_16x16x32_bf16 v[32:35], v[116:119], a[160:163], v[32:35]\n"
        "ds_read_b128 v[124:127], v99 offset:22528\n"
        "v_mfma_f32_16x16x32_bf16 v[36:39], v[120:123], a[160:163], v[36:39]\n"
        "ds_read_b128 v[128:131], v99 offset:22784\n"
        "v_max3_u32 v222, v116, v117, v222\n"
        "v_mfma_f32_16x16x32_bf16 v[40:43], v[116:119], a[168:171], v[40:43]\n"
        "v_max3_u32 v222, v118, v119, v222\n"
        "v_max3_u32 v222, v120, v121, v222\n"
        "v_mfma_f32_16x16x32_bf16 v[44:47], v[120:123], a[168:171], v[44:47]\n"
        "v_max3_u32 v222, v122, v123, v222\n"
        "v_mfma_f32_16x16x32_bf16 v[48:51], v[116:119], a[176:179], v[48:51]\n"
        "v_mfma_f32_16x16x32_bf16 v[52:55], v[120:123], a[176:179], v[52:55]\n"
        "v_mfma_f32_16x16x32_bf16 v[56:59], v[116:119], a[184:187], v[56:59]\n"
        "v_mfma_f32_16x16x32_bf16 v[60:63], v[120:123], a[184:187], v[60:63]\n"
        "s_waitcnt lgkmcnt(0)\n"
        "v_mfma_f32_16x16x32_bf16 v[32:35], v[124:127], a[164:167], v[32:35]\n"
        "ds_read_b128 v[116:119], v99 offset:24576\n"
        "v_mfma_f32_16x16x32_bf16 v[36:39], v[128:131], a[164:167], v[36:39]\n"
        "ds_read_b128 v[120:123], v99 offset:24832\n"
        "v_max3_u32 v222, v124, v125, v222\n"
        "v_mfma_f32_16x16x32_bf16 v[40:43], v[124:127], a[172:175], v[40:43]\n"
        "v_max3_u32 v222, v126, v127, v222\n"
        "v_max3_u32 v222, v128, v129, v222\n"
        "v_mfma_f32_16x16x32_bf16 v[44:47], v[128:131], a[172:175], v[44:47]\n"
        "v_max3_u32 v222, v130, v131, v222\n"
        "v_mfma_f32_16x16x32_bf16 v[48:51], v[124:127], a[180:183], v[48:51]\n"
        "v_mfma_f32_16x16x32_bf16 v[52:55], v[128:131], a[180:183], v[52:55]\n"
        "v_mfma_f32_16x16x32_bf16 v[56:59], v[124:127], a[188:191], v[56:59]\n"
        "v_mfma_f32_16x16x32_bf16 v[60:63], v[128:131], a[188:191], v[60:63]\n"
        "s_waitcnt vmcnt(0)\n"
        "s_waitcnt lgkmcnt(0)\n"
        "v_mfma_f32_16x16x32_bf16 v[32:35], v[116:119], a[192:195], v[32:35]\n"
        "ds_read_b128 v[124:127], v99 offset:26624\n"
        "v_mfma_f32_16x16x32_bf16 v[36:39], v[120:123], a[192:195], v[36:39]\n"
        "ds_read_b128 v[128:131], v99 offset:26880\n"
        "v_max3_u32 v222, v116, v117, v222\n"
        "v_mfma_f32_16x16x32_bf16 v[40:43], v[116:119], a[200:203], v[40:43]\n"
        "v_max3_u32 v222, v118, v119, v222\n"
        "v_max3_u32 v222, v120, v121, v222\n"
        "v_mfma_f32_16x16x32_bf16 v[44:47], v[120:123], a[200:203], v[44:47]\n"
        "v_max3_u32 v222, v122, v123, v222\n"
        "v_mfma_f32_16x16x32_bf16 v[48:51], v[116:119], a[208:211], v[48:51]\n"
        "v_mfma_f32_16x16x32_bf16 v[52:55], v[120:123], a[208:211], v[52:55]\n"
        "v_mfma_f32_16x16x32_bf16 v[56:59], v[116:119], a[216:219], v[56:59]\n"
        "v_mfma_f32_16x16x32_bf16 v[60:63], v[120:123], a[216:219], v[60:63]\n"
        "s_waitcnt lgkmcnt(0)\n"
        "v_mfma_f32_16x16x32_bf16 v[32:35], v[124:127], a[196:199], v[32:35]\n"
        "v_max3_u32 v222, v124, v125, v222\n"
        "v_max3_u32 v222, v126, v127, v222\n"
        "v_mfma_f32_16x16x32_bf16 v[36:39], v[128:131], a[196:199], v[36:39]\n"
        "v_max3_u32 v222, v128, v129, v222\n"
        "v_max3_u32 v222, v130, v131, v222\n"
        "v_mfma_f32_16x16x32_bf16 v[40:43], v[124:127], a[204:207], v[40:43]\n"
        "v_cmp_eq_u32 vcc, -1, v222\n"
        "v_mfma_f32_16x16x32_bf16 v[44:47], v[128:131], a[204:207], v[44:47]\n"
        "s_and_b64 vcc, vcc, %[live]\n"
        "s_cbranch_vccnz Lslowa_%=\n"
        "v_mfma_f32_16x16x32_bf16 v[48:51], v[124:127], a[212:215], v[48:51]\n"
        "v_mfma_f32_16x16x32_bf16 v[52:55], v[128:131], a[212:215], v[52:55]\n"
        "v_mfma_f32_16x16x32_bf16 v[56:59], v[124:127], a[220:223], v[56:59]\n"
        "v_mfma_f32_16x16x32_bf16 v[60:63], v[128:131], a[220:223], v[60:63]\n"
        "s_mov_b32 m0, %[lx]\n"
        "s_nop 0\n"
        "buffer_load_dwordx4 v100, %[rs], %[bx] offen offset:0 sc1 lds\n"
        "buffer_load_dwordx4 v100, %[rs], %[bx] offen offset:1024 sc1 lds\n"
        "buffer_load_dwordx4 v100, %[rs], %[bx] offen offset:2048 sc1 lds\n"
        "buffer_load_dwordx4 v100, %[rs], %[bx] offen offset:3072 sc1 lds\n"
        "s_add_u32 s95, %[lx], 4096\n"
        "s_mov_b32 m0, s95\n"
        "s_add_u32 s95, %[bx], 4096\n"
        "buffer_load_dwordx4 v100, %[rs], s95 offen offset:0 sc1 lds\n"
        "buffer_load_dwordx4 v100, %[rs], s95 offen offset:1024 sc1 lds\n"
        "buffer_load_dwordx4 v100, %[rs], s95 offen offset:2048 sc1 lds\n"
        "s_mov_b32 %[bxc], s87\n"
        "s_xor_b32 %[lxc], %[lxc], 32768\n"
        "v_xor_b32 v99, 32768, v99\n"
        "s_mov_b32 s91, -1\n"
        "v_cvt_pk_bf16_f32 v80, v32, v33\n"
        "v_cvt_pk_bf16_f32 v82, v34, v35\n"
        "v_cvt_pk_bf16_f32 v84, v36, v37\n"
        "v_cvt_pk_bf16_f32 v86, v38, v39\n"
        "v_cvt_pk_bf16_f32 v81, v40, v41\n"
        "v_cvt_pk_bf16_f32 v83, v42, v43\n"
        "v_cvt_pk_bf16_f32 v85, v44, v45\n"
        "v_cvt_pk_bf16_f32 v87, v46, v47\n"
        "v_cvt_pk_bf16_f32 v88, v48, v49\n"
        "v_cvt_pk_bf16_f32 v90, v50, v51\n"
        "v_cvt_pk_bf16_f32 v92, v52, v53\n"
        "v_cvt_pk_bf16_f32 v94, v54, v55\n"
        "v_cvt_pk_bf16_f32 v89, v56, v57\n"
        "v_cvt_pk_bf16_f32 v91, v58, v59\n"
        "v_cvt_pk_bf16_f32 v93, v60, v61\n"
        "v_cvt_pk_bf16_f32 v95, v62, v63\n"
        "v_cvt_pk_bf16_f32 v64, v32, v33\n"
        "v_cvt_pk_bf16_f32 v65, v34, v35\n"
        "v_cvt_pk_bf16_f32 v66, v36, v37\n"
        "v_cvt_pk_bf16_f32 v67, v38, v39\n"
        "v_cvt_pk_bf16_f32 v68, v40, v41\n"
        "v_cvt_pk_bf16_f32 v69, v42, v43\n"
        "v_cvt_pk_bf16_f32 v70, v44, v45\n"
        "v_cvt_pk_bf16_f32 v71, v46, v47\n"
        "v_cvt_pk_bf16_f32 v72, v48, v49\n"
        "v_cvt_pk_bf16_f32 v73, v50, v51\n"
        "v_cvt_pk_bf16_f32 v74, v52, v53\n"
        "v_cvt_pk_bf16_f32 v75, v54, v55\n"
        "v_cvt_pk_bf16_f32 v76, v56, v57\n"
        "v_cvt_pk_bf16_f32 v77, v58, v59\n"
        "v_cvt_pk_bf16_f32 v78, v60, v61\n"
        "v_cvt_pk_bf16_f32 v79, v62, v63\n"
        "s_waitcnt vmcnt(0)\n"
        "Ltop_%=:\n"
        "s_cmp_eq_u32 %[s], 0\n"
        "s_cselect_b32 s91, 0, s91\n"
        "s_sub_u32 s92, %[bm], 9212\n"
        "s_cmp_lg_u32 s91, -1\n"
        "s_cbranch_scc1 Lbcd_%=\n"
        "s_mov_b32 s93, %[capv]\n"
        "Lbc_%=:\n"
        "buffer_load_dword v174, off, %[rs], s92 sc1\n"
        "s_sub_u32 s93, s93, 1\n"
        "s_cbranch_scc1 Lbcd_%=\n"
        "s_waitcnt vmcnt(0)\n"
        "v_readfirstlane_b32 s91, v174\n"
        "s_cmp_lg_u32 s91, -1\n"
        "s_cbranch_scc1 Lbcd_%=\n"
        "s_sleep 3\n"
        "s_branch Lbc_%=\n"
        "Lbcd_%=:\n"
        "s_add_u32 s91, s91, 16\n"
        "Lbw_%=:\n"
        "s_memrealtime s[96:97]\n"
        "s_waitcnt lgkmcnt(0)\n"
        "s_lshl_b32 s96, s96, 4\n"
        "s_sub_u32 s96, s91, s96\n"
        "s_sub_u32 s97, s96, 144\n"
        "s_cmp_lt_u32 s97, 9600\n"
        "s_cbranch_scc1 Lbw_%=\n"
        "s_min_i32 s97, s96, 144\n"
        "Lbwf_%=:\n"
        "s_sub_i32 s97, s97, 21\n"
        "s_cmp_gt_i32 s97, 60\n"
        "s_cbranch_scc1 Lbwf_%=\n"
        "s_mov_b32 s90, s91\n"
        "s_cmp_eq_u32 %[s], 0\n"
        "s_cselect_b32 s88, 1, 0\n"
        "s_and_b32 s90, s90, -2\n"
        "s_or_b32 s90, s90, s88\n"
        "s_mov_b32 s93, %[capv]\n"
        "s_cmp_lt_u32 %[wv], 2\n"
        "s_cbranch_scc1 Lmb_%=\n"
        "s_mov_b32 s93, %[capv]\n"
        "Lm_%=:\n"
        "buffer_load_dwordx4 v[16:19], v97, %[rs], %[bm] offen offset:0 sc1\n"
        "buffer_load_dwordx4 v[20:23], v97, %[rs], %[bm] offen offset:1024 sc1\n"
        "buffer_load_dwordx4 v[24:27], v97, %[rs], %[bm] offen offset:2048 sc1\n"
        "buffer_load_dwordx4 v[28:31], v98, %[rs], %[bm] offen offset:0 sc1\n"
        "s_sub_u32 s93, s93, 1\n"
        "s_waitcnt vmcnt(3)\n"
        "ds_write_b128 v97, v[16:19] offset:28672\n"
        "v_max3_u32 v8, v16, v17, 0\n"
        "v_max3_u32 v8, v18, v19, v8\n"
        "s_waitcnt vmcnt(2)\n"
        "ds_write_b128 v97, v[20:23] offset:29696\n"
        "v_max3_u32 v8, v20, v21, v8\n"
        "v_max3_u32 v8, v22, v23, v8\n"
        "s_waitcnt vmcnt(1)\n"
        "ds_write_b128 v97, v[24:27] offset:30720\n"
        "v_max3_u32 v8, v24, v25, v8\n"
        "v_max3_u32 v8, v26, v27, v8\n"
        "s_waitcnt vmcnt(0)\n"
        "ds_write_b128 v98, v[28:31] offset:28672\n"
        "v_max3_u32 v8, v28, v29, v8\n"
        "v_max3_u32 v8, v30, v31, v8\n"
        "v_cmp_eq_u32 vcc, -1, v8\n"
        "s_cbranch_vccnz Lmr_%=\n"
        "Lmg_%=:\n"
        "v_mov_b32 v8, s90\n"
        "s_lshl_b32 s92, %[wv], 2\n"
        "v_mov_b32 v9, s92\n"
        "ds_write_b32 v9, v8 offset:38912\n"
        "Lmb_%=:\n"
        "v_mov_b32 v9, 0\n"
        "v_add_u32 v223, %[lxc], v103\n"
        "s_waitcnt lgkmcnt(0)\n"
        "s_barrier\n"
        "ds_read_b128 v[32:35], v100 offset:28672\n"
        "ds_read_b128 v[36:39], v100 offset:29696\n"
        "ds_read_b128 v[40:43], v100 offset:30720\n"
        "ds_read_b128 v[44:47], v100 offset:31744\n"
        "ds_read_b128 v[48:51], v100 offset:32768\n"
        "ds_read_b128 v[52:55], v100 offset:33792\n"
        "ds_read_b128 v[56:59], v100 offset:34816\n"
        "ds_read_b32 v223, v223\n"
        "ds_read_b64 v[8:9], v9 offset:38920\n"
        "s_waitcnt lgkmcnt(8)\n"
        "v_smfmac_f32_16x16x64_bf16 v[192:195], v[32:35], a[0:7], v96\n"
        "v_smfmac_f32_16x16x64_bf16 v[196:199], v[32:35], a[8:15], v96\n"
        "s_waitcnt lgkmcnt(7)\n"
        "v_smfmac_f32_16x16x64_bf16 v[192:195], v[36:39], a[32:39], v96\n"
        "v_smfmac_f32_16x16x64_bf16 v[196:199], v[36:39], a[40:47], v96\n"
        "s_waitcnt lgkmcnt(6)\n"
        "v_smfmac_f32_16x16x64_bf16 v[192:195], v[40:43], a[64:71], v96\n"
        "v_smfmac_f32_16x16x64_bf16 v[196:199], v[40:43], a[72:79], v96\n"
        "s_waitcnt lgkmcnt(5)\n"
        "v_smfmac_f32_16x16x64_bf16 v[192:195], v[44:47], a[96:103], v96\n"
        "v_smfmac_f32_16x16x64_bf16 v[196:199], v[44:47], a[104:111], v96\n"
        "s_waitcnt lgkmcnt(4)\n"
        "v_smfmac_f32_16x16x64_bf16 v[192:195], v[48:51], a[128:135], v96\n"
        "v_smfmac_f32_16x16x64_bf16 v[196:199], v[48:51], a[136:143], v96\n"
        "s_waitcnt lgkmcnt(3)\n"
        "v_smfmac_f32_16x16x64_bf16 v[192:195], v[52:55], a[160:167], v96\n"
        "v_smfmac_f32_16x16x64_bf16 v[196:199], v[52:55], a[168:175], v96\n"
        "s_waitcnt lgkmcnt(2)\n"
        "v_smfmac_f32_16x16x64_bf16 v[192:195], v[56:59], a[192:199], v96\n"
        "v_smfmac_f32_16x16x64_bf16 v[196:199], v[56:59], a[200:207], v96\n"
        "s_waitcnt lgkmcnt(0)\n"
        "v_cmp_eq_u32 vcc, -1, v223\n"
        "s_mov_b32 s86, 0\n"
        "s_and_b64 vcc, vcc, %[live]\n"
        "s_cbranch_vccnz Lsc_%=\n"
        "Lscd_%=:\n"
        "v_smfmac_f32_16x16x64_bf16 v[200:203], v[32:35], a[16:23], v96\n"
        "v_readfirstlane_b32 s88, v8\n"
        "v_smfmac_f32_16x16x64_bf16 v[204:207], v[32:35], a[24:31], v96\n"
        "v_readfirstlane_b32 s89, v9\n"
        "v_smfmac_f32_16x16x64_bf16 v[200:203], v[36:39], a[48:55], v96\n"
        "s_add_u32 s92, %[bm], 7168\n"
        "v_permlane32_swap_b32 v192, v196\n"
        "v_smfmac_f32_16x16x64_bf16 v[204:207], v[36:39], a[56:63], v96\n"
        "v_permlane32_swap_b32 v193, v197\n"
        "v_smfmac_f32_16x16x64_bf16 v[200:203], v[40:43], a[80:87], v96\n"
        "v_permlane32_swap_b32 v194, v198\n"
        "v_smfmac_f32_16x16x64_bf16 v[204:207], v[40:43], a[88:95], v96\n"
        "v_permlane32_swap_b32 v195, v199\n"
        "v_smfmac_f32_16x16x64_bf16 v[200:203], v[44:47], a[112:119], v96\n"
        "v_add_f32 v192, v192, v196\n"
        "v_smfmac_f32_16x16x64_bf16 v[204:207], v[44:47], a[120:127], v96\n"
        "v_add_f32 v193, v193, v197\n"
        "v_smfmac_f32_16x16x64_bf16 v[200:203], v[48:51], a[144:151], v96\n"
        "v_add_f32 v194, v194, v198\n"
        "v_smfmac_f32_16x16x64_bf16 v[204:207], v[48:51], a[152:159], v96\n"
        "v_add_f32 v195, v195, v199\n"
        "v_smfmac_f32_16x16x64_bf16 v[200:203], v[52:55], a[176:183], v96\n"
        "v_cvt_pk_bf16_f32 v112, v192, v193\n"
        "v_smfmac_f32_16x16x64_bf16 v[204:207], v[52:55], a[184:191], v96\n"
        "v_cvt_pk_bf16_f32 v113, v194, v195\n"
        "v_smfmac_f32_16x16x64_bf16 v[200:203], v[56:59], a[208:215], v96\n"
        "v_smfmac_f32_16x16x64_bf16 v[204:207], v[56:59], a[216:223], v96\n"
        "s_cmp_eq_u32 %[s], 63\n"
        "s_cbranch_scc1 Llast_%=\n"
        "s_sub_u32 s96, s88, s89\n"
        "s_cmp_gt_i32 s96, 0\n"
        "s_cselect_b32 s90, s88, s89\n"
        "s_or_b32 s88, s88, s89\n"
        "s_add_u32 s94, s90, %[L]\n"
        "s_sub_u32 s89, s94, 320\n"
        "s_add_u32 s96, s94, 192\n"
        "s_or_b32 s96, s96, 1\n"
        "v_mov_b32 v174, s96\n"
        "s_mov_b32 s83, 0\n"
        "s_mov_b32 s82, s89\n"
        "Lub_%=:\n"
        "s_memrealtime s[96:97]\n"
        "s_waitcnt lgkmcnt(0)\n"
        "s_lshl_b32 s96, s96, 4\n"
        "s_sub_u32 s96, s89, s96\n"
        "s_sub_u32 s97, s96, 144\n"
        "s_cmp_lt_u32 s97, 9600\n"
        "s_cbranch_scc1 Lub_%=\n"
        "s_min_i32 s97, s96, 144\n"
        "Lubf_%=:\n"
        "s_sub_i32 s97, s97, 21\n"
        "s_cmp_gt_i32 s97, 60\n"
        "s_cbranch_scc1 Lubf_%=\n"
        "s_cmp_lt_i32 s96, -64\n"
        "s_cbranch_scc1 Lnbl_%=\n"
        "buffer_load_dword v174, off, %[rs], s92 sc1\n"
        "Lnbl_%=:\n"
        "s_nop 1\n"
        "v_permlane32_swap_b32 v200, v204\n"
        "v_permlane32_swap_b32 v201, v205\n"
        "v_permlane32_swap_b32 v202, v206\n"
        "v_permlane32_swap_b32 v203, v207\n"
        "v_add_f32 v200, v200, v204\n"
        "v_add_f32 v201, v201, v205\n"
        "v_add_f32 v202, v202, v206\n"
        "v_add_f32 v203, v203, v207\n"
        "v_cvt_pk_bf16_f32 v114, v200, v201\n"
        "v_cvt_pk_bf16_f32 v115, v202, v203\n"
        "v_mov_b32 v0, v112\n"
        "v_mov_b32 v1, v114\n"
        "v_mov_b32 v2, v113\n"
        "v_mov_b32 v3, v115\n"
        "v_mov_b64 v[12:13], 0\n"
        "v_mov_b64 v[14:15], 0\n"
        "ds_write_b128 v110, v[0:3] offset:40960\n"
        "v_add_u32 v223, %[lxc], v103\n"
        "v_mov_b32 v222, 0\n"
        "ds_read_b128 v[116:119], v99 offset:0\n"
        "ds_read_b128 v[120:123], v99 offset:256\n"
        "ds_read_b32 v223, v223\n"
        "Lu0_%=:\n"
        "s_memrealtime s[96:97]\n"
        "s_waitcnt lgkmcnt(0)\n"
        "s_lshl_b32 s96, s96, 4\n"
        "s_sub_u32 s96, s94, s96\n"
        "s_sub_u32 s97, s96, 144\n"
        "s_cmp_lt_u32 s97, 9600\n"
        "s_cbranch_scc1 Lu0_%=\n"
        "s_min_i32 s97, s96, 144\n"
        "Lu0f_%=:\n"
        "s_sub_i32 s97, s97, 21\n"
        "s_cmp_gt_i32 s97, 60\n"
        "s_cbranch_scc1 Lu0f_%=\n"
        "buffer_load_dwordx4 v[24:27], v101, %[rs], %[bt] offen sc1\n"
        "buffer_load_dwordx4 v[28:31], v101, %[rs], %[bt] offen offset:1024 sc1\n"
        "s_waitcnt vmcnt(2)\n"
        "v_readfirstlane_b32 s91, v174\n"
        "s_sub_u32 s96, s91, s94\n"
        "s_cmp_eq_u32 s91, -1\n"
        "s_cselect_b32 s96, 4096, s96\n"
        "s_cmp_gt_i32 s96, 288\n"
        "s_cbranch_scc1 Lre_%=\n"
        "Lre2_%=:\n"
        "s_sub_u32 s96, s91, s90\n"
        "s_sub_u32 s96, s96, 192\n"
        "s_or_b32 s88, s88, s91\n"
        "s_bitcmp1_b32 s88, 0\n"
        "s_cselect_b32 %[L], %[L], s96\n"
        "s_add_u32 s91, s91, 160\n"
        "s_add_u32 s89, %[bp], %[d2]\n"
        "s_cmp_lg_u32 %[wv], 0\n"
        "s_cbranch_scc1 Lnw_%=\n"
        "Lu2_%=:\n"
        "s_memrealtime s[96:97]\n"
        "s_waitcnt lgkmcnt(0)\n"
        "s_lshl_b32 s96, s96, 4\n"
        "s_sub_u32 s96, s91, s96\n"
        "s_sub_u32 s97, s96, 144\n"
        "s_cmp_lt_u32 s97, 9600\n"
        "s_cbranch_scc1 Lu2_%=\n"
        "s_min_i32 s97, s96, 144\n"
        "Lu2f_%=:\n"
        "s_sub_i32 s97, s97, 21\n"
        "s_cmp_gt_i32 s97, 60\n"
        "s_cbranch_scc1 Lu2f_%=\n"
        "buffer_load_dwordx4 v[176:179], v104, %[rs], %[bp] offen offset:0 sc1\n"
        "buffer_load_dwordx4 v[180:183], v104, %[rs], %[bp] offen offset:1024 sc1\n"
        "buffer_load_dwordx4 v[184:187], v104, %[rs], %[bp] offen offset:2048 sc1\n"
        "buffer_load_dwordx4 v[188:191], v104, %[rs], %[bp] offen offset:3072 sc1\n"
        "s_waitcnt vmcnt(4)\n"
        "Ltc_%=:\n"
        "v_smfmac_f32_16x16x64_bf16 v[208:211], v[24:27], v[80:87], v96\n"
        "v_smfmac_f32_16x16x64_bf16 v[212:215], v[28:31], v[88:95], v96\n"
        "v_max3_u32 v16, v24, v25, v26\n"
        "v_max3_u32 v17, v28, v29, v30\n"
        "v_max3_u32 v16, v16, v27, v31\n"
        "v_max_u32 v16, v16, v17\n"
        "v_cmp_eq_u32 vcc, -1, v16\n"
        "s_cbranch_vccnz Lt0_%=\n"
        "Ltg_%=:\n"
        "s_nop 0\n"
        "v_cvt_pk_bf16_f32 v4, v208, v209\n"
        "v_cvt_pk_bf16_f32 v6, v210, v211\n"
        "v_cvt_pk_bf16_f32 v5, v212, v213\n"
        "v_cvt_pk_bf16_f32 v7, v214, v215\n"
        "s_cmp_lg_u32 %[wv], 0\n"
        "s_mov_b32 s93, -1\n"
        "s_cbranch_scc1 Lox_%=\n"
        "buffer_load_dwordx4 v[232:235], v104, %[rs], s89 offen offset:0 sc1\n"
        "buffer_load_dwordx4 v[236:239], v104, %[rs], s89 offen offset:1024 sc1\n"
        "buffer_load_dwordx4 v[240:243], v104, %[rs], s89 offen offset:2048 sc1\n"
        "buffer_load_dwordx4 v[244:247], v104, %[rs], s89 offen offset:3072 sc1\n"
        "s_barrier\n"
        "ds_read_b128 v[132:135], v100 offset:43008\n"
        "ds_read_b128 v[136:139], v100 offset:44032\n"
        "ds_read_b128 v[140:143], v100 offset:45056\n"
        "ds_read_b128 v[144:147], v100 offset:46080\n"
        "ds_read_b128 v[148:151], v100 offset:47104\n"
        "ds_read_b128 v[152:155], v100 offset:48128\n"
        "s_add_u32 s92, %[bm], 16384\n"
        "s_waitcnt lgkmcnt(0)\n"
        "Lpc_%=:\n"
        "s_waitcnt vmcnt(7)\n"
        "v_smfmac_f32_16x16x64_bf16 v[12:15], v[176:179], v[0:7], v96\n"
        "v_max3_u32 v16, v176, v177, 0\n"
        "v_max3_u32 v16, v178, v179, v16\n"
        "s_waitcnt vmcnt(6)\n"
        "v_smfmac_f32_16x16x64_bf16 v[12:15], v[180:183], v[132:139], v96\n"
        "v_max3_u32 v16, v180, v181, v16\n"
        "v_max3_u32 v16, v182, v183, v16\n"
        "s_waitcnt vmcnt(5)\n"
        "v_smfmac_f32_16x16x64_bf16 v[12:15], v[184:187], v[140:147], v96\n"
        "v_max3_u32 v16, v184, v185, v16\n"
        "v_max3_u32 v16, v186, v187, v16\n"
        "s_waitcnt vmcnt(4)\n"
        "v_smfmac_f32_16x16x64_bf16 v[12:15], v[188:191], v[148:155], v96\n"
        "v_max3_u32 v16, v188, v189, v16\n"
        "v_max3_u32 v16, v190, v191, v16\n"
        "v_cmp_eq_u32 vcc, -1, v16\n"
        "s_cbranch_vccnz Lpr_%=\n"
        "Lpgo_%=:\n"
        "s_nop 3\n"
        "v_permlane32_swap_b32 v12, v14\n"
        "v_permlane32_swap_b32 v13, v15\n"
        "v_add_f32 v12, v12, v14\n"
        "v_add_f32 v13, v13, v15\n"
        "s_nop 0\n"
        "v_mov_b32_dpp v14, v12 quad_perm:[1,0,3,2] row_mask:0xf bank_mask:0xf\n"
        "v_mov_b32_dpp v15, v13 quad_perm:[1,0,3,2] row_mask:0xf bank_mask:0xf\n"
        "v_cvt_pk_bf16_f32 v12, v12, v14\n"
        "v_cvt_pk_bf16_f32 v13, v15, v13\n"
        "v_cndmask_b32 v12, v12, v13, s[84:85]\n"
        "v_min_u32 v12, -2, v12\n"
        "buffer_store_dword v12, v106, %[rs], s92 offen sc1\n"
        "Lnf_%=:\n"
        "s_barrier\n"
        "Lbulk_%=:\n"
        "s_max_u32 s86, s86, %[s]\n"
        "s_cmp_ge_u32 s86, 62\n"
        "s_cbranch_scc1 Lnog_%=\n"
        "Lgemm_%=:\n"
        "v_mfma_f32_16x16x32_bf16 v[32:35], v[116:119], a[0:3], 0\n"
        "ds_read_b128 v[124:127], v99 offset:2048\n"
        "v_mov_b64 v[8:9], -1\n"
        "v_mfma_f32_16x16x32_bf16 v[36:39], v[120:123], a[0:3], 0\n"
        "ds_read_b128 v[128:131], v99 offset:2304\n"
        "v_max3_u32 v222, v116, v117, v222\n"
        "v_mfma_f32_16x16x32_bf16 v[40:43], v[116:119], a[8:11], 0\n"
        "v_max3_u32 v222, v118, v119, v222\n"
        "v_max3_u32 v222, v120, v121, v222\n"
        "v_mfma_f32_16x16x32_bf16 v[44:47], v[120:123], a[8:11], 0\n"
        "v_max3_u32 v222, v122, v123, v222\n"
        "v_mfma_f32_16x16x32_bf16 v[48:51], v[116:119], a[16:19], 0\n"
        "v_cmp_eq_u32 vcc, -1, v223\n"
        "v_mfma_f32_16x16x32_bf16 v[52:55], v[120:123], a[16:19], 0\n"
        "s_and_b64 vcc, vcc, %[live]\n"
        "s_cbranch_vccnz Lslowb_%=\n"
        "v_mfma_f32_16x16x32_bf16 v[56:59], v[116:119], a[24:27], 0\n"
        "v_mfma_f32_16x16x32_bf16 v[60:63], v[120:123], a[24:27], 0\n"
        "s_waitcnt lgkmcnt(0)\n"
        "v_mfma_f32_16x16x32_bf16 v[32:35], v[124:127], a[4:7], v[32:35]\n"
        "ds_read_b128 v[116:119], v99 offset:4096\n"
        "v_mfma_f32_16x16x32_bf16 v[36:39], v[128:131], a[4:7], v[36:39]\n"
        "ds_read_b128 v[120:123], v99 offset:4352\n"
        "v_max3_u32 v222, v124, v125, v222\n"
        "v_mfma_f32_16x16x32_bf16 v[40:43], v[124:127], a[12:15], v[40:43]\n"
        "s_add_u32 s95, %[bm], 147456\n"
        "buffer_store_dword v8, v106, %[rs], s95 offen sc1\n"
        "v_max3_u32 v222, v126, v127, v222\n"
        "v_mfma_f32_16x16x32_bf16 v[44:47], v[128:131], a[12:15], v[44:47]\n"
        "s_add_u32 s95, %[x1], 229376\n"
        "buffer_store_dwordx2 v[8:9], v109, %[rs], s95 offen sc1\n"
        "v_max3_u32 v222, v128, v129, v222\n"
        "v_mfma_f32_16x16x32_bf16 v[48:51], v[124:127], a[20:23], v[48:51]\n"
        "v_max3_u32 v222, v130, v131, v222\n"
        "v_mov_b64 v[192:193], 0\n"
        "v_mfma_f32_16x16x32_bf16 v[52:55], v[128:131], a[20:23], v[52:55]\n"
        "v_mov_b64 v[194:195], 0\n"
        "v_mov_b64 v[196:197], 0\n"
        "v_mfma_f32_16x16x32_bf16 v[56:59], v[124:127], a[28:31], v[56:59]\n"
        "v_mov_b64 v[198:199], 0\n"
        "v_mov_b64 v[200:201], 0\n"
        "v_mfma_f32_16x16x32_bf16 v[60:63], v[128:131], a[28:31], v[60:63]\n"
        "v_mov_b64 v[202:203], 0\n"
        "v_mov_b64 v[204:205], 0\n"
        "s_waitcnt lgkmcnt(0)\n"
        "v_mfma_f32_16x16x32_bf16 v[32:35], v[116:119], a[32:35], v[32:35]\n"
        "ds_read_b128 v[124:127], v99 offset:6144\n"
        "v_mov_b64 v[206:207], 0\n"
        "v_mfma_f32_16x16x32_bf16 v[36:39], v[120:123], a[32:35], v[36:39]\n"
        "ds_read_b128 v[128:131], v99 offset:6400\n"
        "v_max3_u32 v222, v116, v117, v222\n"
        "v_mfma_f32_16x16x32_bf16 v[40:43], v[116:119], a[40:43], v[40:43]\n"
        "v_max3_u32 v222, v118, v119, v222\n"
        "v_max3_u32 v222, v120, v121, v222\n"
        "v_mfma_f32_16x16x32_bf16 v[44:47], v[120:123], a[40:43], v[44:47]\n"
        "v_max3_u32 v222, v122, v123, v222\n"
        "v_mov_b64 v[208:209], 0\n"
        "v_mfma_f32_16x16x32_bf16 v[48:51], v[116:119], a[48:51], v[48:51]\n"
        "v_mov_b64 v[210:211], 0\n"
        "v_mov_b64 v[212:213], 0\n"
        "v_mfma_f32_16x16x32_bf16 v[52:55], v[120:123], a[48:51], v[52:55]\n"
        "v_mov_b64 v[214:215], 0\n"
        "v_mfma_f32_16x16x32_bf16 v[56:59], v[116:119], a[56:59], v[56:59]\n"
        "v_mfma_f32_16x16x32_bf16 v[60:63], v[120:123], a[56:59], v[60:63]\n"
        "s_waitcnt lgkmcnt(0)\n"
        "v_mfma_f32_16x16x32_bf16 v[32:35], v[124:127], a[36:39], v[32:35]\n"
        "ds_read_b128 v[116:119], v99 offset:8192\n"
        "v_mfma_f32_16x16x32_bf16 v[36:39], v[128:131], a[36:39], v[36:39]\n"
        "ds_read_b128 v[120:123], v99 offset:8448\n"
        "v_max3_u32 v222, v124, v125, v222\n"
        "v_mfma_f32_16x16x32_bf16 v[40:43], v[124:127], a[44:47], v[40:43]\n"
        "v_max3_u32 v222, v126, v127, v222\n"
        "v_max3_u32 v222, v128, v129, v222\n"
        "v_mfma_f32_16x16x32_bf16 v[44:47], v[128:131], a[44:47], v[44:47]\n"
        "v_max3_u32 v222, v130, v131, v222\n"
        "v_mfma_f32_16x16x32_bf16 v[48:51], v[124:127], a[52:55], v[48:51]\n"
        "v_mfma_f32_16x16x32_bf16 v[52:55], v[128:131], a[52:55], v[52:55]\n"
        "v_mfma_f32_16x16x32_bf16 v[56:59], v[124:127], a[60:63], v[56:59]\n"
        "v_mfma_f32_16x16x32_bf16 v[60:63], v[128:131], a[60:63], v[60:63]\n"
        "s_add_u32 s92, %[bxc], 57344\n"
        "s_cmp_lt_u32 %[s], 60\n"
        "s_cselect_b32 s87, s92, %[bxc]\n"
        "s_xor_b32 s92, %[lxc], 32768\n"
        "s_mul_i32 s95, %[wq], 7168\n"
        "s_add_u32 %[lx], s95, s92\n"
        "s_add_u32 %[bx], s95, s87\n"
        "s_mov_b32 m0, %[lx]\n"
        "s_nop 0\n"
        "buffer_load_dwordx4 v100, %[rs], %[bx] offen offset:0 sc1 lds\n"
        "s_waitcnt lgkmcnt(0)\n"
        "v_mfma_f32_16x16x32_bf16 v[32:35], v[116:119], a[64:67], v[32:35]\n"
        "ds_read_b128 v[124:127], v99 offset:10240\n"
        "v_mfma_f32_16x16x32_bf16 v[36:39], v[120:123], a[64:67], v[36:39]\n"
        "ds_read_b128 v[128:131], v99 offset:10496\n"
        "v_max3_u32 v222, v116, v117, v222\n"
        "v_mfma_f32_16x16x32_bf16 v[40:43], v[116:119], a[72:75], v[40:43]\n"
        "buffer_load_dwordx4 v100, %[rs], %[bx] offen offset:1024 sc1 lds\n"
        "v_max3_u32 v222, v118, v119, v222\n"
        "v_mfma_f32_16x16x32_bf16 v[44:47], v[120:123], a[72:75], v[44:47]\n"
        "v_max3_u32 v222, v120, v121, v222\n"
        "v_max3_u32 v222, v122, v123, v222\n"
        "v_mfma_f32_16x16x32_bf16 v[48:51], v[116:119], a[80:83], v[48:51]\n"
        "v_mfma_f32_16x16x32_bf16 v[52:55], v[120:123], a[80:83], v[52:55]\n"
        "buffer_load_dwordx4 v100, %[rs], %[bx] offen offset:2048 sc1 lds\n"
        "v_mfma_f32_16x16x32_bf16 v[56:59], v[116:119], a[88:91], v[56:59]\n"
        "buffer_load_dwordx4 v100, %[rs], %[bx] offen offset:3072 sc1 lds\n"
        "v_mfma_f32_16x16x32_bf16 v[60:63], v[120:123], a[88:91], v[60:63]\n"
        "s_add_u32 s95, %[lx], 4096\n"
        "s_mov_b32 m0, s95\n"
        "s_add_u32 s95, %[bx], 4096\n"
        "buffer_load_dwordx4 v100, %[rs], s95 offen offset:0 sc1 lds\n"
        "s_waitcnt lgkmcnt(0)\n"
        "v_mfma_f32_16x16x32_bf16 v[32:35], v[124:127], a[68:71], v[32:35]\n"
        "ds_read_b128 v[116:119], v99 offset:12288\n"
        "v_mfma_f32_16x16x32_bf16 v[36:39], v[128:131], a[68:71], v[36:39]\n"
        "ds_read_b128 v[120:123], v99 offset:12544\n"
        "v_max3_u32 v222, v124, v125, v222\n"
        "v_mfma_f32_16x16x32_bf16 v[40:43], v[124:127], a[76:79], v[40:43]\n"
        "buffer_load_dwordx4 v100, %[rs], s95 offen offset:1024 sc1 lds\n"
        "v_max3_u32 v222, v126, v127, v222\n"
        "v_mfma_f32_16x16x32_bf16 v[44:47], v[128:131], a[76:79], v[44:47]\n"
        "v_max3_u32 v222, v128, v129, v222\n"
        "v_max3_u32 v222, v130, v131, v222\n"
        "v_mfma_f32_16x16x32_bf16 v[48:51], v[124:127], a[84:87], v[48:51]\n"
        "v_mfma_f32_16x16x32_bf16 v[52:55], v[128:131], a[84:87], v[52:55]\n"
        "buffer_load_dwordx4 v100, %[rs], s95 offen offset:2048 sc1 lds\n"
        "v_mfma_f32_16x16x32_bf16 v[56:59], v[124:127], a[92:95], v[56:59]\n"
        "v_mfma_f32_16x16x32_bf16 v[60:63], v[128:131], a[92:95], v[60:63]\n"
        "s_waitcnt lgkmcnt(0)\n"
        "v_mfma_f32_16x16x32_bf16 v[32:35], v[116:119], a[96:99], v[32:35]\n"
        "ds_read_b128 v[124:127], v99 offset:14336\n"
        "v_mfma_f32_16x16x32_bf16 v[36:39], v[120:123], a[96:99], v[36:39]\n"
        "ds_read_b128 v[128:131], v99 offset:14592\n"
        "v_max3_u32 v222, v116, v117, v222\n"
        "v_mfma_f32_16x16x32_bf16 v[40:43], v[116:119], a[104:107], v[40:43]\n"
        "s_add_u32 s89, %[pt], 5120\n"
        "buffer_load_dwordx2 v[172:173], v108, %[rs], %[xr] offen sc1\n"
        "v_max3_u32 v222, v118, v119, v222\n"
        "v_mfma_f32_16x16x32_bf16 v[44:47], v[120:123], a[104:107], v[44:47]\n"
        "v_max3_u32 v222, v120, v121, v222\n"
        "v_max3_u32 v222, v122, v123, v222\n"
        "v_mfma_f32_16x16x32_bf16 v[48:51], v[116:119], a[112:115], v[48:51]\n"
        "v_mfma_f32_16x16x32_bf16 v[52:55], v[120:123], a[112:115], v[52:55]\n"
        "buffer_load_dwordx4 v[132:135], v100, %[rs], %[pt] offen offset:0 sc1\n"
        "v_mfma_f32_16x16x32_bf16 v[56:59], v[116:119], a[120:123], v[56:59]\n"
        "buffer_load_dwordx4 v[136:139], v100, %[rs], %[pt] offen offset:1024 sc1\n"
        "v_mfma_f32_16x16x32_bf16 v[60:63], v[120:123], a[120:123], v[60:63]\n"
        "buffer_load_dwordx4 v[140:143], v100, %[rs], %[pt] offen offset:2560 sc1\n"
        "s_waitcnt lgkmcnt(0)\n"
        "v_mfma_f32_16x16x32_bf16 v[32:35], v[124:127], a[100:103], v[32:35]\n"
        "ds_read_b128 v[116:119], v99 offset:16384\n"
        "v_mfma_f32_16x16x32_bf16 v[36:39], v[128:131], a[100:103], v[36:39]\n"
        "ds_read_b128 v[120:123], v99 offset:16640\n"
        "v_max3_u32 v222, v124, v125, v222\n"
        "v_mfma_f32_16x16x32_bf16 v[40:43], v[124:127], a[108:111], v[40:43]\n"
        "buffer_load_dwordx4 v[144:147], v100, %[rs], %[pt] offen offset:3584 sc1\n"
        "v_max3_u32 v222, v126, v127, v222\n"
        "v_mfma_f32_16x16x32_bf16 v[44:47], v[128:131], a[108:111], v[44:47]\n"
        "v_max3_u32 v222, v128, v129, v222\n"
        "v_max3_u32 v222, v130, v131, v222\n"
        "v_mfma_f32_16x16x32_bf16 v[48:51], v[124:127], a[116:119], v[48:51]\n"
        "v_mfma_f32_16x16x32_bf16 v[52:55], v[128:131], a[116:119], v[52:55]\n"
        "buffer_load_dwordx4 v[148:151], v100, %[rs], s89 offen offset:0 sc1\n"
        "v_mfma_f32_16x16x32_bf16 v[56:59], v[124:127], a[124:127], v[56:59]\n"
        "buffer_load_dwordx4 v[152:155], v100, %[rs], s89 offen offset:1024 sc1\n"
        "v_mfma_f32_16x16x32_bf16 v[60:63], v[128:131], a[124:127], v[60:63]\n"
        "buffer_load_dwordx4 v[156:159], v100, %[rs], s89 offen offset:2560 sc1\n"
        "s_waitcnt lgkmcnt(0)\n"
        "v_mfma_f32_16x16x32_bf16 v[32:35], v[116:119], a[128:131], v[32:35]\n"
        "ds_read_b128 v[124:127], v99 offset:18432\n"
        "v_mfma_f32_16x16x32_bf16 v[36:39], v[120:123], a[128:131], v[36:39]\n"
        "ds_read_b128 v[128:131], v99 offset:18688\n"
        "v_max3_u32 v222, v116, v117, v222\n"
        "v_mfma_f32_16x16x32_bf16 v[40:43], v[116:119], a[136:139], v[40:43]\n"
        "buffer_load_dwordx4 v[160:163], v100, %[rs], s89 offen offset:3584 sc1\n"
        "v_max3_u32 v222, v118, v119, v222\n"
        "v_mfma_f32_16x16x32_bf16 v[44:47], v[120:123], a[136:139], v[44:47]\n"
        "v_max3_u32 v222, v120, v121, v222\n"
        "v_max3_u32 v222, v122, v123, v222\n"
        "v_mfma_f32_16x16x32_bf16 v[48:51], v[116:119], a[144:147], v[48:51]\n"
        "v_mfma_f32_16x16x32_bf16 v[52:55], v[120:123], a[144:147], v[52:55]\n"
        "buffer_load_dwordx2 v[164:165], v107, %[rs], %[pt] offen offset:0 sc1\n"
        "v_mfma_f32_16x16x32_bf16 v[56:59], v[116:119], a[152:155], v[56:59]\n"
        "buffer_load_dwordx2 v[166:167], v107, %[rs], s89 offen offset:0 sc1\n"
        "v_mfma_f32_16x16x32_bf16 v[60:63], v[120:123], a[152:155], v[60:63]\n"
        "buffer_load_dwordx2 v[168:169], v107, %[rs], %[pt] offen offset:256 sc1\n"
        "s_waitcnt lgkmcnt(0)\n"
        "v_mfma_f32_16x16x32_bf16 v[32:35], v[124:127], a[132:135], v[32:35]\n"
        "ds_read_b128 v[116:119], v99 offset:20480\n"
        "v_mfma_f32_16x16x32_bf16 v[36:39], v[128:131], a[132:135], v[36:39]\n"
        "ds_read_b128 v[120:123], v99 offset:20736\n"
        "v_max3_u32 v222, v124, v125, v222\n"
        "v_mfma_f32_16x16x32_bf16 v[40:43], v[124:127], a[140:143], v[40:43]\n"
        "buffer_load_dwordx2 v[170:171], v107, %[rs], s89 offen offset:256 sc1\n"
        "v_max3_u32 v222, v126, v127, v222\n"
        "v_mfma_f32_16x16x32_bf16 v[44:47], v[128:131], a[140:143], v[44:47]\n"
        "v_max3_u32 v222, v128, v129, v222\n"
        "v_max3_u32 v222, v130, v131, v222\n"
        "v_mfma_f32_16x16x32_bf16 v[48:51], v[124:127], a[148:151], v[48:51]\n"
        "v_mfma_f32_16x16x32_bf16 v[52:55], v[128:131], a[148:151], v[52:55]\n"
        "v_mfma_f32_16x16x32_bf16 v[56:59], v[124:127], a[156:159], v[56:59]\n"
        "v_mfma_f32_16x16x32_bf16 v[60:63], v[128:131], a[156:159], v[60:63]\n"
        "s_waitcnt lgkmcnt(0)\n"
        "v_mfma_f32_16x16x32_bf16 v[32:35], v[116:119], a[160:163], v[32:35]\n"
        "ds_read_b128 v[124:127], v99 offset:22528\n"
        "v_mfma_f32_16x16x32_bf16 v[36:39], v[120:123], a[160:163], v[36:39]\n"
        "ds_read_b128 v[128:131], v99 offset:22784\n"
        "v_max3_u32 v222, v116, v117, v222\n"
        "v_mfma_f32_16x16x32_bf16 v[40:43], v[116:119], a[168:171], v[40:43]\n"
        "v_max3_u32 v222, v118, v119, v222\n"
        "v_max3_u32 v222, v120, v121, v222\n"
        "v_mfma_f32_16x16x32_bf16 v[44:47], v[120:123], a[168:171], v[44:47]\n"
        "v_max3_u32 v222, v122, v123, v222\n"
        "v_mfma_f32_16x16x32_bf16 v[48:51], v[116:119], a[176:179], v[48:51]\n"
        "v_mfma_f32_16x16x32_bf16 v[52:55], v[120:123], a[176:179], v[52:55]\n"
        "v_mfma_f32_16x16x32_bf16 v[56:59], v[116:119], a[184:187], v[56:59]\n"
        "v_mfma_f32_16x16x32_bf16 v[60:63], v[120:123], a[184:187], v[60:63]\n"
        "s_waitcnt lgkmcnt(0)\n"
        "v_mfma_f32_16x16x32_bf16 v[32:35], v[124:127], a[164:167], v[32:35]\n"
        "ds_read_b128 v[116:119], v99 offset:24576\n"
        "v_mfma_f32_16x16x32_bf16 v[36:39], v[128:131], a[164:167], v[36:39]\n"
        "ds_read_b128 v[120:123], v99 offset:24832\n"
        "v_max3_u32 v222, v124, v125, v222\n"
        "v_mfma_f32_16x16x32_bf16 v[40:43], v[124:127], a[172:175], v[40:43]\n"
        "v_max3_u32 v222, v126, v127, v222\n"
        "v_max3_u32 v222, v128, v129, v222\n"
        "v_mfma_f32_16x16x32_bf16 v[44:47], v[128:131], a[172:175], v[44:47]\n"
        "v_max3_u32 v222, v130, v131, v222\n"
        "v_mfma_f32_16x16x32_bf16 v[48:51], v[124:127], a[180:183], v[48:51]\n"
        "v_mfma_f32_16x16x32_bf16 v[52:55], v[128:131], a[180:183], v[52:55]\n"
        "v_mfma_f32_16x16x32_bf16 v[56:59], v[124:127], a[188:191], v[56:59]\n"
        "v_mfma_f32_16x16x32_bf16 v[60:63], v[128:131], a[188:191], v[60:63]\n"
        "s_waitcnt lgkmcnt(0)\n"
        "v_mfma_f32_16x16x32_bf16 v[32:35], v[116:119], a[192:195], v[32:35]\n"
        "ds_read_b128 v[124:127], v99 offset:26624\n"
        "v_mfma_f32_16x16x32_bf16 v[36:39], v[120:123], a[192:195], v[36:39]\n"
        "ds_read_b128 v[128:131], v99 offset:26880\n"
        "v_max3_u32 v222, v116, v117, v222\n"
        "v_mfma_f32_16x16x32_bf16 v[40:43], v[116:119], a[200:203], v[40:43]\n"
        "v_max3_u32 v222, v118, v119, v222\n"
        "v_max3_u32 v222, v120, v121, v222\n"
        "v_mfma_f32_16x16x32_bf16 v[44:47], v[120:123], a[200:203], v[44:47]\n"
        "v_max3_u32 v222, v122, v123, v222\n"
        "v_mfma_f32_16x16x32_bf16 v[48:51], v[116:119], a[208:211], v[48:51]\n"
        "v_mfma_f32_16x16x32_bf16 v[52:55], v[120:123], a[208:211], v[52:55]\n"
        "v_mfma_f32_16x16x32_bf16 v[56:59], v[116:119], a[216:219], v[56:59]\n"
        "v_mfma_f32_16x16x32_bf16 v[60:63], v[120:123], a[216:219], v[60:63]\n"
        "s_waitcnt lgkmcnt(0)\n"
        "v_mfma_f32_16x16x32_bf16 v[32:35], v[124:127], a[196:199], v[32:35]\n"
        "v_max3_u32 v222, v124, v125, v222\n"
        "v_max3_u32 v222, v126, v127, v222\n"
        "v_mfma_f32_16x16x32_bf16 v[36:39], v[128:131], a[196:199], v[36:39]\n"
        "v_max3_u32 v222, v128, v129, v222\n"
        "v_max3_u32 v222, v130, v131, v222\n"
        "v_mfma_f32_16x16x32_bf16 v[40:43], v[124:127], a[204:207], v[40:43]\n"
        "v_cmp_eq_u32 vcc, -1, v222\n"
        "v_mfma_f32_16x16x32_bf16 v[44:47], v[128:131], a[204:207], v[44:47]\n"
        "s_and_b64 vcc, vcc, %[live]\n"
        "s_cbranch_vccnz Lslowb_%=\n"
        "v_mfma_f32_16x16x32_bf16 v[48:51], v[124:127], a[212:215], v[48:51]\n"
        "v_mfma_f32_16x16x32_bf16 v[52:55], v[128:131], a[212:215], v[52:55]\n"
        "v_mfma_f32_16x16x32_bf16 v[56:59], v[124:127], a[220:223], v[56:59]\n"
        "v_mfma_f32_16x16x32_bf16 v[60:63], v[128:131], a[220:223], v[60:63]\n"
        "s_mov_b32 %[bxc], s87\n"
        "s_xor_b32 %[lxc], %[lxc], 32768\n"
        "v_xor_b32 v99, 32768, v99\n"
        "s_add_u32 s95, %[bm], 23556\n"
        "buffer_load_dword v174, off, %[rs], s95 sc1\n"
        "v_cvt_pk_bf16_f32 v80, v32, v33\n"
        "v_cvt_pk_bf16_f32 v82, v34, v35\n"
        "v_cvt_pk_bf16_f32 v84, v36, v37\n"
        "v_cvt_pk_bf16_f32 v86, v38, v39\n"
        "v_cvt_pk_bf16_f32 v81, v40, v41\n"
        "v_cvt_pk_bf16_f32 v83, v42, v43\n"
        "v_cvt_pk_bf16_f32 v85, v44, v45\n"
        "v_cvt_pk_bf16_f32 v87, v46, v47\n"
        "v_cvt_pk_bf16_f32 v88, v48, v49\n"
        "v_cvt_pk_bf16_f32 v90, v50, v51\n"
        "v_cvt_pk_bf16_f32 v92, v52, v53\n"
        "v_cvt_pk_bf16_f32 v94, v54, v55\n"
        "v_cvt_pk_bf16_f32 v89, v56, v57\n"
        "v_cvt_pk_bf16_f32 v91, v58, v59\n"
        "v_cvt_pk_bf16_f32 v93, v60, v61\n"
        "v_cvt_pk_bf16_f32 v95, v62, v63\n"
        "Lpv_%=:\n"
        "s_waitcnt vmcnt(13)\n"
        "v_max_u32 v8, v172, v173\n"
        "v_lshlrev_b32 v12, 16, v172\n"
        "v_and_b32 v13, 0xffff0000, v172\n"
        "v_lshlrev_b32 v14, 16, v173\n"
        "v_and_b32 v15, 0xffff0000, v173\n"
        "s_waitcnt vmcnt(12)\n"
        "v_mfma_f32_16x16x32_bf16 v[224:227], v[64:67], v[132:135], 0\n"
        "v_max3_u32 v8, v132, v133, v8\n"
        "v_max3_u32 v8, v134, v135, v8\n"
        "s_waitcnt vmcnt(11)\n"
        "v_mfma_f32_16x16x32_bf16 v[228:231], v[64:67], v[136:139], 0\n"
        "v_max3_u32 v8, v136, v137, v8\n"
        "v_max3_u32 v8, v138, v139, v8\n"
        "s_waitcnt vmcnt(10)\n"
        "v_mfma_f32_16x16x32_bf16 v[224:227], v[68:71], v[140:143], v[224:227]\n"
        "v_max3_u32 v8, v140, v141, v8\n"
        "v_max3_u32 v8, v142, v143, v8\n"
        "s_waitcnt vmcnt(9)\n"
        "v_mfma_f32_16x16x32_bf16 v[228:231], v[68:71], v[144:147], v[228:231]\n"
        "v_max3_u32 v8, v144, v145, v8\n"
        "v_max3_u32 v8, v146, v147, v8\n"
        "s_waitcnt vmcnt(8)\n"
        "v_mfma_f32_16x16x32_bf16 v[224:227], v[72:75], v[148:151], v[224:227]\n"
        "v_max3_u32 v8, v148, v149, v8\n"
        "v_max3_u32 v8, v150, v151, v8\n"
        "s_waitcnt vmcnt(7)\n"
        "v_mfma_f32_16x16x32_bf16 v[228:231], v[72:75], v[152:155], v[228:231]\n"
        "v_max3_u32 v8, v152, v153, v8\n"
        "v_max3_u32 v8, v154, v155, v8\n"
        "s_waitcnt vmcnt(6)\n"
        "v_mfma_f32_16x16x32_bf16 v[224:227], v[76:79], v[156:159], v[224:227]\n"
        "v_max3_u32 v8, v156, v157, v8\n"
        "v_max3_u32 v8, v158, v159, v8\n"
        "s_waitcnt vmcnt(5)\n"
        "v_mfma_f32_16x16x32_bf16 v[228:231], v[76:79], v[160:163], v[228:231]\n"
        "v_max3_u32 v8, v160, v161, v8\n"
        "v_max3_u32 v8, v162, v163, v8\n"
        "s_waitcnt vmcnt(3)\n"
        "v_mfma_f32_16x16x32_bf16 v[224:227], v[112:115], v[164:167], v[224:227]\n"
        "v_max3_u32 v8, v164, v165, v8\n"
        "v_max3_u32 v8, v166, v167, v8\n"
        "s_waitcnt vmcnt(1)\n"
        "v_mfma_f32_16x16x32_bf16 v[228:231], v[112:115], v[168:171], v[228:231]\n"
        "v_max3_u32 v8, v168, v169, v8\n"
        "v_max3_u32 v8, v170, v171, v8\n"
        "v_cmp_eq_u32 vcc, -1, v8\n"
        "s_and_b64 vcc, vcc, %[live]\n"
        "s_cbranch_vccnz Lpw_%=\n"
        "ds_write_b128 v110, v[224:227] offset:40960\n"
        "s_nop 1\n"
        "ds_write_b128 v110, v[228:231] offset:41984\n"
        "v_cvt_pk_bf16_f32 v64, v32, v33\n"
        "v_cvt_pk_bf16_f32 v65, v34, v35\n"
        "v_cvt_pk_bf16_f32 v66, v36, v37\n"
        "v_cvt_pk_bf16_f32 v67, v38, v39\n"
        "v_cvt_pk_bf16_f32 v68, v40, v41\n"
        "v_cvt_pk_bf16_f32 v69, v42, v43\n"
        "v_cvt_pk_bf16_f32 v70, v44, v45\n"
        "v_cvt_pk_bf16_f32 v71, v46, v47\n"
        "v_cvt_pk_bf16_f32 v72, v48, v49\n"
        "v_cvt_pk_bf16_f32 v73, v50, v51\n"
        "s_waitcnt lgkmcnt(0)\n"
        "s_barrier\n"
        "ds_read_b128 v[232:235], v102 offset:40960\n"
        "ds_read_b128 v[236:239], v102 offset:43008\n"
        "ds_read_b128 v[240:243], v102 offset:45056\n"
        "ds_read_b128 v[244:247], v102 offset:47104\n"
        "v_cvt_pk_bf16_f32 v74, v52, v53\n"
        "v_cvt_pk_bf16_f32 v75, v54, v55\n"
        "v_cvt_pk_bf16_f32 v76, v56, v57\n"
        "v_cvt_pk_bf16_f32 v77, v58, v59\n"
        "v_cvt_pk_bf16_f32 v78, v60, v61\n"
        "v_cvt_pk_bf16_f32 v79, v62, v63\n"
        "s_waitcnt lgkmcnt(3)\n"
        "v_pk_add_f32 v[12:13], v[12:13], v[232:233]\n"
        "v_pk_add_f32 v[14:15], v[14:15], v[234:235]\n"
        "s_waitcnt lgkmcnt(1)\n"
        "v_pk_add_f32 v[236:237], v[236:237], v[240:241]\n"
        "v_pk_add_f32 v[238:239], v[238:239], v[242:243]\n"
        "s_waitcnt lgkmcnt(0)\n"
        "v_pk_add_f32 v[12:13], v[12:13], v[244:245]\n"
        "v_pk_add_f32 v[14:15], v[14:15], v[246:247]\n"
        "v_pk_add_f32 v[12:13], v[12:13], v[236:237]\n"
        "v_pk_add_f32 v[14:15], v[14:15], v[238:239]\n"
        "v_cvt_pk_bf16_f32 v12, v12, v13\n"
        "v_cvt_pk_bf16_f32 v13, v14, v15\n"
        "v_min_u32 v12, -2, v12\n"
        "v_min_u32 v13, -2, v13\n"
        "s_cmp_gt_u32 %[wv], 1\n"
        "s_waitcnt vmcnt(0)\n"
        "s_cbranch_scc1 Lns_%=\n"
        "buffer_store_dwordx2 v[12:13], v109, %[rs], %[x1] offen sc1\n"
        "Lns_%=:\n"
        "v_readfirstlane_b32 s91, v174\n"
        "s_add_u32 %[bm], %[bm], 32768\n"
        "s_add_u32 %[bt], %[bt], 32768\n"
        "s_add_u32 %[bp], %[bp], 16384\n"
        "s_add_u32 %[pt], %[pt], 81920\n"
        "s_add_u32 %[xr], %[xr], 57344\n"
        "s_add_u32 %[x1], %[x1], 57344\n"
        "s_add_u32 %[s], %[s], 2\n"
        "s_cmp_lt_u32 %[s], 64\n"
        "s_cbranch_scc1 Ltop_%=\n"
        "s_branch Lend_%=\n"
        "Llast_%=:\n"
        "s_nop 7\n"
        "v_permlane32_swap_b32 v200, v204\n"
        "v_permlane32_swap_b32 v201, v205\n"
        "v_permlane32_swap_b32 v202, v206\n"
        "v_permlane32_swap_b32 v203, v207\n"
        "v_add_f32 v200, v200, v204\n"
        "v_add_f32 v201, v201, v205\n"
        "v_add_f32 v202, v202, v206\n"
        "v_add_f32 v203, v203, v207\n"
        "v_cvt_pk_bf16_f32 v114, v200, v201\n"
        "v_cvt_pk_bf16_f32 v115, v202, v203\n"
        "Lnog_%=:\n"
        "s_cmp_ge_u32 %[s], 62\n"
        "s_cbranch_scc1 Lng2_%=\n"
        "v_add_u32 v223, %[lxc], v103\n"
        "v_mov_b32 v222, 0\n"
        "ds_read_b128 v[116:119], v99 offset:0\n"
        "ds_read_b128 v[120:123], v99 offset:256\n"
        "ds_read_b32 v223, v223\n"
        "s_waitcnt lgkmcnt(0)\n"
        "s_branch Lgemm_%=\n"
        "Lsc_%=:\n"
        "s_barrier\n"
        "s_mul_i32 s92, %[wq], 7168\n"
        "s_add_u32 s93, s92, %[lxc]\n"
        "s_mov_b32 m0, s93\n"
        "s_add_u32 s92, s92, %[bxc]\n"
        "buffer_load_dwordx4 v100, %[rs], s92 offen offset:0 sc1 lds\n"
        "buffer_load_dwordx4 v100, %[rs], s92 offen offset:1024 sc1 lds\n"
        "buffer_load_dwordx4 v100, %[rs], s92 offen offset:2048 sc1 lds\n"
        "buffer_load_dwordx4 v100, %[rs], s92 offen offset:3072 sc1 lds\n"
        "s_add_u32 s93, s93, 4096\n"
        "s_mov_b32 m0, s93\n"
        "s_add_u32 s92, s92, 4096\n"
        "buffer_load_dwordx4 v100, %[rs], s92 offen offset:0 sc1 lds\n"
        "buffer_load_dwordx4 v100, %[rs], s92 offen offset:1024 sc1 lds\n"
        "buffer_load_dwordx4 v100, %[rs], s92 offen offset:2048 sc1 lds\n"
        "s_mov_b32 s86, 99\n"
        "s_branch Lscd_%=\n"
        "Lng2_%=:\n"
        "s_sleep 8\n"
        "Lpl_%=:\n"
        "s_add_u32 s89, %[pt], 5120\n"
        "buffer_load_dwordx2 v[172:173], v108, %[rs], %[xr] offen sc1\n"
        "buffer_load_dwordx4 v[132:135], v100, %[rs], %[pt] offen offset:0 sc1\n"
        "buffer_load_dwordx4 v[136:139], v100, %[rs], %[pt] offen offset:1024 sc1\n"
        "buffer_load_dwordx4 v[140:143], v100, %[rs], %[pt] offen offset:2560 sc1\n"
        "buffer_load_dwordx4 v[144:147], v100, %[rs], %[pt] offen offset:3584 sc1\n"
        "buffer_load_dwordx4 v[148:151], v100, %[rs], s89 offen offset:0 sc1\n"
        "buffer_load_dwordx4 v[152:155], v100, %[rs], s89 offen offset:1024 sc1\n"
        "buffer_load_dwordx4 v[156:159], v100, %[rs], s89 offen offset:2560 sc1\n"
        "buffer_load_dwordx4 v[160:163], v100, %[rs], s89 offen offset:3584 sc1\n"
        "buffer_load_dwordx2 v[164:165], v107, %[rs], %[pt] offen offset:0 sc1\n"
        "buffer_load_dwordx2 v[166:167], v107, %[rs], s89 offen offset:0 sc1\n"
        "buffer_load_dwordx2 v[168:169], v107, %[rs], %[pt] offen offset:256 sc1\n"
        "buffer_load_dwordx2 v[170:171], v107, %[rs], s89 offen offset:256 sc1\n"
        "s_waitcnt vmcnt(0)\n"
        "s_branch Lpv_%=\n"
        "Lmr_%=:\n"
        "s_cbranch_scc1 Lmx_%=\n"
        "s_memrealtime s[96:97]\n"
        "s_waitcnt lgkmcnt(0)\n"
        "s_lshl_b32 s90, s96, 4\n"
        "s_add_u32 s90, s90, 129\n"
        "s_branch Lm_%=\n"
        "Lmx_%=:\n"
        "s_cbranch_scc0 Lm_%=\n"
        "s_mov_b32 %[capv], 0\n"
        "v_mov_b32 v16, 1\n"
        "s_mov_b32 s94, 4227328\n"
        "buffer_store_dword v16, off, %[rs], s94 sc1\n"
        "s_mov_b64 %[live], 0\n"
        "s_branch Lmg_%=\n"
        "Lre_%=:\n"
        "s_cmp_lg_u32 s91, -1\n"
        "s_cbranch_scc1 Lbod_%=\n"
        "s_add_u32 s82, s82, 512\n"
        "Lc1_%=:\n"
        "s_memrealtime s[96:97]\n"
        "s_waitcnt lgkmcnt(0)\n"
        "s_lshl_b32 s96, s96, 4\n"
        "s_sub_u32 s96, s82, s96\n"
        "s_sub_u32 s97, s96, 144\n"
        "s_cmp_lt_u32 s97, 9600\n"
        "s_cbranch_scc1 Lc1_%=\n"
        "s_min_i32 s97, s96, 144\n"
        "Lc1f_%=:\n"
        "s_sub_i32 s97, s97, 21\n"
        "s_cmp_gt_i32 s97, 60\n"
        "s_cbranch_scc1 Lc1f_%=\n"
        "buffer_load_dword v174, off, %[rs], s92 offset:128 sc1\n"
        "s_waitcnt vmcnt(0)\n"
        "v_readfirstlane_b32 s91, v174\n"
        "s_cmp_lg_u32 s91, -1\n"
        "s_cbranch_scc1 Lbod_%=\n"
        "s_add_u32 s82, s82, 512\n"
        "Lc2_%=:\n"
        "s_memrealtime s[96:97]\n"
        "s_waitcnt lgkmcnt(0)\n"
        "s_lshl_b32 s96, s96, 4\n"
        "s_sub_u32 s96, s82, s96\n"
        "s_sub_u32 s97, s96, 144\n"
        "s_cmp_lt_u32 s97, 9600\n"
        "s_cbranch_scc1 Lc2_%=\n"
        "s_min_i32 s97, s96, 144\n"
        "Lc2f_%=:\n"
        "s_sub_i32 s97, s97, 21\n"
        "s_cmp_gt_i32 s97, 60\n"
        "s_cbranch_scc1 Lc2f_%=\n"
        "buffer_load_dword v174, off, %[rs], s92 offset:256 sc1\n"
        "s_waitcnt vmcnt(0)\n"
        "v_readfirstlane_b32 s91, v174\n"
        "s_cmp_lg_u32 s91, -1\n"
        "s_cbranch_scc1 Lbod_%=\n"
        "s_mov_b32 s93, %[capv]\n"
        "Lbo_%=:\n"
        "buffer_load_dword v174, off, %[rs], s92 sc1\n"
        "s_sub_u32 s93, s93, 1\n"
        "s_cbranch_scc1 Lbod_%=\n"
        "s_waitcnt vmcnt(0)\n"
        "v_readfirstlane_b32 s91, v174\n"
        "s_cmp_lg_u32 s91, -1\n"
        "s_cbranch_scc1 Lbod_%=\n"
        "s_sleep 3\n"
        "s_branch Lbo_%=\n"
        "Lbod_%=:\n"
        "s_sub_u32 s94, s91, 96\n"
        "s_waitcnt vmcnt(0)\n"
        "Lu1_%=:\n"
        "s_memrealtime s[96:97]\n"
        "s_waitcnt lgkmcnt(0)\n"
        "s_lshl_b32 s96, s96, 4\n"
        "s_sub_u32 s96, s94, s96\n"
        "s_sub_u32 s97, s96, 144\n"
        "s_cmp_lt_u32 s97, 9600\n"
        "s_cbranch_scc1 Lu1_%=\n"
        "s_min_i32 s97, s96, 144\n"
        "Lu1f_%=:\n"
        "s_sub_i32 s97, s97, 21\n"
        "s_cmp_gt_i32 s97, 60\n"
        "s_cbranch_scc1 Lu1f_%=\n"
        "buffer_load_dwordx4 v[24:27], v101, %[rs], %[bt] offen sc1\n"
        "buffer_load_dwordx4 v[28:31], v101, %[rs], %[bt] offen offset:1024 sc1\n"
        "s_branch Lre2_%=\n"
        "Lnw_%=:\n"
        "s_waitcnt vmcnt(0)\n"
        "s_branch Ltc_%=\n"
        "Lox_%=:\n"
        "ds_write_b128 v110, v[4:7] offset:41984\n"
        "s_waitcnt lgkmcnt(0)\n"
        "s_barrier\n"
        "s_branch Lnf_%=\n"
        "Lt0_%=:\n"
        "s_nop 7\n"
        "v_mov_b64 v[208:209], 0\n"
        "v_mov_b64 v[210:211], 0\n"
        "v_mov_b64 v[212:213], 0\n"
        "v_mov_b64 v[214:215], 0\n"
        "s_mov_b32 s91, -1\n"
        "s_mov_b32 s88, 1\n"
        "s_cmp_eq_u32 s83, 0\n"
        "s_mov_b32 s83, 1\n"
        "s_cbranch_scc1 Lre_%=\n"
        "s_cmp_lg_u32 s91, -1\n"
        "s_cbranch_scc1 Lbpd_%=\n"
        "s_mov_b32 s93, %[capv]\n"
        "Lbp_%=:\n"
        "buffer_load_dword v174, off, %[rs], s92 sc1\n"
        "s_sub_u32 s93, s93, 1\n"
        "s_cbranch_scc1 Lbpd_%=\n"
        "s_waitcnt vmcnt(0)\n"
        "v_readfirstlane_b32 s91, v174\n"
        "s_cmp_lg_u32 s91, -1\n"
        "s_cbranch_scc1 Lbpd_%=\n"
        "s_sleep 3\n"
        "s_branch Lbp_%=\n"
        "Lbpd_%=:\n"
        "s_sub_u32 s94, s91, 192\n"
        "s_add_u32 s91, s91, 160\n"
        "Lu3_%=:\n"
        "s_memrealtime s[96:97]\n"
        "s_waitcnt lgkmcnt(0)\n"
        "s_lshl_b32 s96, s96, 4\n"
        "s_sub_u32 s96, s94, s96\n"
        "s_sub_u32 s97, s96, 144\n"
        "s_cmp_lt_u32 s97, 9600\n"
        "s_cbranch_scc1 Lu3_%=\n"
        "s_min_i32 s97, s96, 144\n"
        "Lu3f_%=:\n"
        "s_sub_i32 s97, s97, 21\n"
        "s_cmp_gt_i32 s97, 60\n"
        "s_cbranch_scc1 Lu3f_%=\n"
        "s_mov_b32 s93, %[capv]\n"
        "Lt_%=:\n"
        "buffer_load_dwordx4 v[24:27], v101, %[rs], %[bt] offen sc1\n"
        "buffer_load_dwordx4 v[28:31], v101, %[rs], %[bt] offen offset:1024 sc1\n"
        "s_sub_u32 s93, s93, 1\n"
        "s_waitcnt vmcnt(0)\n"
        "v_max3_u32 v16, v24, v25, v26\n"
        "v_max3_u32 v17, v28, v29, v30\n"
        "v_max3_u32 v16, v16, v27, v31\n"
        "v_max_u32 v16, v16, v17\n"
        "v_cmp_eq_u32 vcc, -1, v16\n"
        "s_cbranch_scc1 Ltt_%=\n"
        "s_cbranch_vccnz Lt_%=\n"
        "s_cmp_lg_u32 %[wv], 0\n"
        "s_cbranch_scc1 Ltc_%=\n"
        "Lu4_%=:\n"
        "s_memrealtime s[96:97]\n"
        "s_waitcnt lgkmcnt(0)\n"
        "s_lshl_b32 s96, s96, 4\n"
        "s_sub_u32 s96, s91, s96\n"
        "s_sub_u32 s97, s96, 144\n"
        "s_cmp_lt_u32 s97, 9600\n"
        "s_cbranch_scc1 Lu4_%=\n"
        "s_min_i32 s97, s96, 144\n"
        "Lu4f_%=:\n"
        "s_sub_i32 s97, s97, 21\n"
        "s_cmp_gt_i32 s97, 60\n"
        "s_cbranch_scc1 Lu4f_%=\n"
        "buffer_load_dwordx4 v[176:179], v104, %[rs], %[bp] offen offset:0 sc1\n"
        "buffer_load_dwordx4 v[180:183], v104, %[rs], %[bp] offen offset:1024 sc1\n"
        "buffer_load_dwordx4 v[184:187], v104, %[rs], %[bp] offen offset:2048 sc1\n"
        "buffer_load_dwordx4 v[188:191], v104, %[rs], %[bp] offen offset:3072 sc1\n"
        "s_branch Ltc_%=\n"
        "Ltt_%=:\n"
        "s_mov_b32 %[capv], 0\n"
        "v_mov_b32 v16, 1\n"
        "s_mov_b32 s94, 4227328\n"
        "buffer_store_dword v16, off, %[rs], s94 sc1\n"
        "s_mov_b64 %[live], 0\n"
        "s_branch Ltg_%=\n"
        "Lpr_%=:\n"
        "s_cmp_lg_u32 s93, -1\n"
        "s_cbranch_scc1 Lp2_%=\n"
        "s_mov_b32 s93, %[capv]\n"
        "s_nop 3\n"
        "v_mov_b64 v[12:13], 0\n"
        "v_mov_b64 v[14:15], 0\n"
        "s_waitcnt vmcnt(0)\n"
        "v_mov_b64 v[176:177], v[232:233]\n"
        "v_mov_b64 v[178:179], v[234:235]\n"
        "v_mov_b64 v[180:181], v[236:237]\n"
        "v_mov_b64 v[182:183], v[238:239]\n"
        "v_mov_b64 v[184:185], v[240:241]\n"
        "v_mov_b64 v[186:187], v[242:243]\n"
        "v_mov_b64 v[188:189], v[244:245]\n"
        "v_mov_b64 v[190:191], v[246:247]\n"
        "s_branch Lpc_%=\n"
        "Lp2_%=:\n"
        "s_sub_u32 s93, s93, 1\n"
        "s_cbranch_scc1 Lpt_%=\n"
        "buffer_load_dwordx4 v[176:179], v104, %[rs], %[bp] offen offset:0 sc1\n"
        "buffer_load_dwordx4 v[180:183], v104, %[rs], %[bp] offen offset:1024 sc1\n"
        "buffer_load_dwordx4 v[184:187], v104, %[rs], %[bp] offen offset:2048 sc1\n"
        "buffer_load_dwordx4 v[188:191], v104, %[rs], %[bp] offen offset:3072 sc1\n"
        "s_nop 3\n"
        "v_mov_b64 v[12:13], 0\n"
        "v_mov_b64 v[14:15], 0\n"
        "s_nop 0\n"
        "s_waitcnt vmcnt(0)\n"
        "s_branch Lpc_%=\n"
        "Lpt_%=:\n"
        "s_mov_b32 %[capv], 0\n"
        "v_mov_b32 v16, 1\n"
        "s_mov_b32 s94, 4227328\n"
        "buffer_store_dword v16, off, %[rs], s94 sc1\n"
        "s_mov_b64 %[live], 0\n"
        "s_nop 3\n"
        "s_branch Lpgo_%=\n"
        "Lpw_%=:\n"
        "s_mov_b32 s93, %[capv]\n"
        "s_sub_u32 %[slowc], %[slowc], 1\n"
        "s_cbranch_scc1 Lpwt_%=\n"
        "Lpwp_%=:\n"
        "buffer_load_dword v8, v111, %[rs], %[pt] offen sc1\n"
        "s_sub_u32 s93, s93, 1\n"
        "s_waitcnt vmcnt(0)\n"
        "v_cmp_eq_u32 vcc, -1, v8\n"
        "s_cbranch_scc1 Lpwt_%=\n"
        "s_cbranch_vccz Lpl_%=\n"
        "s_sleep 1\n"
        "s_branch Lpwp_%=\n"
        "Lpwt_%=:\n"
        "s_mov_b32 %[capv], 0\n"
        "v_mov_b32 v8, 1\n"
        "s_mov_b32 s94, 4227328\n"
        "buffer_store_dword v8, off, %[rs], s94 sc1\n"
        "s_mov_b64 %[live], 0\n"
        "s_branch Lpl_%=\n"
        "Lslowa_%=:\n"
        "s_nop 7\n"
        "s_mov_b32 s93, %[capv]\n"
        "s_sub_u32 %[slowc], %[slowc], 1\n"
        "s_cbranch_scc1 Lslowa_%=t\n"
        "Lslowa_%=p:\n"
        "buffer_load_dword v223, v103, %[rs], %[bxc] offen sc1\n"
        "s_sub_u32 s93, s93, 1\n"
        "s_waitcnt vmcnt(0)\n"
        "v_cmp_eq_u32 vcc, -1, v223\n"
        "s_cbranch_scc1 Lslowa_%=t\n"
        "s_cbranch_vccz Lslowa_%=d\n"
        "s_sleep 1\n"
        "s_branch Lslowa_%=p\n"
        "Lslowa_%=t:\n"
        "s_mov_b32 %[capv], 0\n"
        "v_mov_b32 v223, 1\n"
        "s_mov_b32 s94, 4227328\n"
        "buffer_store_dword v223, off, %[rs], s94 sc1\n"
        "Lslowa_%=d:\n"
        "s_waitcnt lgkmcnt(0)\n"
        "s_barrier\n"
        "s_mul_i32 s92, %[wq], 7168\n"
        "s_add_u32 s93, s92, %[lxc]\n"
        "s_mov_b32 m0, s93\n"
        "s_add_u32 s92, s92, %[bxc]\n"
        "buffer_load_dwordx4 v100, %[rs], s92 offen offset:0 sc1 lds\n"
        "buffer_load_dwordx4 v100, %[rs], s92 offen offset:1024 sc1 lds\n"
        "buffer_load_dwordx4 v100, %[rs], s92 offen offset:2048 sc1 lds\n"
        "buffer_load_dwordx4 v100, %[rs], s92 offen offset:3072 sc1 lds\n"
        "s_add_u32 s93, s93, 4096\n"
        "s_mov_b32 m0, s93\n"
        "s_add_u32 s92, s92, 4096\n"
        "buffer_load_dwordx4 v100, %[rs], s92 offen offset:0 sc1 lds\n"
        "buffer_load_dwordx4 v100, %[rs], s92 offen offset:1024 sc1 lds\n"
        "buffer_load_dwordx4 v100, %[rs], s92 offen offset:2048 sc1 lds\n"
        "s_waitcnt vmcnt(0)\n"
        "s_barrier\n"
        "s_cmp_eq_u32 %[capv], 0\n"
        "s_cselect_b64 %[live], 0, -1\n"
        "s_branch Lfirst_%=\n"
        "Lslowb_%=:\n"
        "s_nop 7\n"
        "s_mov_b32 s93, %[capv]\n"
        "s_sub_u32 %[slowc], %[slowc], 1\n"
        "s_cbranch_scc1 Lslowb_%=t\n"
        "Lslowb_%=p:\n"
        "buffer_load_dword v223, v103, %[rs], %[bxc] offen sc1\n"
        "s_sub_u32 s93, s93, 1\n"
        "s_waitcnt vmcnt(0)\n"
        "v_cmp_eq_u32 vcc, -1, v223\n"
        "s_cbranch_scc1 Lslowb_%=t\n"
        "s_cbranch_vccz Lslowb_%=d\n"
        "s_sleep 1\n"
        "s_branch Lslowb_%=p\n"
        "Lslowb_%=t:\n"
        "s_mov_b32 %[capv], 0\n"
        "v_mov_b32 v223, 1\n"
        "s_mov_b32 s94, 4227328\n"
        "buffer_store_dword v223, off, %[rs], s94 sc1\n"
        "Lslowb_%=d:\n"
        "s_waitcnt lgkmcnt(0)\n"
        "s_barrier\n"
        "s_mul_i32 s92, %[wq], 7168\n"
        "s_add_u32 s93, s92, %[lxc]\n"
        "s_mov_b32 m0, s93\n"
        "s_add_u32 s92, s92, %[bxc]\n"
        "buffer_load_dwordx4 v100, %[rs], s92 offen offset:0 sc1 lds\n"
        "buffer_load_dwordx4 v100, %[rs], s92 offen offset:1024 sc1 lds\n"
        "buffer_load_dwordx4 v100, %[rs], s92 offen offset:2048 sc1 lds\n"
        "buffer_load_dwordx4 v100, %[rs], s92 offen offset:3072 sc1 lds\n"
        "s_add_u32 s93, s93, 4096\n"
        "s_mov_b32 m0, s93\n"
        "s_add_u32 s92, s92, 4096\n"
        "buffer_load_dwordx4 v100, %[rs], s92 offen offset:0 sc1 lds\n"
        "buffer_load_dwordx4 v100, %[rs], s92 offen offset:1024 sc1 lds\n"
        "buffer_load_dwordx4 v100, %[rs], s92 offen offset:2048 sc1 lds\n"
        "s_waitcnt vmcnt(0)\n"
        "s_barrier\n"
        "v_add_u32 v223, %[lxc], v103\n"
        "v_mov_b32 v222, 0\n"
        "ds_read_b128 v[116:119], v99 offset:0\n"
        "ds_read_b128 v[120:123], v99 offset:256\n"
        "ds_read_b32 v223, v223\n"
        "s_cmp_eq_u32 %[capv], 0\n"
        "s_cselect_b64 %[live], 0, -1\n"
        "s_waitcnt lgkmcnt(0)\n"
        "s_branch Lgemm_%=\n"
        "Lend_%=:\n"
        "s_waitcnt vmcnt(0) lgkmcnt(0)\n"
        "s_endpgm\n"
        : "+{v[96:111]}"(A1), [s] "+s"(s), [bm] "+s"(bm), [bt] "+s"(bt), [bp] "+s"(bp), [bx] "+s"(bx), [lx] "+s"(lx), [bxc] "+s"(bxc), [lxc] "+s"(lxc), [pt] "+s"(pt), [xr] "+s"(xr), [x1] "+s"(x1), [capv] "+s"(capv), [slowc] "+s"(slowc), [L] "+s"(L), [live] "=&s"(live)
        : [rs] "s"(rs), [wb] "s"(wb), [wv] "s"(wv), [wq] "s"(wq), [d2] "s"(d2)
        : "vcc", "scc", "memory", "v255", "a255", "s82", "s83", "s84", "s85", "s86", "s87", "s88", "s89", "s90", "s91", "s92", "s93", "s94", "s95", "s96", "s97", "s98", "s99");
    __builtin_amdgcn_endpgm();
}
DI void role_V(Ctx& C, int c, int rep) {
    constexpr unsigned VL_RED = 36864; (void)VL_RED;
    const int lane = C.lane, w = C.w, l15 = lane & 15, g = lane >> 4;
    u32x16 A1;
    const unsigned xo = (2 * c + (g >> 1)) * 512 + ((w & 1) * 16 + l15) * 16 + (g & 1) * 8;
    A1[0] = (lane & 8) ? 0xEEEE : 0x4444;
    A1[1] = (w & 1) * 3584 + lane * 16;
    A1[2] = (w & 1) * 3584 + 3072 + (lane & 31) * 16;
    A1[3] = SL_X + g * 512 + l15 * 16;
    A1[4] = lane * 16;
    A1[5] = (4 * w + ((lane >> 3) & 1)) * 512 + (lane & 7) * 64 + g * 16;
    A1[6] = w * 1024 + lane * 16;
    A1[7] = (14 * (lane & 3) + 4 * ((lane >> 2) & 3) + (lane >> 5)) * 512 + ((lane >> 4) & 1) * 256 + l15 * 16 + g * 4;
    A1[8] = ((g & 1) + 2 * ((lane >> 3) & 1)) * 256 + (g >> 1) * 128 + (lane & 7) * 16;
    A1[9] = 0;
    A1[10] = w ? OOB : (unsigned)(c * 256 + (4 * (g & 1) + 2 * (g >> 1) + (lane & 1) + 8 * ((l15 >> 1) & 1)) * 16 + (l15 >> 2) * 4);
    A1[11] = (g >> 1) * 2560 + 2048 + l15 * 16 + (g & 1) * 8;
    A1[12] = xo;
    A1[13] = w < 2 ? xo : OOB;
    A1[14] = w * 2048 + lane * 16;
    A1[15] = (lane & 3) * 2560 + 2048 + ((lane >> 2) & 1) * 256;
    const unsigned wq = (w + c) & 3, bxc = O_XN + C.layer * XSZ + rep * SEGB;
    v_asm(A1, C.rw, rep, C.hop0 + rep * 16384u, C.hop1 + rep * 16384u, C.hop0 + J_PM * 16384u + rep * 4096u, bxc + wq * 7168u, SL_X + wq * 7168u, bxc, SL_X,
          O_PT + C.layer * PTSZ + (rep * 16 + 4 * w) * 2560u, O_XR + C.layer * XSZ + rep * SEGB, O_X1 + C.layer * XSZ + rep * SEGB,
          O_WV + (c * 4 + w) * 57344u, w, wq, O_HOP2 - O_HOP0, O_DBG + C.id * 8192u);
}

DI void g1_asm(const u32x8 (&W)[28], u32x16 A1, i32x4 rs, unsigned x1, unsigned hb, unsigned bb, unsigned wv, unsigned dbg) {
    unsigned s = 0, capv = SPIN_CAP, slowc = SPIN_CAP; unsigned long long live;
    asm volatile(
        "s_mov_b64 %[live], -1\n"
        "v_mov_b64 v[140:141], -1\n"
        "v_mov_b64 v[142:143], -1\n"
        "s_mov_b32 s91, -1\n"
        "s_mov_b32 s90, 3600\n"
        "v_mov_b32 v160, 0x3f803f80\n"
        "v_mov_b32 v161, 0x3f803f80\n"
        "v_mov_b32 v162, 0x3f803f80\n"
        "v_mov_b32 v163, 0x3f803f80\n"
        "s_nop 1\n"
        "v_mfma_f32_16x16x32_bf16 v[164:167], a[0:3], v[160:163], 0\n"
        "v_mfma_f32_16x16x32_bf16 v[168:171], a[4:7], v[160:163], 0\n"
        "v_mfma_f32_16x16x32_bf16 v[172:175], a[112:115], v[160:163], 0\n"
        "v_mfma_f32_16x16x32_bf16 v[176:179], a[116:119], v[160:163], 0\n"
        "v_mfma_f32_16x16x32_bf16 v[164:167], a[8:11], v[160:163], v[164:167]\n"
        "v_mfma_f32_16x16x32_bf16 v[168:171], a[12:15], v[160:163], v[168:171]\n"
        "v_mfma_f32_16x16x32_bf16 v[172:175], a[120:123], v[160:163], v[172:175]\n"
        "v_mfma_f32_16x16x32_bf16 v[176:179], a[124:127], v[160:163], v[176:179]\n"
        "v_mfma_f32_16x16x32_bf16 v[164:167], a[16:19], v[160:163], v[164:167]\n"
        "v_mfma_f32_16x16x32_bf16 v[168:171], a[20:23], v[160:163], v[168:171]\n"
        "v_mfma_f32_16x16x32_bf16 v[172:175], a[128:131], v[160:163], v[172:175]\n"
        "v_mfma_f32_16x16x32_bf16 v[176:179], a[132:135], v[160:163], v[176:179]\n"
        "v_mfma_f32_16x16x32_bf16 v[164:167], a[24:27], v[160:163], v[164:167]\n"
        "v_mfma_f32_16x16x32_bf16 v[168:171], a[28:31], v[160:163], v[168:171]\n"
        "v_mfma_f32_16x16x32_bf16 v[172:175], a[136:139], v[160:163], v[172:175]\n"
        "v_mfma_f32_16x16x32_bf16 v[176:179], a[140:143], v[160:163], v[176:179]\n"
        "v_mfma_f32_16x16x32_bf16 v[164:167], a[32:35], v[160:163], v[164:167]\n"
        "v_mfma_f32_16x16x32_bf16 v[168:171], a[36:39], v[160:163], v[168:171]\n"
        "v_mfma_f32_16x16x32_bf16 v[172:175], a[144:147], v[160:163], v[172:175]\n"
        "v_mfma_f32_16x16x32_bf16 v[176:179], a[148:151], v[160:163], v[176:179]\n"
        "v_mfma_f32_16x16x32_bf16 v[164:167], a[40:43], v[160:163], v[164:167]\n"
        "v_mfma_f32_16x16x32_bf16 v[168:171], a[44:47], v[160:163], v[168:171]\n"
        "v_mfma_f32_16x16x32_bf16 v[172:175], a[152:155], v[160:163], v[172:175]\n"
        "v_mfma_f32_16x16x32_bf16 v[176:179], a[156:159], v[160:163], v[176:179]\n"
        "v_mfma_f32_16x16x32_bf16 v[164:167], a[48:51], v[160:163], v[164:167]\n"
        "v_mfma_f32_16x16x32_bf16 v[168:171], a[52:55], v[160:163], v[168:171]\n"
        "v_mfma_f32_16x16x32_bf16 v[172:175], a[160:163], v[160:163], v[172:175]\n"
        "v_mfma_f32_16x16x32_bf16 v[176:179], a[164:167], v[160:163], v[176:179]\n"
        "v_mfma_f32_16x16x32_bf16 v[164:167], a[56:59], v[160:163], v[164:167]\n"
        "v_mfma_f32_16x16x32_bf16 v[168:171], a[60:63], v[160:163], v[168:171]\n"
        "v_mfma_f32_16x16x32_bf16 v[172:175], a[168:171], v[160:163], v[172:175]\n"
        "v_mfma_f32_16x16x32_bf16 v[176:179], a[172:175], v[160:163], v[176:179]\n"
        "v_mfma_f32_16x16x32_bf16 v[164:167], a[64:67], v[160:163], v[164:167]\n"
        "v_mfma_f32_16x16x32_bf16 v[168:171], a[68:71], v[160:163], v[168:171]\n"
        "v_mfma_f32_16x16x32_bf16 v[172:175], a[176:179], v[160:163], v[172:175]\n"
        "v_mfma_f32_16x16x32_bf16 v[176:179], a[180:183], v[160:163], v[176:179]\n"
        "v_mfma_f32_16x16x32_bf16 v[164:167], a[72:75], v[160:163], v[164:167]\n"
        "v_mfma_f32_16x16x32_bf16 v[168:171], a[76:79], v[160:163], v[168:171]\n"
        "v_mfma_f32_16x16x32_bf16 v[172:175], a[184:187], v[160:163], v[172:175]\n"
        "v_mfma_f32_16x16x32_bf16 v[176:179], a[188:191], v[160:163], v[176:179]\n"
        "v_mfma_f32_16x16x32_bf16 v[164:167], a[80:83], v[160:163], v[164:167]\n"
        "v_mfma_f32_16x16x32_bf16 v[168:171], a[84:87], v[160:163], v[168:171]\n"
        "v_mfma_f32_16x16x32_bf16 v[172:175], a[192:195], v[160:163], v[172:175]\n"
        "v_mfma_f32_16x16x32_bf16 v[176:179], a[196:199], v[160:163], v[176:179]\n"
        "v_mfma_f32_16x16x32_bf16 v[164:167], a[88:91], v[160:163], v[164:167]\n"
        "v_mfma_f32_16x16x32_bf16 v[168:171], a[92:95], v[160:163], v[168:171]\n"
        "v_mfma_f32_16x16x32_bf16 v[172:175], a[200:203], v[160:163], v[172:175]\n"
        "v_mfma_f32_16x16x32_bf16 v[176:179], a[204:207], v[160:163], v[176:179]\n"
        "v_mfma_f32_16x16x32_bf16 v[164:167], a[96:99], v[160:163], v[164:167]\n"
        "v_mfma_f32_16x16x32_bf16 v[168:171], a[100:103], v[160:163], v[168:171]\n"
        "v_mfma_f32_16x16x32_bf16 v[172:175], a[208:211], v[160:163], v[172:175]\n"
        "v_mfma_f32_16x16x32_bf16 v[176:179], a[212:215], v[160:163], v[176:179]\n"
        "v_mfma_f32_16x16x32_bf16 v[164:167], a[104:107], v[160:163], v[164:167]\n"
        "v_mfma_f32_16x16x32_bf16 v[168:171], a[108:111], v[160:163], v[168:171]\n"
        "v_mfma_f32_16x16x32_bf16 v[172:175], a[216:219], v[160:163], v[172:175]\n"
        "v_mfma_f32_16x16x32_bf16 v[176:179], a[220:223], v[160:163], v[176:179]\n"
        "Ltop_%=:\n"
        "s_cmp_lg_u32 s91, -1\n"
        "s_cbranch_scc1 Lbcd_%=\n"
        "s_mov_b32 s93, %[capv]\n"
        "Lbc_%=:\n"
        "buffer_load_dword v138, off, %[rs], %[bb] sc1\n"
        "s_sub_u32 s93, s93, 1\n"
        "s_cbranch_scc1 Lbcd_%=\n"
        "s_waitcnt vmcnt(0)\n"
        "v_readfirstlane_b32 s91, v138\n"
        "s_cmp_lg_u32 s91, -1\n"
        "s_cbranch_scc1 Lbcd_%=\n"
        "s_sleep 3\n"
        "s_branch Lbc_%=\n"
        "Lbcd_%=:\n"
        "s_cmp_ge_u32 %[s], 62\n"
        "s_cselect_b32 s92, 480, 0\n"
        "s_add_u32 s91, s91, s90\n"
        "s_sub_u32 s91, s91, s92\n"
        "Lbw_%=:\n"
        "s_memrealtime s[96:97]\n"
        "s_waitcnt lgkmcnt(0)\n"
        "s_lshl_b32 s96, s96, 4\n"
        "s_sub_u32 s96, s91, s96\n"
        "s_sub_u32 s97, s96, 144\n"
        "s_cmp_lt_u32 s97, 9600\n"
        "s_cbranch_scc1 Lbw_%=\n"
        "s_min_i32 s97, s96, 144\n"
        "Lbwf_%=:\n"
        "s_sub_i32 s97, s97, 21\n"
        "s_cmp_gt_i32 s97, 60\n"
        "s_cbranch_scc1 Lbwf_%=\n"
        "buffer_load_dwordx4 v[0:3], v96, %[rs], %[x1] offen sc1\n"
        "buffer_load_dwordx4 v[4:7], v97, %[rs], %[x1] offen sc1\n"
        "buffer_load_dwordx4 v[8:11], v98, %[rs], %[x1] offen sc1\n"
        "buffer_load_dwordx4 v[12:15], v99, %[rs], %[x1] offen sc1\n"
        "s_cmp_gt_i32 s96, 0\n"
        "s_cselect_b32 s89, -16, 0\n"
        "s_waitcnt vmcnt(0)\n"
        "Lchk_%=:\n"
        "ds_write_b128 v101, v[0:3] offset:0\n"
        "v_max3_u32 v136, v0, v1, 0\n"
        "v_max3_u32 v136, v2, v3, v136\n"
        "ds_write_b128 v101, v[4:7] offset:4096\n"
        "v_max3_u32 v136, v4, v5, v136\n"
        "v_max3_u32 v136, v6, v7, v136\n"
        "ds_write_b128 v101, v[8:11] offset:8192\n"
        "v_max3_u32 v136, v8, v9, v136\n"
        "v_max3_u32 v136, v10, v11, v136\n"
        "ds_write_b128 v101, v[12:15] offset:12288\n"
        "v_max3_u32 v136, v12, v13, v136\n"
        "v_max3_u32 v136, v14, v15, v136\n"
        "v_cmp_eq_u32 vcc, -1, v136\n"
        "v_dot2_f32_bf16 v16, v0, v160, 0\n"
        "v_dot2_f32_bf16 v17, v0, v0, 0\n"
        "v_dot2_f32_bf16 v18, v1, v160, 0\n"
        "v_dot2_f32_bf16 v19, v1, v1, 0\n"
        "s_and_b64 vcc, vcc, %[live]\n"
        "s_cbranch_vccnz Lwait_%=\n"
        "v_dot2_f32_bf16 v16, v2, v160, v16\n"
        "v_dot2_f32_bf16 v17, v2, v2, v17\n"
        "v_dot2_f32_bf16 v18, v3, v160, v18\n"
        "v_dot2_f32_bf16 v19, v3, v3, v19\n"
        "v_dot2_f32_bf16 v16, v4, v160, v16\n"
        "v_dot2_f32_bf16 v17, v4, v4, v17\n"
        "v_dot2_f32_bf16 v18, v5, v160, v18\n"
        "v_dot2_f32_bf16 v19, v5, v5, v19\n"
        "v_dot2_f32_bf16 v16, v6, v160, v16\n"
        "v_dot2_f32_bf16 v17, v6, v6, v17\n"
        "v_dot2_f32_bf16 v18, v7, v160, v18\n"
        "v_dot2_f32_bf16 v19, v7, v7, v19\n"
        "v_dot2_f32_bf16 v16, v8, v160, v16\n"
        "v_dot2_f32_bf16 v17, v8, v8, v17\n"
        "v_dot2_f32_bf16 v18, v9, v160, v18\n"
        "v_dot2_f32_bf16 v19, v9, v9, v19\n"
        "v_dot2_f32_bf16 v16, v10, v160, v16\n"
        "v_dot2_f32_bf16 v17, v10, v10, v17\n"
        "v_dot2_f32_bf16 v18, v11, v160, v18\n"
        "v_dot2_f32_bf16 v19, v11, v11, v19\n"
        "v_dot2_f32_bf16 v16, v12, v160, v16\n"
        "v_dot2_f32_bf16 v17, v12, v12, v17\n"
        "v_dot2_f32_bf16 v18, v13, v160, v18\n"
        "v_dot2_f32_bf16 v19, v13, v13, v19\n"
        "v_dot2_f32_bf16 v16, v14, v160, v16\n"
        "v_dot2_f32_bf16 v17, v14, v14, v17\n"
        "v_dot2_f32_bf16 v18, v15, v160, v18\n"
        "v_dot2_f32_bf16 v19, v15, v15, v19\n"
        "s_add_u32 s92, %[bb], 16384\n"
        "s_add_u32 s90, s90, s89\n"
        "s_min_u32 s90, s90, 5200\n"
        "v_add_f32 v16, v16, v18\n"
        "v_add_f32 v17, v17, v19\n"
        "s_nop 1\n"
        "v_permlane32_swap_b32 v16, v17\n"
        "v_add_f32 v16, v16, v17\n"
        "v_mov_b32 v17, v16\n"
        "s_nop 1\n"
        "v_permlane16_swap_b32 v16, v17\n"
        "v_add_f32 v16, v16, v17\n"
        "ds_write_b32 v103, v16 offset:20480\n"
        "s_memrealtime s[96:97]\n"
        "buffer_load_dword v138, off, %[rs], s92 sc1\n"
        "s_add_u32 %[x1], %[x1], 28672\n"
        "s_add_u32 %[s], %[s], 1\n"
        "s_waitcnt lgkmcnt(0)\n"
        "s_barrier\n"
        "ds_read_b128 v[64:67], v105 offset:0\n"
        "ds_read_b128 v[68:71], v105 offset:1024\n"
        "ds_read_b128 v[72:75], v105 offset:2048\n"
        "ds_read_b128 v[76:79], v105 offset:3072\n"
        "s_lshl_b32 s96, s96, 4\n"
        "v_mov_b32 v136, s96\n"
        "s_add_u32 s92, %[bb], 131072\n"
        "buffer_store_dword v136, v102, %[rs], %[bb] offen sc1\n"
        "s_add_u32 %[bb], %[bb], 16384\n"
        "s_waitcnt lgkmcnt(3)\n"
        "v_mfma_f32_16x16x32_bf16 v[48:51], a[0:3], v[64:67], 0\n"
        "ds_read_b128 v[80:83], v105 offset:4096\n"
        "v_mfma_f32_16x16x32_bf16 v[52:55], a[4:7], v[64:67], 0\n"
        "ds_read_b128 v[84:87], v105 offset:5120\n"
        "s_waitcnt lgkmcnt(4)\n"
        "v_mfma_f32_16x16x32_bf16 v[48:51], a[8:11], v[68:71], v[48:51]\n"
        "ds_read_b128 v[88:91], v105 offset:6144\n"
        "v_mfma_f32_16x16x32_bf16 v[52:55], a[12:15], v[68:71], v[52:55]\n"
        "ds_read_b128 v[92:95], v105 offset:7168\n"
        "s_waitcnt lgkmcnt(5)\n"
        "v_mfma_f32_16x16x32_bf16 v[48:51], a[16:19], v[72:75], v[48:51]\n"
        "ds_read_b128 v[112:115], v105 offset:8192\n"
        "v_mfma_f32_16x16x32_bf16 v[52:55], a[20:23], v[72:75], v[52:55]\n"
        "ds_read_b128 v[116:119], v105 offset:9216\n"
        "s_waitcnt lgkmcnt(6)\n"
        "v_mfma_f32_16x16x32_bf16 v[48:51], a[24:27], v[76:79], v[48:51]\n"
        "ds_read_b128 v[120:123], v105 offset:10240\n"
        "v_mfma_f32_16x16x32_bf16 v[52:55], a[28:31], v[76:79], v[52:55]\n"
        "ds_read_b128 v[124:127], v105 offset:11264\n"
        "s_waitcnt lgkmcnt(7)\n"
        "v_mfma_f32_16x16x32_bf16 v[48:51], a[32:35], v[80:83], v[48:51]\n"
        "ds_read_b128 v[128:131], v105 offset:12288\n"
        "v_mfma_f32_16x16x32_bf16 v[52:55], a[36:39], v[80:83], v[52:55]\n"
        "ds_read_b128 v[132:135], v105 offset:13312\n"
        "s_waitcnt lgkmcnt(8)\n"
        "v_mfma_f32_16x16x32_bf16 v[48:51], a[40:43], v[84:87], v[48:51]\n"
        "ds_read_b64 v[24:25], v104 offset:20480\n"
        "v_mfma_f32_16x16x32_bf16 v[52:55], a[44:47], v[84:87], v[52:55]\n"
        "ds_read_b64 v[26:27], v104 offset:20608\n"
        "s_waitcnt lgkmcnt(9)\n"
        "v_mfma_f32_16x16x32_bf16 v[48:51], a[48:51], v[88:91], v[48:51]\n"
        "ds_read_b64 v[28:29], v104 offset:20736\n"
        "v_mfma_f32_16x16x32_bf16 v[52:55], a[52:55], v[88:91], v[52:55]\n"
        "ds_read_b64 v[30:31], v104 offset:20864\n"
        "v_xor_b32 v101, 32768, v101\n"
        "s_waitcnt lgkmcnt(10)\n"
        "v_mfma_f32_16x16x32_bf16 v[48:51], a[56:59], v[92:95], v[48:51]\n"
        "v_xor_b32 v103, 32768, v103\n"
        "v_xor_b32 v104, 32768, v104\n"
        "v_mfma_f32_16x16x32_bf16 v[52:55], a[60:63], v[92:95], v[52:55]\n"
        "v_xor_b32 v105, 32768, v105\n"
        "s_waitcnt lgkmcnt(9)\n"
        "v_mfma_f32_16x16x32_bf16 v[48:51], a[64:67], v[112:115], v[48:51]\n"
        "s_waitcnt lgkmcnt(0)\n"
        "v_add_f32 v24, v24, v26\n"
        "v_add_f32 v25, v25, v27\n"
        "v_mfma_f32_16x16x32_bf16 v[52:55], a[68:71], v[112:115], v[52:55]\n"
        "v_add_f32 v28, v28, v30\n"
        "v_add_f32 v29, v29, v31\n"
        "v_mfma_f32_16x16x32_bf16 v[48:51], a[72:75], v[116:119], v[48:51]\n"
        "v_add_f32 v24, v24, v28\n"
        "v_add_f32 v25, v25, v29\n"
        "v_mfma_f32_16x16x32_bf16 v[52:55], a[76:79], v[116:119], v[52:55]\n"
        "v_mul_f32 v24, 0xbb124925, v24\n"
        "v_mul_f32 v25, 0x3b124925, v25\n"
        "v_mfma_f32_16x16x32_bf16 v[48:51], a[80:83], v[120:123], v[48:51]\n"
        "v_fma_f32 v25, -v24, v24, v25\n"
        "v_max_f32 v25, 0, v25\n"
        "v_mfma_f32_16x16x32_bf16 v[52:55], a[84:87], v[120:123], v[52:55]\n"
        "v_add_f32 v25, 0x3727c5ac, v25\n"
        "v_mfma_f32_16x16x32_bf16 v[48:51], a[88:91], v[124:127], v[48:51]\n"
        "v_rsq_f32 v25, v25\n"
        "v_mfma_f32_16x16x32_bf16 v[52:55], a[92:95], v[124:127], v[52:55]\n"
        "v_mul_f32 v26, 0xbfb8aa3b, v25\n"
        "v_mul_f32 v27, v25, v25\n"
        "v_mfma_f32_16x16x32_bf16 v[48:51], a[96:99], v[128:131], v[48:51]\n"
        "v_mfma_f32_16x16x32_bf16 v[52:55], a[100:103], v[128:131], v[52:55]\n"
        "v_mfma_f32_16x16x32_bf16 v[48:51], a[104:107], v[132:135], v[48:51]\n"
        "v_mfma_f32_16x16x32_bf16 v[52:55], a[108:111], v[132:135], v[52:55]\n"
        "v_mfma_f32_16x16x32_bf16 v[56:59], a[112:115], v[64:67], 0\n"
        "s_waitcnt vmcnt(1)\n"
        "v_readfirstlane_b32 s91, v138\n"
        "s_mov_b32 s88, 0\n"
        "s_add_u32 s97, s91, s90\n"
        "s_sub_u32 s97, s97, s96\n"
        "s_cmp_eq_u32 s91, -1\n"
        "s_cselect_b32 s97, 9999, s97\n"
        "s_cmp_ge_u32 %[s], 64\n"
        "s_cselect_b32 s97, 9999, s97\n"
        "s_cmp_gt_i32 s97, 480\n"
        "s_cbranch_scc1 Lna_%=\n"
        "buffer_load_dwordx4 v[0:3], v96, %[rs], %[x1] offen sc1\n"
        "buffer_load_dwordx4 v[4:7], v97, %[rs], %[x1] offen sc1\n"
        "buffer_load_dwordx4 v[8:11], v98, %[rs], %[x1] offen sc1\n"
        "buffer_load_dwordx4 v[12:15], v99, %[rs], %[x1] offen sc1\n"
        "s_mov_b32 s88, 1\n"
        "s_mov_b32 s89, 0\n"
        "Lna_%=:\n"
        "v_mfma_f32_16x16x32_bf16 v[60:63], a[116:119], v[64:67], 0\n"
        "buffer_store_dword v140, v102, %[rs], s92 offen sc1\n"
        "v_fmac_f32 v48, v24, v164\n"
        "v_fmac_f32 v49, v24, v165\n"
        "v_mfma_f32_16x16x32_bf16 v[56:59], a[120:123], v[68:71], v[56:59]\n"
        "v_fmac_f32 v50, v24, v166\n"
        "v_fmac_f32 v51, v24, v167\n"
        "v_fmac_f32 v52, v24, v168\n"
        "v_mfma_f32_16x16x32_bf16 v[60:63], a[124:127], v[68:71], v[60:63]\n"
        "v_fmac_f32 v53, v24, v169\n"
        "v_fmac_f32 v54, v24, v170\n"
        "v_fmac_f32 v55, v24, v171\n"
        "v_mfma_f32_16x16x32_bf16 v[56:59], a[128:131], v[72:75], v[56:59]\n"
        "v_mul_f32 v16, v26, v48\n"
        "v_mul_f32 v17, v26, v49\n"
        "v_mul_f32 v18, v26, v50\n"
        "v_mfma_f32_16x16x32_bf16 v[60:63], a[132:135], v[72:75], v[60:63]\n"
        "v_mul_f32 v19, v26, v51\n"
        "v_mul_f32 v20, v26, v52\n"
        "v_mul_f32 v21, v26, v53\n"
        "v_mfma_f32_16x16x32_bf16 v[56:59], a[136:139], v[76:79], v[56:59]\n"
        "v_mul_f32 v22, v26, v54\n"
        "v_mul_f32 v23, v26, v55\n"
        "v_mfma_f32_16x16x32_bf16 v[60:63], a[140:143], v[76:79], v[60:63]\n"
        "v_exp_f32 v16, v16\n"
        "v_mfma_f32_16x16x32_bf16 v[56:59], a[144:147], v[80:83], v[56:59]\n"
        "v_exp_f32 v17, v17\n"
        "v_mfma_f32_16x16x32_bf16 v[60:63], a[148:151], v[80:83], v[60:63]\n"
        "v_exp_f32 v18, v18\n"
        "v_mfma_f32_16x16x32_bf16 v[56:59], a[152:155], v[84:87], v[56:59]\n"
        "v_exp_f32 v19, v19\n"
        "v_mfma_f32_16x16x32_bf16 v[60:63], a[156:159], v[84:87], v[60:63]\n"
        "v_exp_f32 v20, v20\n"
        "v_mfma_f32_16x16x32_bf16 v[56:59], a[160:163], v[88:91], v[56:59]\n"
        "v_exp_f32 v21, v21\n"
        "v_mfma_f32_16x16x32_bf16 v[60:63], a[164:167], v[88:91], v[60:63]\n"
        "v_exp_f32 v22, v22\n"
        "v_mfma_f32_16x16x32_bf16 v[56:59], a[168:171], v[92:95], v[56:59]\n"
        "v_exp_f32 v23, v23\n"
        "v_add_f32 v16, 1.0, v16\n"
        "v_mfma_f32_16x16x32_bf16 v[60:63], a[172:175], v[92:95], v[60:63]\n"
        "v_add_f32 v17, 1.0, v17\n"
        "v_add_f32 v18, 1.0, v18\n"
        "v_add_f32 v19, 1.0, v19\n"
        "v_mfma_f32_16x16x32_bf16 v[56:59], a[176:179], v[112:115], v[56:59]\n"
        "v_add_f32 v20, 1.0, v20\n"
        "v_add_f32 v21, 1.0, v21\n"
        "v_add_f32 v22, 1.0, v22\n"
        "v_mfma_f32_16x16x32_bf16 v[60:63], a[180:183], v[112:115], v[60:63]\n"
        "v_add_f32 v23, 1.0, v23\n"
        "v_rcp_f32 v16, v16\n"
        "v_mfma_f32_16x16x32_bf16 v[56:59], a[184:187], v[116:119], v[56:59]\n"
        "v_rcp_f32 v17, v17\n"
        "v_mfma_f32_16x16x32_bf16 v[60:63], a[188:191], v[116:119], v[60:63]\n"
        "v_rcp_f32 v18, v18\n"
        "v_mfma_f32_16x16x32_bf16 v[56:59], a[192:195], v[120:123], v[56:59]\n"
        "v_rcp_f32 v19, v19\n"
        "v_mfma_f32_16x16x32_bf16 v[60:63], a[196:199], v[120:123], v[60:63]\n"
        "v_rcp_f32 v20, v20\n"
        "v_mfma_f32_16x16x32_bf16 v[56:59], a[200:203], v[124:127], v[56:59]\n"
        "v_rcp_f32 v21, v21\n"
        "v_mfma_f32_16x16x32_bf16 v[60:63], a[204:207], v[124:127], v[60:63]\n"
        "v_rcp_f32 v22, v22\n"
        "v_mfma_f32_16x16x32_bf16 v[56:59], a[208:211], v[128:131], v[56:59]\n"
        "v_rcp_f32 v23, v23\n"
        "v_mul_f32 v48, v27, v48\n"
        "v_mfma_f32_16x16x32_bf16 v[60:63], a[212:215], v[128:131], v[60:63]\n"
        "v_mul_f32 v49, v27, v49\n"
        "v_mul_f32 v50, v27, v50\n"
        "v_mul_f32 v51, v27, v51\n"
        "v_mfma_f32_16x16x32_bf16 v[56:59], a[216:219], v[132:135], v[56:59]\n"
        "v_mul_f32 v52, v27, v52\n"
        "v_mul_f32 v53, v27, v53\n"
        "v_mul_f32 v54, v27, v54\n"
        "v_mfma_f32_16x16x32_bf16 v[60:63], a[220:223], v[132:135], v[60:63]\n"
        "v_mul_f32 v55, v27, v55\n"
        "v_mul_f32 v16, v16, v48\n"
        "v_mul_f32 v17, v17, v49\n"
        "v_mul_f32 v18, v18, v50\n"
        "v_mul_f32 v19, v19, v51\n"
        "v_mul_f32 v20, v20, v52\n"
        "v_mul_f32 v21, v21, v53\n"
        "v_mul_f32 v22, v22, v54\n"
        "v_mul_f32 v23, v23, v55\n"
        "v_pk_fma_f32 v[56:57], v[172:173], v[24:25], v[56:57] op_sel_hi:[1,0,1]\n"
        "v_pk_fma_f32 v[58:59], v[174:175], v[24:25], v[58:59] op_sel_hi:[1,0,1]\n"
        "v_pk_fma_f32 v[60:61], v[176:177], v[24:25], v[60:61] op_sel_hi:[1,0,1]\n"
        "v_pk_fma_f32 v[62:63], v[178:179], v[24:25], v[62:63] op_sel_hi:[1,0,1]\n"
        "v_pk_mul_f32 v[16:17], v[16:17], v[56:57]\n"
        "v_pk_mul_f32 v[18:19], v[18:19], v[58:59]\n"
        "v_pk_mul_f32 v[20:21], v[20:21], v[60:61]\n"
        "v_pk_mul_f32 v[22:23], v[22:23], v[62:63]\n"
        "v_cvt_pk_bf16_f32 v144, v16, v17\n"
        "v_cvt_pk_bf16_f32 v145, v18, v19\n"
        "v_cvt_pk_bf16_f32 v146, v20, v21\n"
        "v_cvt_pk_bf16_f32 v147, v22, v23\n"
        "v_min_u32 v144, -2, v144\n"
        "v_min_u32 v145, -2, v145\n"
        "v_min_u32 v146, -2, v146\n"
        "v_min_u32 v147, -2, v147\n"
        "s_waitcnt vmcnt(0)\n"
        "buffer_store_dwordx4 v[144:147], v100, %[rs], %[hb] offen sc1\n"
        "s_add_u32 s92, %[hb], 917504\n"
        "buffer_store_dwordx4 v[140:143], v100, %[rs], s92 offen sc1\n"
        "s_add_u32 %[hb], %[hb], 114688\n"
        "s_cmp_eq_u32 s88, 1\n"
        "s_cbranch_scc1 Lchk_%=\n"
        "s_cmp_lt_u32 %[s], 64\n"
        "s_cbranch_scc1 Ltop_%=\n"
        "s_branch Lend_%=\n"
        "Lwait_%=:\n"
        "s_mov_b32 s89, 96\n"
        "s_sub_u32 %[slowc], %[slowc], 1\n"
        "s_cbranch_scc0 Lw2_%=\n"
        "s_mov_b32 %[capv], 0\n"
        "v_mov_b32 v136, 1\n"
        "s_mov_b32 s94, 4227328\n"
        "buffer_store_dword v136, off, %[rs], s94 sc1\n"
        "s_mov_b64 %[live], 0\n"
        "Lw2_%=:\n"
        "s_sleep 2\n"
        "buffer_load_dwordx4 v[0:3], v96, %[rs], %[x1] offen sc1\n"
        "buffer_load_dwordx4 v[4:7], v97, %[rs], %[x1] offen sc1\n"
        "buffer_load_dwordx4 v[8:11], v98, %[rs], %[x1] offen sc1\n"
        "buffer_load_dwordx4 v[12:15], v99, %[rs], %[x1] offen sc1\n"
        "s_waitcnt vmcnt(0)\n"
        "s_branch Lchk_%=\n"
        "Lend_%=:\n"
        "s_waitcnt vmcnt(0) lgkmcnt(0)\n"
        "s_endpgm\n"
        : "+{v[96:111]}"(A1), [s] "+s"(s), [x1] "+s"(x1), [hb] "+s"(hb), [bb] "+s"(bb), [capv] "+s"(capv), [slowc] "+s"(slowc), [live] "=&s"(live)
        : [rs] "s"(rs), "{a[0:7]}"(W[0]), "{a[8:15]}"(W[1]), "{a[16:23]}"(W[2]), "{a[24:31]}"(W[3]), "{a[32:39]}"(W[4]), "{a[40:47]}"(W[5]), "{a[48:55]}"(W[6]), "{a[56:63]}"(W[7]), "{a[64:71]}"(W[8]), "{a[72:79]}"(W[9]), "{a[80:87]}"(W[10]), "{a[88:95]}"(W[11]), "{a[96:103]}"(W[12]), "{a[104:111]}"(W[13]), "{a[112:119]}"(W[14]), "{a[120:127]}"(W[15]), "{a[128:135]}"(W[16]), "{a[136:143]}"(W[17]), "{a[144:151]}"(W[18]), "{a[152:159]}"(W[19]), "{a[160:167]}"(W[20]), "{a[168:175]}"(W[21]), "{a[176:183]}"(W[22]), "{a[184:191]}"(W[23]), "{a[192:199]}"(W[24]), "{a[200:207]}"(W[25]), "{a[208:215]}"(W[26]), "{a[216:223]}"(W[27])
        : "vcc", "scc", "memory", "v255", "a255", "s86", "s87", "s88", "s89", "s90", "s91", "s92", "s93", "s94", "s95", "s96", "s97", "s98", "s99");
    __builtin_amdgcn_endpgm();
}
DI void role_G1(Ctx& C, const Params& p, int th, int fs) {
    const int lane = C.lane, w = C.w, l15 = lane & 15, g = lane >> 4;
    char* sm = C.sm;
    u32x8 W[28];
    {
        if (MIX && C.layer == 0) {
            unsigned x[28];
#pragma unroll
            for (int i = 0; i < 28; ++i) {
                const unsigned u = i * 56 + fs * 4 + w, st = u & 63, pc = u >> 6;
                x[i] = __builtin_amdgcn_raw_buffer_load_b32(C.rs, lane * 64, O_WS + st * 100352u + 2048u + (pc < 24u ? pc : 23u) * 4096u, 0);
            }
#pragma unroll
            for (int i = 0; i < 28; ++i) asm volatile("" :: "v"(x[i]));
            SB;
        }
        u32x4 t[2][28];
#pragma unroll
        for (int m = 0; m < 2; ++m) {
            const unsigned short* src = (m ? p.wu : p.wg) + fs * 128 + l15 * 8 + (size_t)(threadIdx.x >> 4) * FF;
#pragma unroll
            for (int it = 0; it < 28; ++it) t[m][it] = *(const u32x4*)(src + (size_t)it * 16 * FF);
        }
        SB;
        sfor<0, 2>([&](auto m) {
            if (m) bar();
#pragma unroll
            for (int it = 0; it < 28; ++it) sts16(sm, (it * 16 + (threadIdx.x >> 4)) * 272 + l15 * 16, t[m][it]);
            bar();
            sfor<0, 14>([&](auto ks) {
                const unsigned o = (32 * ks + 8 * g + (l15 >> 2)) * 272 + w * 64 + (l15 & 3) * 16;
                const u32x2 a = ldtr(sm, o), b = ldtr(sm, o + 1088), c2 = ldtr(sm, o + 8), d2 = ldtr(sm, o + 1096);
                W[m * 14 + ks] = (u32x8){a[0], a[1], b[0], b[1], c2[0], c2[1], d2[0], d2[1]};
                WR<m * 14 + ks>::pin(W[m * 14 + ks]);
            });
            SB;
        });
    }
    bar();
    const int row = threadIdx.x >> 4;
    u32x16 A1;
#pragma unroll
    for (int i = 0; i < 16; ++i) A1[i] = 0;
#pragma unroll
    for (int i = 0; i < 4; ++i) A1[i] = (i < 3 || row < 8) ? (row + 16 * i) * 512 + (th * 16 + l15) * 16 : OOB;
    A1[4] = lane * 16;
    A1[5] = threadIdx.x * 16;
    A1[6] = (fs == 0 && threadIdx.x == 0) ? 512 + th * 128 : OOB;
    A1[7] = w * 128 + l15 * 8 + (lane >> 5) * 4;
    A1[8] = l15 * 8;
    A1[9] = lane * 16;
    g1_asm(W, A1, C.rw, O_X1 + C.layer * XSZ, O_HID + C.layer * HIDSZ + (th * 56 + 4 * fs + w) * 1024u, C.hop0 + 7168u, w, O_DBG + C.id * 8192u);
}

DI void g2_asm(const u32x8 (&W)[49], u32x16 A1, i32x4 rs, i32x4 ro, unsigned s, unsigned hb, unsigned k0, unsigned x1, unsigned xn, unsigned xr, unsigned st, unsigned bb, unsigned nr, unsigned wv, unsigned lay, unsigned dbg) {
    unsigned capv = SPIN_CAP, slowc = 1u << 16; unsigned long long live;
    asm volatile(
        "s_mov_b64 %[live], -1\n"
        "v_mov_b64 v[112:113], -1\n"
        "v_mov_b64 v[114:115], -1\n"
        "s_mov_b32 s91, -1\n"
        "s_mov_b32 s86, 1312\n"
        "Ltop_%=:\n"
        "s_lshr_b32 s92, %[s], 1\n"
        "s_lshl_b32 s95, s92, 14\n"
        "s_add_u32 s95, s95, %[bb]\n"
        "s_and_b32 s94, %[s], 1\n"
        "s_lshl_b32 s94, s94, 7\n"
        "s_add_u32 s95, s95, s94\n"
        "s_cmp_lg_u32 s91, -1\n"
        "s_cbranch_scc1 Lbcd_%=\n"
        "s_mov_b32 s93, %[capv]\n"
        "Lbc_%=:\n"
        "buffer_load_dword v88, off, %[rs], s95 sc1\n"
        "s_sub_u32 s93, s93, 1\n"
        "s_cbranch_scc1 Lbcd_%=\n"
        "s_waitcnt vmcnt(0)\n"
        "v_readfirstlane_b32 s91, v88\n"
        "s_cmp_lg_u32 s91, -1\n"
        "s_cbranch_scc1 Lbcd_%=\n"
        "s_sleep 3\n"
        "s_branch Lbc_%=\n"
        "Lbcd_%=:\n"
        "s_add_u32 s91, s91, s86\n"
        "s_lshr_b32 s92, %[s], 1\n"
        "s_mul_i32 s92, s92, 28672\n"
        "s_and_b32 s94, %[s], 1\n"
        "s_lshl_b32 s94, s94, 8\n"
        "s_add_u32 s87, s92, s94\n"
        "s_add_u32 s90, s87, %[x1]\n"
        "Lbw_%=:\n"
        "s_memrealtime s[96:97]\n"
        "s_waitcnt lgkmcnt(0)\n"
        "s_lshl_b32 s96, s96, 4\n"
        "s_sub_u32 s96, s91, s96\n"
        "s_sub_u32 s97, s96, 144\n"
        "s_cmp_lt_u32 s97, 9600\n"
        "s_cbranch_scc1 Lbw_%=\n"
        "s_min_i32 s97, s96, 144\n"
        "Lbwf_%=:\n"
        "s_sub_i32 s97, s97, 21\n"
        "s_cmp_gt_i32 s97, 60\n"
        "s_cbranch_scc1 Lbwf_%=\n"
        "s_cmp_gt_i32 s96, 0\n"
        "s_cselect_b32 s85, -8, 0\n"
        "Lwd_%=:\n"
        "buffer_load_dword v88, v96, %[rs], %[hb] offen sc1\n"
        "s_mov_b32 s95, %[k0]\n"
        "s_add_u32 s94, s95, %[hb]\n"
        "buffer_load_dwordx4 v[0:3], v100, %[rs], s94 offen offset:0 sc1\n"
        "buffer_load_dwordx4 v[4:7], v100, %[rs], s94 offen offset:1024 sc1\n"
        "s_add_u32 s95, s95, 2048\n"
        "s_cmp_ge_u32 s95, 14336\n"
        "s_cselect_b32 s95, 0, s95\n"
        "s_add_u32 s94, s95, %[hb]\n"
        "buffer_load_dwordx4 v[8:11], v100, %[rs], s94 offen offset:0 sc1\n"
        "buffer_load_dwordx4 v[12:15], v100, %[rs], s94 offen offset:1024 sc1\n"
        "s_add_u32 s95, s95, 2048\n"
        "s_cmp_ge_u32 s95, 14336\n"
        "s_cselect_b32 s95, 0, s95\n"
        "s_add_u32 s94, s95, %[hb]\n"
        "buffer_load_dwordx4 v[16:19], v100, %[rs], s94 offen offset:0 sc1\n"
        "buffer_load_dwordx4 v[20:23], v100, %[rs], s94 offen offset:1024 sc1\n"
        "s_add_u32 s95, s95, 2048\n"
        "s_cmp_ge_u32 s95, 14336\n"
        "s_cselect_b32 s95, 0, s95\n"
        "s_add_u32 s94, s95, %[hb]\n"
        "buffer_load_dwordx4 v[24:27], v100, %[rs], s94 offen offset:0 sc1\n"
        "buffer_load_dwordx4 v[28:31], v100, %[rs], s94 offen offset:1024 sc1\n"
        "s_add_u32 s95, s95, 2048\n"
        "s_cmp_ge_u32 s95, 14336\n"
        "s_cselect_b32 s95, 0, s95\n"
        "s_add_u32 s94, s95, %[hb]\n"
        "buffer_load_dwordx4 v[32:35], v100, %[rs], s94 offen offset:0 sc1\n"
        "buffer_load_dwordx4 v[36:39], v100, %[rs], s94 offen offset:1024 sc1\n"
        "s_add_u32 s95, s95, 2048\n"
        "s_cmp_ge_u32 s95, 14336\n"
        "s_cselect_b32 s95, 0, s95\n"
        "s_add_u32 s94, s95, %[hb]\n"
        "buffer_load_dwordx4 v[40:43], v100, %[rs], s94 offen offset:0 sc1\n"
        "buffer_load_dwordx4 v[44:47], v100, %[rs], s94 offen offset:1024 sc1\n"
        "s_add_u32 s95, s95, 2048\n"
        "s_cmp_ge_u32 s95, 14336\n"
        "s_cselect_b32 s95, 0, s95\n"
        "s_add_u32 s94, s95, %[hb]\n"
        "buffer_load_dwordx4 v[48:51], v100, %[rs], s94 offen offset:0 sc1\n"
        "buffer_load_dwordx4 v[52:55], v100, %[rs], s94 offen offset:1024 sc1\n"
        "buffer_load_dwordx4 v[84:87], v97, %[rs], s90 offen sc1\n"
        "buffer_load_dword v91, v110, %[rs], 0 offen\n"
        "buffer_load_dword v91, v110, %[rs], 0 offen\n"
        "Lmf_%=:\n"
        "s_waitcnt vmcnt(17)\n"
        "v_cmp_eq_u32 vcc, -1, v88\n"
        "s_and_b64 vcc, vcc, %[live]\n"
        "s_cbranch_vccnz Lretry_%=\n"
        "s_waitcnt vmcnt(16)\n"
        "v_mfma_f32_16x16x32_bf16 v[56:59], a[0:3], v[0:3], 0\n"
        "s_add_u32 s95, %[s], %[nr]\n"
        "v_max3_u32 v88, v0, v1, v88\n"
        "v_max3_u32 v88, v2, v3, v88\n"
        "v_mfma_f32_16x16x32_bf16 v[60:63], a[8:11], v[0:3], 0\n"
        "v_mfma_f32_16x16x32_bf16 v[64:67], a[16:19], v[0:3], 0\n"
        "s_and_b32 s93, s95, 1\n"
        "v_mfma_f32_16x16x32_bf16 v[68:71], a[24:27], v[0:3], 0\n"
        "s_lshl_b32 s93, s93, 7\n"
        "v_mfma_f32_16x16x32_bf16 v[72:75], a[32:35], v[0:3], 0\n"
        "s_lshr_b32 s95, s95, 1\n"
        "v_mfma_f32_16x16x32_bf16 v[76:79], a[40:43], v[0:3], 0\n"
        "s_lshl_b32 s95, s95, 14\n"
        "v_mfma_f32_16x16x32_bf16 v[80:83], a[48:51], v[0:3], 0\n"
        "s_add_u32 s95, s95, %[bb]\n"
        "s_waitcnt vmcnt(15)\n"
        "v_mfma_f32_16x16x32_bf16 v[56:59], a[4:7], v[4:7], v[56:59]\n"
        "s_add_u32 s95, s95, s93\n"
        "v_max3_u32 v88, v4, v5, v88\n"
        "v_max3_u32 v88, v6, v7, v88\n"
        "v_mfma_f32_16x16x32_bf16 v[60:63], a[12:15], v[4:7], v[60:63]\n"
        "v_mfma_f32_16x16x32_bf16 v[64:67], a[20:23], v[4:7], v[64:67]\n"
        "v_mfma_f32_16x16x32_bf16 v[68:71], a[28:31], v[4:7], v[68:71]\n"
        "v_mfma_f32_16x16x32_bf16 v[72:75], a[36:39], v[4:7], v[72:75]\n"
        "v_mfma_f32_16x16x32_bf16 v[76:79], a[44:47], v[4:7], v[76:79]\n"
        "v_mfma_f32_16x16x32_bf16 v[80:83], a[52:55], v[4:7], v[80:83]\n"
        "s_waitcnt vmcnt(14)\n"
        "v_mfma_f32_16x16x32_bf16 v[56:59], a[56:59], v[8:11], v[56:59]\n"
        "v_max3_u32 v88, v8, v9, v88\n"
        "v_max3_u32 v88, v10, v11, v88\n"
        "v_mfma_f32_16x16x32_bf16 v[60:63], a[64:67], v[8:11], v[60:63]\n"
        "v_mfma_f32_16x16x32_bf16 v[64:67], a[72:75], v[8:11], v[64:67]\n"
        "v_mfma_f32_16x16x32_bf16 v[68:71], a[80:83], v[8:11], v[68:71]\n"
        "v_mfma_f32_16x16x32_bf16 v[72:75], a[88:91], v[8:11], v[72:75]\n"
        "v_mfma_f32_16x16x32_bf16 v[76:79], a[96:99], v[8:11], v[76:79]\n"
        "v_mfma_f32_16x16x32_bf16 v[80:83], a[104:107], v[8:11], v[80:83]\n"
        "s_waitcnt vmcnt(13)\n"
        "v_mfma_f32_16x16x32_bf16 v[56:59], a[60:63], v[12:15], v[56:59]\n"
        "v_max3_u32 v88, v12, v13, v88\n"
        "v_max3_u32 v88, v14, v15, v88\n"
        "v_mfma_f32_16x16x32_bf16 v[60:63], a[68:71], v[12:15], v[60:63]\n"
        "v_mfma_f32_16x16x32_bf16 v[64:67], a[76:79], v[12:15], v[64:67]\n"
        "v_mfma_f32_16x16x32_bf16 v[68:71], a[84:87], v[12:15], v[68:71]\n"
        "v_mfma_f32_16x16x32_bf16 v[72:75], a[92:95], v[12:15], v[72:75]\n"
        "v_mfma_f32_16x16x32_bf16 v[76:79], a[100:103], v[12:15], v[76:79]\n"
        "v_mfma_f32_16x16x32_bf16 v[80:83], a[108:111], v[12:15], v[80:83]\n"
        "s_waitcnt vmcnt(12)\n"
        "v_mfma_f32_16x16x32_bf16 v[56:59], a[112:115], v[16:19], v[56:59]\n"
        "v_max3_u32 v88, v16, v17, v88\n"
        "v_max3_u32 v88, v18, v19, v88\n"
        "v_mfma_f32_16x16x32_bf16 v[60:63], a[120:123], v[16:19], v[60:63]\n"
        "v_mfma_f32_16x16x32_bf16 v[64:67], a[128:131], v[16:19], v[64:67]\n"
        "v_mfma_f32_16x16x32_bf16 v[68:71], a[136:139], v[16:19], v[68:71]\n"
        "v_mfma_f32_16x16x32_bf16 v[72:75], a[144:147], v[16:19], v[72:75]\n"
        "buffer_load_dword v89, off, %[rs], s95 sc1\n"
        "v_mfma_f32_16x16x32_bf16 v[76:79], a[152:155], v[16:19], v[76:79]\n"
        "v_mfma_f32_16x16x32_bf16 v[80:83], a[160:163], v[16:19], v[80:83]\n"
        "s_waitcnt vmcnt(12)\n"
        "v_mfma_f32_16x16x32_bf16 v[56:59], a[116:119], v[20:23], v[56:59]\n"
        "v_max3_u32 v88, v20, v21, v88\n"
        "v_max3_u32 v88, v22, v23, v88\n"
        "v_mfma_f32_16x16x32_bf16 v[60:63], a[124:127], v[20:23], v[60:63]\n"
        "v_mfma_f32_16x16x32_bf16 v[64:67], a[132:135], v[20:23], v[64:67]\n"
        "v_mfma_f32_16x16x32_bf16 v[68:71], a[140:143], v[20:23], v[68:71]\n"
        "v_mfma_f32_16x16x32_bf16 v[72:75], a[148:151], v[20:23], v[72:75]\n"
        "v_mfma_f32_16x16x32_bf16 v[76:79], a[156:159], v[20:23], v[76:79]\n"
        "v_mfma_f32_16x16x32_bf16 v[80:83], a[164:167], v[20:23], v[80:83]\n"
        "s_waitcnt vmcnt(11)\n"
        "v_mfma_f32_16x16x32_bf16 v[56:59], a[168:171], v[24:27], v[56:59]\n"
        "v_max3_u32 v88, v24, v25, v88\n"
        "v_max3_u32 v88, v26, v27, v88\n"
        "v_mfma_f32_16x16x32_bf16 v[60:63], a[176:179], v[24:27], v[60:63]\n"
        "v_mfma_f32_16x16x32_bf16 v[64:67], a[184:187], v[24:27], v[64:67]\n"
        "v_mfma_f32_16x16x32_bf16 v[68:71], a[192:195], v[24:27], v[68:71]\n"
        "v_mfma_f32_16x16x32_bf16 v[72:75], a[200:203], v[24:27], v[72:75]\n"
        "v_mfma_f32_16x16x32_bf16 v[76:79], a[208:211], v[24:27], v[76:79]\n"
        "v_mfma_f32_16x16x32_bf16 v[80:83], a[216:219], v[24:27], v[80:83]\n"
        "s_waitcnt vmcnt(10)\n"
        "v_mfma_f32_16x16x32_bf16 v[56:59], a[172:175], v[28:31], v[56:59]\n"
        "v_max3_u32 v88, v28, v29, v88\n"
        "v_max3_u32 v88, v30, v31, v88\n"
        "v_mfma_f32_16x16x32_bf16 v[60:63], a[180:183], v[28:31], v[60:63]\n"
        "v_mfma_f32_16x16x32_bf16 v[64:67], a[188:191], v[28:31], v[64:67]\n"
        "v_mfma_f32_16x16x32_bf16 v[68:71], a[196:199], v[28:31], v[68:71]\n"
        "v_mfma_f32_16x16x32_bf16 v[72:75], a[204:207], v[28:31], v[72:75]\n"
        "v_mfma_f32_16x16x32_bf16 v[76:79], a[212:215], v[28:31], v[76:79]\n"
        "v_mfma_f32_16x16x32_bf16 v[80:83], a[220:223], v[28:31], v[80:83]\n"
        "s_waitcnt vmcnt(9)\n"
        "v_mfma_f32_16x16x32_bf16 v[56:59], a[224:227], v[32:35], v[56:59]\n"
        "v_max3_u32 v88, v32, v33, v88\n"
        "v_max3_u32 v88, v34, v35, v88\n"
        "v_mfma_f32_16x16x32_bf16 v[60:63], a[232:235], v[32:35], v[60:63]\n"
        "v_mfma_f32_16x16x32_bf16 v[64:67], a[240:243], v[32:35], v[64:67]\n"
        "v_mfma_f32_16x16x32_bf16 v[68:71], a[248:251], v[32:35], v[68:71]\n"
        "v_mfma_f32_16x16x32_bf16 v[72:75], v[120:123], v[32:35], v[72:75]\n"
        "v_mfma_f32_16x16x32_bf16 v[76:79], v[128:131], v[32:35], v[76:79]\n"
        "v_mfma_f32_16x16x32_bf16 v[80:83], v[136:139], v[32:35], v[80:83]\n"
        "s_waitcnt vmcnt(8)\n"
        "v_mfma_f32_16x16x32_bf16 v[56:59], a[228:231], v[36:39], v[56:59]\n"
        "v_max3_u32 v88, v36, v37, v88\n"
        "v_max3_u32 v88, v38, v39, v88\n"
        "v_mfma_f32_16x16x32_bf16 v[60:63], a[236:239], v[36:39], v[60:63]\n"
        "s_memrealtime s[96:97]\n"
        "v_mfma_f32_16x16x32_bf16 v[64:67], a[244:247], v[36:39], v[64:67]\n"
        "v_mfma_f32_16x16x32_bf16 v[68:71], a[252:255], v[36:39], v[68:71]\n"
        "v_mfma_f32_16x16x32_bf16 v[72:75], v[124:127], v[36:39], v[72:75]\n"
        "v_mfma_f32_16x16x32_bf16 v[76:79], v[132:135], v[36:39], v[76:79]\n"
        "v_mfma_f32_16x16x32_bf16 v[80:83], v[140:143], v[36:39], v[80:83]\n"
        "s_cmp_lg_u32 %[lay], 0\n"
        "s_cbranch_scc1 Lla_%=\n"
        "Ltl_%=:\n"
        "s_waitcnt vmcnt(7)\n"
        "v_mfma_f32_16x16x32_bf16 v[56:59], v[144:147], v[40:43], v[56:59]\n"
        "v_max3_u32 v88, v40, v41, v88\n"
        "v_max3_u32 v88, v42, v43, v88\n"
        "s_waitcnt vmcnt(6)\n"
        "v_mfma_f32_16x16x32_bf16 v[56:59], v[148:151], v[44:47], v[56:59]\n"
        "v_max3_u32 v88, v44, v45, v88\n"
        "v_max3_u32 v88, v46, v47, v88\n"
        "s_waitcnt vmcnt(5)\n"
        "v_mfma_f32_16x16x32_bf16 v[56:59], v[200:203], v[48:51], v[56:59]\n"
        "v_max3_u32 v88, v48, v49, v88\n"
        "v_max3_u32 v88, v50, v51, v88\n"
        "s_waitcnt vmcnt(4)\n"
        "v_mfma_f32_16x16x32_bf16 v[56:59], v[204:207], v[52:55], v[56:59]\n"
        "v_max3_u32 v88, v52, v53, v88\n"
        "v_max3_u32 v88, v54, v55, v88\n"
        "v_mfma_f32_16x16x32_bf16 v[60:63], v[152:155], v[40:43], v[60:63]\n"
        "s_waitcnt vmcnt(3)\n"
        "v_max3_u32 v88, v84, v85, v88\n"
        "v_max3_u32 v88, v86, v87, v88\n"
        "v_mfma_f32_16x16x32_bf16 v[60:63], v[156:159], v[44:47], v[60:63]\n"
        "v_cmp_eq_u32 vcc, -1, v88\n"
        "v_lshlrev_b32 v92, 16, v84\n"
        "v_mfma_f32_16x16x32_bf16 v[60:63], v[208:211], v[48:51], v[60:63]\n"
        "v_lshlrev_b32 v94, 16, v85\n"
        "v_lshlrev_b32 v116, 16, v86\n"
        "v_mfma_f32_16x16x32_bf16 v[60:63], v[212:215], v[52:55], v[60:63]\n"
        "ds_write_b128 v98, v[56:59] offset:0\n"
        "v_lshlrev_b32 v118, 16, v87\n"
        "v_mfma_f32_16x16x32_bf16 v[64:67], v[160:163], v[40:43], v[64:67]\n"
        "v_and_b32 v93, 0xffff0000, v84\n"
        "v_and_b32 v95, 0xffff0000, v85\n"
        "v_mfma_f32_16x16x32_bf16 v[64:67], v[164:167], v[44:47], v[64:67]\n"
        "v_and_b32 v117, 0xffff0000, v86\n"
        "v_and_b32 v119, 0xffff0000, v87\n"
        "v_mfma_f32_16x16x32_bf16 v[64:67], v[216:219], v[48:51], v[64:67]\n"
        "s_add_u32 s89, s87, %[xr]\n"
        "s_add_u32 s88, s87, %[xn]\n"
        "v_mfma_f32_16x16x32_bf16 v[64:67], v[220:223], v[52:55], v[64:67]\n"
        "ds_write_b128 v98, v[60:63] offset:1024\n"
        "v_mfma_f32_16x16x32_bf16 v[68:71], v[168:171], v[40:43], v[68:71]\n"
        "s_add_u32 s92, s88, 229376\n"
        "s_add_u32 s94, s89, 229376\n"
        "buffer_store_dwordx4 v[112:115], v109, %[rs], s92 offen sc1\n"
        "v_mfma_f32_16x16x32_bf16 v[68:71], v[172:175], v[44:47], v[68:71]\n"
        "v_mfma_f32_16x16x32_bf16 v[68:71], v[224:227], v[48:51], v[68:71]\n"
        "buffer_store_dwordx4 v[112:115], v109, %[rs], s94 offen sc1\n"
        "v_mfma_f32_16x16x32_bf16 v[68:71], v[228:231], v[52:55], v[68:71]\n"
        "ds_write_b128 v98, v[64:67] offset:2048\n"
        "v_mfma_f32_16x16x32_bf16 v[72:75], v[176:179], v[40:43], v[72:75]\n"
        "v_mfma_f32_16x16x32_bf16 v[72:75], v[180:183], v[44:47], v[72:75]\n"
        "v_mfma_f32_16x16x32_bf16 v[72:75], v[232:235], v[48:51], v[72:75]\n"
        "v_mfma_f32_16x16x32_bf16 v[72:75], v[236:239], v[52:55], v[72:75]\n"
        "s_nop 2\n"
        "ds_write_b128 v98, v[68:71] offset:3072\n"
        "v_mfma_f32_16x16x32_bf16 v[76:79], v[184:187], v[40:43], v[76:79]\n"
        "v_mfma_f32_16x16x32_bf16 v[76:79], v[188:191], v[44:47], v[76:79]\n"
        "v_mfma_f32_16x16x32_bf16 v[76:79], v[240:243], v[48:51], v[76:79]\n"
        "v_mfma_f32_16x16x32_bf16 v[76:79], v[244:247], v[52:55], v[76:79]\n"
        "ds_write_b128 v98, v[72:75] offset:4096\n"
        "v_mfma_f32_16x16x32_bf16 v[80:83], v[192:195], v[40:43], v[80:83]\n"
        "v_mfma_f32_16x16x32_bf16 v[80:83], v[196:199], v[44:47], v[80:83]\n"
        "v_mfma_f32_16x16x32_bf16 v[80:83], v[248:251], v[48:51], v[80:83]\n"
        "v_mfma_f32_16x16x32_bf16 v[80:83], v[252:255], v[52:55], v[80:83]\n"
        "s_nop 2\n"
        "ds_write_b128 v98, v[76:79] offset:5120\n"
        "s_and_b64 vcc, vcc, %[live]\n"
        "s_cbranch_vccnz Lretry_%=\n"
        "s_add_u32 s86, s86, s85\n"
        "s_min_u32 s86, s86, 1792\n"
        "ds_write_b128 v98, v[80:83] offset:6144\n"
        "s_mov_b32 s83, %[s]\n"
        "s_memrealtime s[96:97]\n"
        "v_mov_b32 v90, v96\n"
        "s_nop 2\n"
        "v_mov_b32 v90, v96\n"
        "s_nop 2\n"
        "v_mov_b32 v90, v96\n"
        "s_nop 2\n"
        "s_waitcnt lgkmcnt(0)\n"
        "s_barrier\n"
        "Lrd_%=:\n"
        "ds_read_b128 v[56:59], v99 offset:0\n"
        "ds_read_b128 v[60:63], v99 offset:256\n"
        "v_mov_b32 v90, v96\n"
        "ds_read_b128 v[64:67], v99 offset:7168\n"
        "ds_read_b128 v[68:71], v99 offset:7424\n"
        "v_mov_b32 v90, v96\n"
        "ds_read_b128 v[72:75], v99 offset:14336\n"
        "ds_read_b128 v[76:79], v99 offset:14592\n"
        "v_mov_b32 v90, v96\n"
        "ds_read_b128 v[80:83], v99 offset:21504\n"
        "s_waitcnt lgkmcnt(5)\n"
        "v_pk_add_f32 v[92:93], v[92:93], v[56:57]\n"
        "v_pk_add_f32 v[94:95], v[94:95], v[58:59]\n"
        "v_pk_add_f32 v[116:117], v[116:117], v[60:61]\n"
        "v_pk_add_f32 v[118:119], v[118:119], v[62:63]\n"
        "ds_read_b128 v[56:59], v99 offset:21760\n"
        "s_waitcnt lgkmcnt(4)\n"
        "v_pk_add_f32 v[92:93], v[92:93], v[64:65]\n"
        "v_pk_add_f32 v[94:95], v[94:95], v[66:67]\n"
        "v_pk_add_f32 v[116:117], v[116:117], v[68:69]\n"
        "v_pk_add_f32 v[118:119], v[118:119], v[70:71]\n"
        "s_waitcnt lgkmcnt(2)\n"
        "v_pk_add_f32 v[92:93], v[92:93], v[72:73]\n"
        "v_pk_add_f32 v[94:95], v[94:95], v[74:75]\n"
        "v_pk_add_f32 v[116:117], v[116:117], v[76:77]\n"
        "v_pk_add_f32 v[118:119], v[118:119], v[78:79]\n"
        "s_waitcnt lgkmcnt(0)\n"
        "v_pk_add_f32 v[92:93], v[92:93], v[80:81]\n"
        "v_pk_add_f32 v[94:95], v[94:95], v[82:83]\n"
        "v_pk_add_f32 v[116:117], v[116:117], v[56:57]\n"
        "v_pk_add_f32 v[118:119], v[118:119], v[58:59]\n"
        "s_nop 0\n"
        "v_cvt_pk_bf16_f32 v119, v118, v119\n"
        "v_cvt_pk_bf16_f32 v118, v116, v117\n"
        "v_cvt_pk_bf16_f32 v116, v92, v93\n"
        "v_cvt_pk_bf16_f32 v117, v94, v95\n"
        "s_cmp_lg_u32 %[lay], 0\n"
        "s_cbranch_scc1 Lout_%=\n"
        "v_lshlrev_b32 v56, 16, v116\n"
        "v_and_b32 v57, 0xffff0000, v116\n"
        "v_lshlrev_b32 v58, 16, v117\n"
        "v_and_b32 v59, 0xffff0000, v117\n"
        "v_lshlrev_b32 v60, 16, v118\n"
        "v_and_b32 v61, 0xffff0000, v118\n"
        "v_lshlrev_b32 v62, 16, v119\n"
        "v_and_b32 v63, 0xffff0000, v119\n"
        "v_pk_add_f32 v[64:65], v[56:57], v[58:59]\n"
        "v_pk_mul_f32 v[68:69], v[56:57], v[56:57]\n"
        "v_pk_add_f32 v[66:67], v[60:61], v[62:63]\n"
        "v_pk_mul_f32 v[70:71], v[58:59], v[58:59]\n"
        "v_pk_fma_f32 v[68:69], v[60:61], v[60:61], v[68:69]\n"
        "v_pk_add_f32 v[64:65], v[64:65], v[66:67]\n"
        "v_pk_fma_f32 v[70:71], v[62:63], v[62:63], v[70:71]\n"
        "v_add_f32 v64, v64, v65\n"
        "v_pk_add_f32 v[68:69], v[68:69], v[70:71]\n"
        "v_mul_f32 v64, v64, v103\n"
        "v_add_f32 v65, v68, v69\n"
        "v_mul_f32 v65, v65, v103\n"
        "s_lshr_b32 s84, %[s], 2\n"
        "s_lshl_b32 s84, s84, 14\n"
        "v_permlane32_swap_b32 v64, v65\n"
        "v_add_f32 v64, v64, v65\n"
        "v_mov_b32 v65, v64\n"
        "s_and_b32 s94, %[s], 3\n"
        "s_lshl_b32 s94, s94, 11\n"
        "v_permlane16_swap_b32 v64, v65\n"
        "v_add_f32 v64, v64, v65\n"
        "s_add_u32 s84, s84, s94\n"
        "s_add_u32 s84, s84, %[st]\n"
        "v_min_u32 v64, -2, v64\n"
        "buffer_store_dword v64, v104, %[rs], s84 offen sc1\n"
        "s_barrier\n"
        "v_min_u32 v116, -2, v116\n"
        "v_min_u32 v117, -2, v117\n"
        "v_min_u32 v118, -2, v118\n"
        "v_min_u32 v119, -2, v119\n"
        "s_waitcnt vmcnt(3)\n"
        "v_readfirstlane_b32 s91, v89\n"
        "s_mul_i32 s92, %[nr], 57344\n"
        "s_add_u32 %[hb], %[hb], s92\n"
        "s_add_u32 %[s], %[s], %[nr]\n"
        "s_lshr_b32 s92, %[s], 1\n"
        "s_mul_i32 s92, s92, 28672\n"
        "s_and_b32 s94, %[s], 1\n"
        "s_lshl_b32 s94, s94, 8\n"
        "s_add_u32 s87, s92, s94\n"
        "s_add_u32 s90, s87, %[x1]\n"
        "s_lshl_b32 s96, s96, 4\n"
        "s_add_u32 s97, s91, s86\n"
        "s_sub_u32 s96, s97, s96\n"
        "s_cmp_eq_u32 s91, -1\n"
        "s_cselect_b32 s96, 9999, s96\n"
        "s_cmp_ge_u32 %[s], 128\n"
        "s_cselect_b32 s96, 9999, s96\n"
        "s_cmp_lt_i32 s96, 640\n"
        "s_cselect_b32 s82, 1, 0\n"
        "s_sleep 5\n"
        "s_mov_b32 s93, %[capv]\n"
        "buffer_load_dwordx2 v[66:67], v105, %[rs], s84 offen offset:0 sc1\n"
        "buffer_load_dwordx2 v[68:69], v105, %[rs], s84 offen offset:512 sc1\n"
        "buffer_load_dwordx2 v[70:71], v105, %[rs], s84 offen offset:1024 sc1\n"
        "buffer_load_dwordx2 v[72:73], v105, %[rs], s84 offen offset:1536 sc1\n"
        "s_cmp_eq_u32 s82, 0\n"
        "s_cbranch_scc1 Lq0_%=\n"
        "buffer_load_dword v88, v96, %[rs], %[hb] offen sc1\n"
        "s_mov_b32 s95, %[k0]\n"
        "s_add_u32 s94, s95, %[hb]\n"
        "buffer_load_dwordx4 v[0:3], v100, %[rs], s94 offen offset:0 sc1\n"
        "buffer_load_dwordx4 v[4:7], v100, %[rs], s94 offen offset:1024 sc1\n"
        "s_add_u32 s95, s95, 2048\n"
        "s_cmp_ge_u32 s95, 14336\n"
        "s_cselect_b32 s95, 0, s95\n"
        "s_add_u32 s94, s95, %[hb]\n"
        "buffer_load_dwordx4 v[8:11], v100, %[rs], s94 offen offset:0 sc1\n"
        "buffer_load_dwordx4 v[12:15], v100, %[rs], s94 offen offset:1024 sc1\n"
        "s_add_u32 s95, s95, 2048\n"
        "s_cmp_ge_u32 s95, 14336\n"
        "s_cselect_b32 s95, 0, s95\n"
        "s_add_u32 s94, s95, %[hb]\n"
        "buffer_load_dwordx4 v[16:19], v100, %[rs], s94 offen offset:0 sc1\n"
        "buffer_load_dwordx4 v[20:23], v100, %[rs], s94 offen offset:1024 sc1\n"
        "s_add_u32 s95, s95, 2048\n"
        "s_cmp_ge_u32 s95, 14336\n"
        "s_cselect_b32 s95, 0, s95\n"
        "s_add_u32 s94, s95, %[hb]\n"
        "buffer_load_dwordx4 v[24:27], v100, %[rs], s94 offen offset:0 sc1\n"
        "buffer_load_dwordx4 v[28:31], v100, %[rs], s94 offen offset:1024 sc1\n"
        "s_add_u32 s95, s95, 2048\n"
        "s_cmp_ge_u32 s95, 14336\n"
        "s_cselect_b32 s95, 0, s95\n"
        "s_add_u32 s94, s95, %[hb]\n"
        "buffer_load_dwordx4 v[32:35], v100, %[rs], s94 offen offset:0 sc1\n"
        "buffer_load_dwordx4 v[36:39], v100, %[rs], s94 offen offset:1024 sc1\n"
        "s_add_u32 s95, s95, 2048\n"
        "s_cmp_ge_u32 s95, 14336\n"
        "s_cselect_b32 s95, 0, s95\n"
        "s_add_u32 s94, s95, %[hb]\n"
        "buffer_load_dwordx4 v[40:43], v100, %[rs], s94 offen offset:0 sc1\n"
        "buffer_load_dwordx4 v[44:47], v100, %[rs], s94 offen offset:1024 sc1\n"
        "s_add_u32 s95, s95, 2048\n"
        "s_cmp_ge_u32 s95, 14336\n"
        "s_cselect_b32 s95, 0, s95\n"
        "s_add_u32 s94, s95, %[hb]\n"
        "buffer_load_dwordx4 v[48:51], v100, %[rs], s94 offen offset:0 sc1\n"
        "buffer_load_dwordx4 v[52:55], v100, %[rs], s94 offen offset:1024 sc1\n"
        "buffer_load_dwordx4 v[84:87], v97, %[rs], s90 offen sc1\n"
        "s_mov_b32 s85, 0\n"
        "s_waitcnt vmcnt(16)\n"
        "s_branch Lqc_%=\n"
        "Lq_%=:\n"
        "s_sub_u32 s93, s93, 1\n"
        "s_cbranch_scc1 Lqt_%=\n"
        "buffer_load_dwordx2 v[66:67], v105, %[rs], s84 offen offset:0 sc1\n"
        "buffer_load_dwordx2 v[68:69], v105, %[rs], s84 offen offset:512 sc1\n"
        "buffer_load_dwordx2 v[70:71], v105, %[rs], s84 offen offset:1024 sc1\n"
        "buffer_load_dwordx2 v[72:73], v105, %[rs], s84 offen offset:1536 sc1\n"
        "Lq0_%=:\n"
        "s_waitcnt vmcnt(0)\n"
        "Lqc_%=:\n"
        "v_max3_u32 v76, v66, v67, v68\n"
        "v_max3_u32 v77, v69, v70, v71\n"
        "v_pk_add_f32 v[78:79], v[66:67], v[68:69]\n"
        "v_max3_u32 v76, v76, v72, v73\n"
        "v_pk_add_f32 v[80:81], v[70:71], v[72:73]\n"
        "v_max_u32 v76, v76, v77\n"
        "v_cmp_eq_u32 vcc, -1, v76\n"
        "v_pk_add_f32 v[66:67], v[78:79], v[80:81]\n"
        "s_cbranch_vccnz Lq_%=\n"
        "Lqd_%=:\n"
        "v_mov_b32 v68, v66\n"
        "v_mov_b32 v69, v67\n"
        "s_nop 0\n"
        "v_permlane16_swap_b32 v66, v68\n"
        "v_permlane16_swap_b32 v67, v69\n"
        "v_add_f32 v66, v66, v68\n"
        "v_add_f32 v67, v67, v69\n"
        "v_mov_b32 v68, v66\n"
        "v_mov_b32 v69, v67\n"
        "s_nop 0\n"
        "v_permlane32_swap_b32 v66, v68\n"
        "v_permlane32_swap_b32 v67, v69\n"
        "v_add_f32 v66, v66, v68\n"
        "v_add_f32 v67, v67, v69\n"
        "v_mul_f32 v64, 0xbb124925, v66\n"
        "v_mul_f32 v67, 0x3b124925, v67\n"
        "v_fma_f32 v67, -v64, v64, v67\n"
        "v_max_f32 v67, 0, v67\n"
        "v_add_f32 v67, 0x3727c5ac, v67\n"
        "v_rsq_f32 v65, v67\n"
        "s_nop 0\n"
        "v_mul_f32 v64, v64, v65\n"
        "v_pk_fma_f32 v[56:57], v[56:57], v[64:65], v[64:65] op_sel:[0,1,0] op_sel_hi:[1,1,0]\n"
        "v_pk_fma_f32 v[58:59], v[58:59], v[64:65], v[64:65] op_sel:[0,1,0] op_sel_hi:[1,1,0]\n"
        "v_pk_fma_f32 v[60:61], v[60:61], v[64:65], v[64:65] op_sel:[0,1,0] op_sel_hi:[1,1,0]\n"
        "v_pk_fma_f32 v[62:63], v[62:63], v[64:65], v[64:65] op_sel:[0,1,0] op_sel_hi:[1,1,0]\n"
        "v_cvt_pk_bf16_f32 v92, v56, v57\n"
        "v_cvt_pk_bf16_f32 v93, v58, v59\n"
        "v_cvt_pk_bf16_f32 v94, v60, v61\n"
        "v_cvt_pk_bf16_f32 v95, v62, v63\n"
        "v_min_u32 v92, -2, v92\n"
        "v_min_u32 v93, -2, v93\n"
        "v_min_u32 v94, -2, v94\n"
        "v_min_u32 v95, -2, v95\n"
        "buffer_store_dwordx4 v[92:95], v102, %[rs], s88 offen sc1\n"
        "buffer_store_dwordx4 v[116:119], v102, %[rs], s89 offen sc1\n"
        "Lnext_%=:\n"
        "s_cmp_eq_u32 s82, 1\n"
        "s_cbranch_scc1 Lmf_%=\n"
        "s_cmp_lt_u32 %[s], 128\n"
        "s_cbranch_scc1 Ltop_%=\n"
        "s_branch Lend_%=\n"
        "Lla_%=:\n"
        "s_waitcnt vmcnt(0) lgkmcnt(0)\n"
        "v_readfirstlane_b32 s91, v89\n"
        "s_add_u32 s94, %[s], %[nr]\n"
        "s_lshl_b32 s96, s96, 4\n"
        "s_add_u32 s97, s91, s86\n"
        "s_sub_u32 s96, s97, s96\n"
        "s_cmp_eq_u32 s91, -1\n"
        "s_cselect_b32 s96, 9999, s96\n"
        "s_cmp_ge_u32 s94, 128\n"
        "s_cselect_b32 s96, 9999, s96\n"
        "s_cmp_lt_i32 s96, 48\n"
        "s_cselect_b32 s82, 1, 0\n"
        "s_cbranch_scc0 Ltl_%=\n"
        "s_mul_i32 s84, %[nr], 57344\n"
        "s_add_u32 s84, s84, %[hb]\n"
        "s_mov_b32 s95, %[k0]\n"
        "v_mfma_f32_16x16x32_bf16 v[56:59], v[144:147], v[40:43], v[56:59]\n"
        "s_add_u32 s94, s95, s84\n"
        "s_add_u32 s95, s95, 2048\n"
        "buffer_load_dwordx4 v[0:3], v100, %[rs], s94 offen offset:0 sc1\n"
        "v_max3_u32 v88, v40, v41, v88\n"
        "v_mfma_f32_16x16x32_bf16 v[56:59], v[148:151], v[44:47], v[56:59]\n"
        "v_max3_u32 v88, v42, v43, v88\n"
        "v_max3_u32 v88, v44, v45, v88\n"
        "v_mfma_f32_16x16x32_bf16 v[56:59], v[200:203], v[48:51], v[56:59]\n"
        "v_max3_u32 v88, v46, v47, v88\n"
        "v_max3_u32 v88, v48, v49, v88\n"
        "v_mfma_f32_16x16x32_bf16 v[56:59], v[204:207], v[52:55], v[56:59]\n"
        "v_max3_u32 v88, v50, v51, v88\n"
        "v_max3_u32 v88, v52, v53, v88\n"
        "v_mfma_f32_16x16x32_bf16 v[60:63], v[152:155], v[40:43], v[60:63]\n"
        "v_max3_u32 v88, v54, v55, v88\n"
        "v_mfma_f32_16x16x32_bf16 v[60:63], v[156:159], v[44:47], v[60:63]\n"
        "buffer_load_dwordx4 v[4:7], v100, %[rs], s94 offen offset:1024 sc1\n"
        "v_max3_u32 v88, v84, v85, v88\n"
        "v_mfma_f32_16x16x32_bf16 v[60:63], v[208:211], v[48:51], v[60:63]\n"
        "v_max3_u32 v88, v86, v87, v88\n"
        "v_cmp_eq_u32 vcc, -1, v88\n"
        "v_mfma_f32_16x16x32_bf16 v[60:63], v[212:215], v[52:55], v[60:63]\n"
        "ds_write_b128 v98, v[56:59] offset:0\n"
        "v_lshlrev_b32 v92, 16, v84\n"
        "v_mfma_f32_16x16x32_bf16 v[64:67], v[160:163], v[40:43], v[64:67]\n"
        "v_lshlrev_b32 v94, 16, v85\n"
        "v_lshlrev_b32 v116, 16, v86\n"
        "v_mfma_f32_16x16x32_bf16 v[64:67], v[164:167], v[44:47], v[64:67]\n"
        "v_lshlrev_b32 v118, 16, v87\n"
        "v_and_b32 v93, 0xffff0000, v84\n"
        "v_mfma_f32_16x16x32_bf16 v[64:67], v[216:219], v[48:51], v[64:67]\n"
        "v_and_b32 v95, 0xffff0000, v85\n"
        "v_and_b32 v117, 0xffff0000, v86\n"
        "v_mfma_f32_16x16x32_bf16 v[64:67], v[220:223], v[52:55], v[64:67]\n"
        "ds_write_b128 v98, v[60:63] offset:1024\n"
        "v_and_b32 v119, 0xffff0000, v87\n"
        "v_mfma_f32_16x16x32_bf16 v[68:71], v[168:171], v[40:43], v[68:71]\n"
        "v_mfma_f32_16x16x32_bf16 v[68:71], v[172:175], v[44:47], v[68:71]\n"
        "s_cmp_ge_u32 s95, 14336\n"
        "s_cselect_b32 s95, 0, s95\n"
        "s_add_u32 s94, s95, s84\n"
        "s_add_u32 s95, s95, 2048\n"
        "buffer_load_dwordx4 v[8:11], v100, %[rs], s94 offen offset:0 sc1\n"
        "v_mfma_f32_16x16x32_bf16 v[68:71], v[224:227], v[48:51], v[68:71]\n"
        "buffer_load_dwordx4 v[12:15], v100, %[rs], s94 offen offset:1024 sc1\n"
        "v_mfma_f32_16x16x32_bf16 v[68:71], v[228:231], v[52:55], v[68:71]\n"
        "ds_write_b128 v98, v[64:67] offset:2048\n"
        "v_mfma_f32_16x16x32_bf16 v[72:75], v[176:179], v[40:43], v[72:75]\n"
        "s_cmp_ge_u32 s95, 14336\n"
        "s_cselect_b32 s95, 0, s95\n"
        "s_add_u32 s94, s95, s84\n"
        "s_add_u32 s95, s95, 2048\n"
        "buffer_load_dwordx4 v[16:19], v100, %[rs], s94 offen offset:0 sc1\n"
        "v_mfma_f32_16x16x32_bf16 v[72:75], v[180:183], v[44:47], v[72:75]\n"
        "buffer_load_dwordx4 v[20:23], v100, %[rs], s94 offen offset:1024 sc1\n"
        "v_mfma_f32_16x16x32_bf16 v[72:75], v[232:235], v[48:51], v[72:75]\n"
        "s_cmp_ge_u32 s95, 14336\n"
        "s_cselect_b32 s95, 0, s95\n"
        "s_add_u32 s94, s95, s84\n"
        "s_add_u32 s95, s95, 2048\n"
        "buffer_load_dwordx4 v[24:27], v100, %[rs], s94 offen offset:0 sc1\n"
        "v_mfma_f32_16x16x32_bf16 v[72:75], v[236:239], v[52:55], v[72:75]\n"
        "ds_write_b128 v98, v[68:71] offset:3072\n"
        "v_mfma_f32_16x16x32_bf16 v[76:79], v[184:187], v[40:43], v[76:79]\n"
        "buffer_load_dwordx4 v[28:31], v100, %[rs], s94 offen offset:1024 sc1\n"
        "v_mfma_f32_16x16x32_bf16 v[76:79], v[188:191], v[44:47], v[76:79]\n"
        "s_cmp_ge_u32 s95, 14336\n"
        "s_cselect_b32 s95, 0, s95\n"
        "s_add_u32 s94, s95, s84\n"
        "s_add_u32 s95, s95, 2048\n"
        "buffer_load_dwordx4 v[32:35], v100, %[rs], s94 offen offset:0 sc1\n"
        "v_mfma_f32_16x16x32_bf16 v[76:79], v[240:243], v[48:51], v[76:79]\n"
        "buffer_load_dwordx4 v[36:39], v100, %[rs], s94 offen offset:1024 sc1\n"
        "v_mfma_f32_16x16x32_bf16 v[76:79], v[244:247], v[52:55], v[76:79]\n"
        "ds_write_b128 v98, v[72:75] offset:4096\n"
        "v_mfma_f32_16x16x32_bf16 v[80:83], v[192:195], v[40:43], v[80:83]\n"
        "s_cmp_ge_u32 s95, 14336\n"
        "s_cselect_b32 s95, 0, s95\n"
        "v_mfma_f32_16x16x32_bf16 v[80:83], v[196:199], v[44:47], v[80:83]\n"
        "v_mfma_f32_16x16x32_bf16 v[80:83], v[248:251], v[48:51], v[80:83]\n"
        "v_mfma_f32_16x16x32_bf16 v[80:83], v[252:255], v[52:55], v[80:83]\n"
        "s_nop 0\n"
        "ds_write_b128 v98, v[76:79] offset:5120\n"
        "s_and_b64 vcc, vcc, %[live]\n"
        "s_cbranch_vccnz Lretry_%=\n"
        "s_add_u32 s86, s86, s85\n"
        "s_min_u32 s86, s86, 1792\n"
        "s_nop 1\n"
        "ds_write_b128 v98, v[80:83] offset:6144\n"
        "s_mov_b32 s83, %[s]\n"
        "s_add_u32 s94, s95, s84\n"
        "s_add_u32 s95, s95, 2048\n"
        "buffer_load_dwordx4 v[40:43], v100, %[rs], s94 offen offset:0 sc1\n"
        "buffer_load_dwordx4 v[44:47], v100, %[rs], s94 offen offset:1024 sc1\n"
        "s_cmp_ge_u32 s95, 14336\n"
        "s_cselect_b32 s95, 0, s95\n"
        "s_add_u32 s94, s95, s84\n"
        "s_add_u32 s95, s95, 2048\n"
        "buffer_load_dwordx4 v[48:51], v100, %[rs], s94 offen offset:0 sc1\n"
        "buffer_load_dwordx4 v[52:55], v100, %[rs], s94 offen offset:1024 sc1\n"
        "s_cmp_ge_u32 s95, 14336\n"
        "s_cselect_b32 s95, 0, s95\n"
        "s_mul_i32 s92, %[nr], 57344\n"
        "s_add_u32 %[hb], %[hb], s92\n"
        "s_add_u32 %[s], %[s], %[nr]\n"
        "s_lshr_b32 s92, %[s], 1\n"
        "s_mul_i32 s92, s92, 28672\n"
        "s_and_b32 s94, %[s], 1\n"
        "s_lshl_b32 s94, s94, 8\n"
        "s_add_u32 s87, s92, s94\n"
        "s_add_u32 s90, s87, %[x1]\n"
        "buffer_load_dwordx4 v[84:87], v97, %[rs], s90 offen sc1\n"
        "v_mov_b32 v88, 0\n"
        "s_mov_b32 s85, 0\n"
        "s_waitcnt lgkmcnt(0)\n"
        "s_barrier\n"
        "s_branch Lrd_%=\n"
        "Lout_%=:\n"
        "s_mul_i32 s92, s83, 14336\n"
        "buffer_store_dwordx4 v[116:119], v101, %[ro], s92 offen\n"
        "buffer_load_dword v91, v110, %[rs], 0 offen\n"
        "s_barrier\n"
        "s_cmp_eq_u32 s82, 1\n"
        "s_cbranch_scc1 Lnext_%=\n"
        "s_mul_i32 s92, %[nr], 57344\n"
        "s_add_u32 %[hb], %[hb], s92\n"
        "s_add_u32 %[s], %[s], %[nr]\n"
        "s_lshr_b32 s92, %[s], 1\n"
        "s_mul_i32 s92, s92, 28672\n"
        "s_and_b32 s94, %[s], 1\n"
        "s_lshl_b32 s94, s94, 8\n"
        "s_add_u32 s87, s92, s94\n"
        "s_add_u32 s90, s87, %[x1]\n"
        "s_branch Lnext_%=\n"
        "Lretry_%=:\n"
        "s_mov_b32 s85, 96\n"
        "s_nop 7\n"
        "s_sub_u32 %[slowc], %[slowc], 1\n"
        "s_cbranch_scc1 Lvt_%=\n"
        "s_mov_b32 s93, %[capv]\n"
        "Lv_%=:\n"
        "buffer_load_dword v88, v96, %[rs], %[hb] offen sc1\n"
        "s_sub_u32 s93, s93, 1\n"
        "s_waitcnt vmcnt(0)\n"
        "v_cmp_eq_u32 vcc, -1, v88\n"
        "s_cbranch_scc1 Lvt_%=\n"
        "s_cbranch_vccz Lvd_%=\n"
        "s_sleep 1\n"
        "s_branch Lv_%=\n"
        "Lvt_%=:\n"
        "s_mov_b32 %[capv], 0\n"
        "v_mov_b32 v88, 1\n"
        "s_mov_b32 s94, 4227328\n"
        "buffer_store_dword v88, off, %[rs], s94 sc1\n"
        "s_mov_b64 %[live], 0\n"
        "Lvd_%=:\n"
        "s_branch Lwd_%=\n"
        "Lqt_%=:\n"
        "s_mov_b32 %[capv], 0\n"
        "v_mov_b32 v76, 1\n"
        "s_mov_b32 s94, 4227328\n"
        "buffer_store_dword v76, off, %[rs], s94 sc1\n"
        "s_mov_b64 %[live], 0\n"
        "s_waitcnt vmcnt(0)\n"
        "s_branch Lqd_%=\n"
        "Lend_%=:\n"
        "s_waitcnt vmcnt(0) lgkmcnt(0)\n"
        "s_endpgm\n"
        : [s] "+s"(s), [hb] "+s"(hb), [capv] "+s"(capv), [slowc] "+s"(slowc), [live] "=&s"(live)
        : "{v[96:111]}"(A1), [rs] "s"(rs), [ro] "s"(ro), [k0] "s"(k0), [x1] "s"(x1), [xn] "s"(xn), [xr] "s"(xr), [st] "s"(st), [bb] "s"(bb), [nr] "s"(nr), [wv] "s"(wv), [lay] "s"(lay), "{a[0:7]}"(W[0]), "{a[8:15]}"(W[1]), "{a[16:23]}"(W[2]), "{a[24:31]}"(W[3]), "{a[32:39]}"(W[4]), "{a[40:47]}"(W[5]), "{a[48:55]}"(W[6]), "{a[56:63]}"(W[7]), "{a[64:71]}"(W[8]), "{a[72:79]}"(W[9]), "{a[80:87]}"(W[10]), "{a[88:95]}"(W[11]), "{a[96:103]}"(W[12]), "{a[104:111]}"(W[13]), "{a[112:119]}"(W[14]), "{a[120:127]}"(W[15]), "{a[128:135]}"(W[16]), "{a[136:143]}"(W[17]), "{a[144:151]}"(W[18]), "{a[152:159]}"(W[19]), "{a[160:167]}"(W[20]), "{a[168:175]}"(W[21]), "{a[176:183]}"(W[22]), "{a[184:191]}"(W[23]), "{a[192:199]}"(W[24]), "{a[200:207]}"(W[25]), "{a[208:215]}"(W[26]), "{a[216:223]}"(W[27]), "{a[224:231]}"(W[28]), "{a[232:239]}"(W[29]), "{a[240:247]}"(W[30]), "{a[248:255]}"(W[31]), "{v[120:127]}"(W[32]), "{v[128:135]}"(W[33]), "{v[136:143]}"(W[34]), "{v[144:151]}"(W[35]), "{v[152:159]}"(W[36]), "{v[160:167]}"(W[37]), "{v[168:175]}"(W[38]), "{v[176:183]}"(W[39]), "{v[184:191]}"(W[40]), "{v[192:199]}"(W[41]), "{v[200:207]}"(W[42]), "{v[208:215]}"(W[43]), "{v[216:223]}"(W[44]), "{v[224:231]}"(W[45]), "{v[232:239]}"(W[46]), "{v[240:247]}"(W[47]), "{v[248:255]}"(W[48])
        : "vcc", "scc", "memory", "s82", "s83", "s84", "s85", "s86", "s87", "s88", "s89", "s90", "s91", "s92", "s93", "s94", "s95", "s96", "s97", "s98", "s99");
    __builtin_amdgcn_endpgm();
}
DI void role_G2(Ctx& C, const Params& p, int rep, int cs) {
    const int lane = C.lane, w = C.w, l15 = lane & 15, g = lane >> 4;
    char* sm = C.sm;
    u32x8 W[49];
    {
        const unsigned sb = w * 28672u;
        u32x4 t[14];
        auto fetch = [&](int rd) {
            const int kp = (rd + 2 * cs) % 7;
#pragma unroll
            for (int it = 0; it < 14; ++it) {
                const int i = it * 64 + lane, k = i / 14, ch = i % 14;
                t[it] = *(const u32x4*)(p.wd + (size_t)(448 * w + 64 * kp + k) * D + cs * 112 + ch * 8);
            }
        };
        fetch(0);
        sfor<0, 7>([&](auto rd) {
            const unsigned sr = sb + (rd & 1) * 14336;
#pragma unroll
            for (int it = 0; it < 14; ++it) sts16(sm, sr + (it * 64 + lane) * 16, t[it]);
            SB;
            if (rd < 6) fetch(rd + 1);
            SB;
            sfor<0, 7>([&](auto tt) {
                const unsigned o = sr + (8 * g + (l15 >> 2)) * 224 + tt * 32 + (l15 & 3) * 8;
                const u32x2 a = ldtr(sm, o), b = ldtr(sm, o + 4 * 224), c2 = ldtr(sm, o + 32 * 224), d2 = ldtr(sm, o + 36 * 224);
                W[rd * 7 + tt] = (u32x8){a[0], a[1], b[0], b[1], c2[0], c2[1], d2[0], d2[1]};
                WR<rd * 7 + tt>::pin(W[rd * 7 + tt]);
            });
            SB;
        });
    }
    bar();
    const int tok = threadIdx.x & 15;
    const bool act = threadIdx.x < 224;
    const int ch = act ? threadIdx.x >> 4 : 0;
    u32x16 A1;
#pragma unroll
    for (int i = 0; i < 16; ++i) A1[i] = 0;
    A1[0] = (lane % 14) * 1024 + cs * 256;
    A1[1] = (cs * 14 + ch) * 512 + tok * 16;
    A1[2] = w * 7168 + lane * 16;
    A1[3] = ch * 512 + tok * 16;
    A1[4] = lane * 16;
    A1[5] = act ? tok * 896 + cs * 224 + ch * 16 : OOB;
    A1[6] = act ? A1[1] : OOB;
    A1[7] = asu(act ? 1.0f : 0.0f);
    A1[8] = (g & 1) ? OOB : (cs * 4 + w) * 128 + tok * 8 + (lane >> 5) * 4;
    A1[9] = lane * 8;
    A1[10] = lane < 16 ? cs * 128 + lane * 8 : OOB;
    A1[11] = g == cs ? OOB : lane * 8;
    A1[12] = tok * 8;
    A1[13] = C.layer ? OOB : A1[6];
    A1[14] = OOB;
    const i32x4 ro = {(int)(size_t)p.out, (int)((size_t)p.out >> 32) & 0xFFFF, 2048 * D * 2, 0x00020000};
    g2_asm(W, A1, C.rw, ro, rep, O_HID + C.layer * HIDSZ + (rep * 56 + 14 * w) * 1024u, ((2 * cs) % 7) * 2048u, O_X1 + C.layer * XSZ, O_XN + XSZ, O_XR + XSZ, C.hop1 + J_STAT * 16384u, C.hop0 + 7168u + 512u,
           C.layer ? 2u : 4u, w, C.layer, O_DBG + C.id * 8192u);
}

__global__ void __launch_bounds__(256, 1) main_kernel(Params p) {
    extern __shared__ __attribute__((aligned(16))) char sm[];
    Ctx C;
    C.rs = __builtin_amdgcn_make_buffer_rsrc(p.ws, 0, WS_USED, 0x00020000);
    C.rw = (i32x4){(int)(size_t)p.ws, (int)((size_t)p.ws >> 32) & 0xFFFF, (int)WS_USED, 0x00020000};
    C.sm = sm; C.ws = p.ws; C.lane = threadIdx.x & 63; C.w = __builtin_amdgcn_readfirstlane(threadIdx.x >> 6);
    const int b = blockIdx.x;
    STAMP(1022);
    const int rot = *(const int*)(p.ws + O_CTL) & 7;
    unsigned pr = *(const unsigned*)(p.ws + O_CTL + 512 + C.lane * 4);
    const int grp = (b + rot) & 7;
    int idx = b >> 3, sub = grp & 3;
    bool mixed = false;
    if (sub == 0 || sub == 3) {
        const int hf = sub ? 1 : 0;
        mixed = true;
        if (idx < 16) { sub = 0; idx += 16 * hf; }
        else if (idx < 30) { sub = 3; idx = idx - 16 + 14 * hf; }
        else { sub = 3; idx = idx - 2 + 2 * hf; }
    }
    C.layer = grp >> 2; C.id = (C.layer * 4 + sub) * 32 + idx;
    int g2rep = sub - 1;
    if (C.layer && sub == 3 && idx >= 28) { C.layer = 0; g2rep = 3; }
#define UMIN_DPP(c) { const unsigned o = (unsigned)__builtin_amdgcn_update_dpp(0, (int)pr, c, 0xF, 0xF, true); pr = o < pr ? o : pr; }
    UMIN_DPP(0x128) UMIN_DPP(0x124) UMIN_DPP(0x122) UMIN_DPP(0x121)
    const unsigned q0 = __builtin_amdgcn_readlane(pr, 0), q1 = __builtin_amdgcn_readlane(pr, 16), q2 = __builtin_amdgcn_readlane(pr, 32), q3 = __builtin_amdgcn_readlane(pr, 48);
    const unsigned par = (unsigned)__builtin_amdgcn_readfirstlane(((q2 < q3 ? q2 : q3) < (q0 < q1 ? q0 : q1) ? 1 : 0) ^ C.layer);
    C.hop0 = O_HOP0 + (par << 13);
    C.hop1 = O_HOP1 + ((par ^ 1u) << 13);
    const bool attn = sub < 3 && (sub == 0 || idx < 28);
    if (C.layer || !attn) { for (int i = 0; i < (grp >= 4 ? (attn ? STAGGER : STAG1) : (idx < 28 ? STAG0 : mixed ? STAG3 : STAG2)); ++i) __builtin_amdgcn_s_sleep(32); }
    if (FFPF && attn && sub && C.layer) {
        unsigned fw[6];
#pragma unroll
        for (int i = 0; i < 6; ++i) {
            const int o = (((sub - 1) * 28 + idx) * 448 + (i & 1) * 224 + (threadIdx.x < 224 ? threadIdx.x : 223)) * 32;
            fw[i] = *(const unsigned*)((i < 2 ? p.wd : i < 4 ? p.wg : p.wu) + o);
        }
        asm volatile("" :: "v"(fw[0]), "v"(fw[1]), "v"(fw[2]), "v"(fw[3]), "v"(fw[4]), "v"(fw[5]) : "memory");
    }
    if (attn) {
        if (sub == 0) role_S(C, idx & 15, idx >> 4);
        else role_V(C, idx, sub - 1);
    } else if (idx < 28) role_G1(C, p, idx / 14, idx % 14);
    else role_G2(C, p, g2rep, idx - 28);
}

extern "C" void kernel_launch(void* const* d_in, const int* in_sizes, int n_in, void* d_out, int out_size, void* d_ws, size_t ws_size,
                              hipStream_t stream) {
    Params p;
    p.x = (const unsigned short*)d_in[0]; p.mem0 = (const unsigned short*)d_in[1];
    p.wq = (const unsigned short*)d_in[2]; p.wk = (const unsigned short*)d_in[3]; p.wv = (const unsigned short*)d_in[4]; p.wo = (const unsigned short*)d_in[5];
    p.wg = (const unsigned short*)d_in[8]; p.wu = (const unsigned short*)d_in[9]; p.wd = (const unsigned short*)d_in[10];
    p.out = (unsigned short*)d_out;
    p.ws = (char*)(((size_t)d_ws + (2u << 20) - 1) & ~(size_t)((2u << 20) - 1));
    constexpr int kLds = 132 * 1024;
    (void)hipFuncSetAttribute((const void*)main_kernel, hipFuncAttributeMaxDynamicSharedMemorySize, kLds);
    (void)hipFuncSetAttribute((const void*)prologue_kernel, hipFuncAttributeMaxDynamicSharedMemorySize, 114688);
    hipLaunchKernelGGL(prologue_kernel, dim3(256), dim3(512), 114688, stream, p);
    hipLaunchKernelGGL(main_kernel, dim3(256), dim3(256), kLds, stream, p);
}
```
